```python
import torch

def _softmax(x, axis=-1):
    return torch.softmax(x, dim=axis)
def _gelu(x):
    return torch.nn.functional.gelu(x, approximate="tanh")
def _silu(x):
    return torch.nn.functional.silu(x)
def _sigmoid(x):
    return torch.sigmoid(x)
def _layernorm(x, axis=-1, eps=1e-5):
    m = x.mean(dim=axis, keepdim=True)
    v = x.var(dim=axis, keepdim=True, unbiased=False)
    return (x - m) / torch.sqrt(v + eps)
def _scatter_add(tgt, idx, src, axis=0):
    return tgt.clone().index_add_(axis, idx, src)
def _segment_sum(src, seg, n):
    out = torch.zeros((n,) + src.shape[1:], dtype=src.dtype)
    return out.index_add_(0, seg, src)
def _pad(x, pw):
    flat = tuple(v for pair in reversed(pw) for v in pair)
    return torch.nn.functional.pad(x, flat)

kernel_name = 'concat_heads__gqa_block_cached__gqa_block_cached__near_ridge'

def setup_inputs():

    _w = (lambda a: (lambda t: t.to(torch.bfloat16) if t.is_floating_point() else t)(torch.as_tensor(a)))
    def _A_setup():
        B, Sq, D = 16, 144, 640
        Hq, Hkv, Sk, Dh = 32, 32, 512, 128
        assert Hq % Hkv == 0, (Hq, Hkv)
        return {
            "x": _w((lambda *s: torch.randn(*s, dtype=torch.bfloat16) * 0.8)(B, Sq, D)),
            "k_cache": _w((lambda *s: torch.randn(*s, dtype=torch.bfloat16) * 0.8)(B, Hkv, Sk, Dh)),
            "v_cache": _w((lambda *s: torch.randn(*s, dtype=torch.bfloat16) * 0.05)(B, Hkv, Sk, Dh)),
            "wq": _w((lambda *s: torch.randn(*s, dtype=torch.bfloat16) * 0.8)(D, Hq * Dh) * (D ** -0.5)),
            "wo": _w((lambda *s: torch.randn(*s, dtype=torch.bfloat16) * 0.05)(Hq * Dh, D) * ((Hq * Dh) ** -0.5)),
        }
    _A_in = _A_setup()

    def _B_setup():
        B, Sq, D = 16, 144, 640
        Hq, Hkv, Sk, Dh = 32, 32, 512, 128
        assert Hq % Hkv == 0, (Hq, Hkv)
        return {
            "x": _w((lambda *s: torch.randn(*s, dtype=torch.bfloat16) * 0.8)(B, Sq, D)),
            "k_cache": _w((lambda *s: torch.randn(*s, dtype=torch.bfloat16) * 0.8)(B, Hkv, Sk, Dh)),
            "v_cache": _w((lambda *s: torch.randn(*s, dtype=torch.bfloat16) * 0.05)(B, Hkv, Sk, Dh)),
            "wq": _w((lambda *s: torch.randn(*s, dtype=torch.bfloat16) * 0.8)(D, Hq * Dh) * (D ** -0.5)),
            "wo": _w((lambda *s: torch.randn(*s, dtype=torch.bfloat16) * 0.05)(Hq * Dh, D) * ((Hq * Dh) ** -0.5)),
        }
    _B_in = _B_setup()

    return {
        "A_x": _A_in["x"],
        "A_k_cache": _A_in["k_cache"],
        "A_v_cache": _A_in["v_cache"],
        "A_wq": _A_in["wq"],
        "A_wo": _A_in["wo"],
        "B_x": _B_in["x"],
        "B_k_cache": _B_in["k_cache"],
        "B_v_cache": _B_in["v_cache"],
        "B_wq": _B_in["wq"],
        "B_wo": _B_in["wo"],
    }

def reference(A_x, A_k_cache, A_v_cache, A_wq, A_wo, B_x, B_k_cache, B_v_cache, B_wq, B_wo):
    def _A_ref(x, k_cache, v_cache, wq, wo):
        B, Sq, D = x.shape
        Hq, Dh = 32, 128
        Hkv = k_cache.shape[1]
        rep = Hq // Hkv
        q = (lambda x, *d: x.permute(*d))((x @ wq).reshape(B, Sq, Hq, Dh), 0, 2, 1, 3)
        k = (lambda x, n, axis: torch.repeat_interleave(x, n, dim=axis))(k_cache, rep, axis=1)
        v = (lambda x, n, axis: torch.repeat_interleave(x, n, dim=axis))(v_cache, rep, axis=1)
        scores = torch.einsum("bhqd,bhkd->bhqk", q, k) * (Dh ** -0.5)
        p = _softmax(scores, axis=-1)
        o = torch.einsum("bhqk,bhkd->bhqd", p, v)
        return (lambda x, *d: x.permute(*d))(o, 0, 2, 1, 3).reshape(B, Sq, Hq * Dh) @ wo

    def _B_ref(x, k_cache, v_cache, wq, wo):
        B, Sq, D = x.shape
        Hq, Dh = 32, 128
        Hkv = k_cache.shape[1]
        rep = Hq // Hkv
        q = (lambda x, *d: x.permute(*d))((x @ wq).reshape(B, Sq, Hq, Dh), 0, 2, 1, 3)
        k = (lambda x, n, axis: torch.repeat_interleave(x, n, dim=axis))(k_cache, rep, axis=1)
        v = (lambda x, n, axis: torch.repeat_interleave(x, n, dim=axis))(v_cache, rep, axis=1)
        scores = torch.einsum("bhqd,bhkd->bhqk", q, k) * (Dh ** -0.5)
        p = _softmax(scores, axis=-1)
        o = torch.einsum("bhqk,bhkd->bhqd", p, v)
        return (lambda x, *d: x.permute(*d))(o, 0, 2, 1, 3).reshape(B, Sq, Hq * Dh) @ wo

    a = _A_ref(A_x, A_k_cache, A_v_cache, A_wq, A_wo)
    b = _B_ref(B_x, B_k_cache, B_v_cache, B_wq, B_wo)
    return torch.concatenate([a, b], axis=-1)
```

```cpp
#include <hip/hip_runtime.h>
#include <hip/hip_bf16.h>

typedef short bf16x8 __attribute__((ext_vector_type(8)));
typedef short bf16x4 __attribute__((ext_vector_type(4)));
typedef float f32x4 __attribute__((ext_vector_type(4)));
typedef float f32x2 __attribute__((ext_vector_type(2)));
typedef __bf16 bf2_t __attribute__((ext_vector_type(2)));
typedef unsigned u32x4 __attribute__((ext_vector_type(4)));
typedef unsigned u32x2 __attribute__((ext_vector_type(2)));
typedef __attribute__((address_space(3))) char* lds_ptr;
typedef __attribute__((address_space(3))) const char* lds_cptr;
#define DI __device__ __forceinline__
#define LDSP(T, p) ((__attribute__((address_space(3))) T*)(p))

constexpr int NB = 16, SQ = 144, DM = 640, NH = 32, SK = 512, DH = 128, HD = NH * DH;
constexpr float QSCALE = 0.12751743082459868f;
constexpr float THR = 10.0f;

#define MFMA32(a, b, c) __builtin_amdgcn_mfma_f32_16x16x32_bf16((a), (b), (c), 0, 0, 0)
#define MFMA16(a, b, c) __builtin_amdgcn_mfma_f32_16x16x16bf16_1k((a), (b), (c), 0, 0, 0)

#define MFMA32T(acc, a, b) asm volatile("v_mfma_f32_16x16x32_bf16 %0, %1, %2, %0" : "+v"(acc) : "v"(a), "v"(b))
#define MFMA32Z(acc, a, b) asm volatile("v_mfma_f32_16x16x32_bf16 %0, %1, %2, 0" : "=&v"(acc) : "v"(a), "v"(b))
#define MFMA16T(acc, a, b) asm volatile("v_mfma_f32_16x16x16_bf16 %0, %1, %2, %0" : "+v"(acc) : "v"(a), "v"(b))
#define MFMA16Z(acc, a, b) asm volatile("v_mfma_f32_16x16x16_bf16 %0, %1, %2, 0" : "=&v"(acc) : "v"(a), "v"(b))
#define MFMA_PAD() asm volatile("s_nop 7\n\ts_nop 3" ::: "memory")
DI unsigned cvtpk(float lo, float hi) { f32x2 v = {lo, hi}; bf2_t b = __builtin_convertvector(v, bf2_t); return __builtin_bit_cast(unsigned, b); }
DI bf16x8 ld128(lds_cptr p) { return *LDSP(const bf16x8, p); }
DI bf16x4 ldtr(lds_cptr p) { return __builtin_amdgcn_ds_read_tr16_b64_v4i16(LDSP(bf16x4, (lds_ptr)p)); }
DI bf16x8 cat(bf16x4 lo, bf16x4 hi) { return __builtin_shufflevector(lo, hi, 0, 1, 2, 3, 4, 5, 6, 7); }
DI int fsw(int u) { return (((u ^ (u >> 1)) & 1) << 1) | ((u >> 1) & 1); }
DI unsigned rfl(unsigned x) { return __builtin_amdgcn_readfirstlane(x); }

#define DMA4(POL, m0v, v0, v1, v2, v3, sb)                                                                                   \
    asm volatile("s_mov_b32 m0, %0\n\ts_nop 0\n\t"                                                                            \
                 "global_load_lds_dwordx4 %1, %5" POL "\n\t"                                                                  \
                 "global_load_lds_dwordx4 %2, %5 offset:1024" POL "\n\t"                                                      \
                 "global_load_lds_dwordx4 %3, %5 offset:2048" POL "\n\t"                                                      \
                 "global_load_lds_dwordx4 %4, %5 offset:3072" POL                                                             \
                 :: "s"(m0v), "v"(v0), "v"(v1), "v"(v2), "v"(v3), "s"(sb) : "memory")
#define DMA1(POL, m0v, v0, sb)                                                                                               \
    asm volatile("s_mov_b32 m0, %0\n\ts_nop 0\n\tglobal_load_lds_dwordx4 %1, %2" POL :: "s"(m0v), "v"(v0), "s"(sb) : "memory")
#define DIAG_LDS 75264
#define WAIT_BAR(N) asm volatile("s_waitcnt vmcnt(" #N ") lgkmcnt(0)\n\ts_barrier" ::: "memory")

#define STAMP(i)
constexpr int XW_W = 18432;
constexpr int STG = XW_W + 16 * 1040;
constexpr int KS0 = 0, VS0 = 16384;
constexpr int QX_OFF = 2 * STG;
constexpr int ST_OFF = QX_OFF + 4096;
constexpr int K1_LDS = ST_OFF + 1024;
constexpr int BIAS = 3072;
#define PRIOV 1
#define PRIO(x) do { if (PRIOV) __builtin_amdgcn_s_setprio(x); } while (0)
#define SB() __builtin_amdgcn_sched_barrier(0)
#define PIN(x) asm volatile("" : "+v"(x))
#define DMAP(POL, m0b, M0OFF, vo, sb, IMM) \
    asm volatile("s_add_u32 m0, %0, %4\n\ts_nop 0\n\tglobal_load_lds_dwordx4 %1, %2 offset:%3" POL :: "s"(m0b), "v"(vo), "s"(sb), "i"(IMM), "i"(M0OFF) : "memory", "scc")

struct K1Args { const char* x[2]; const char* k[2]; const char* v[2]; const char* wq[2]; char* ows; };

struct AttnCtx {
    lds_cptr lds; unsigned ldsk, ldsv;
    unsigned ka[4], va[4], kvo[2], vvo;
    const char* kp; const char* vp;
};
template <int SO, int J> DI void dma_k(const AttnCtx& c, const int t) { DMAP(" nt", c.ldsk, SO + (J & 1) * 896, c.kvo[J >> 1], c.kp + t * 16384, (J >> 1) * 2048 + (J & 1) * 128); }
template <int SO, int J> DI void dma_v(const AttnCtx& c, const int t) { DMAP(" nt", c.ldsv, SO + J * 32, c.vvo, c.vp + t * 16384, J * 1024); }
#define DMA_K(c, SO, t, j) do { switch (j) { case 0: dma_k<SO, 0>(c, t); break; case 1: dma_k<SO, 1>(c, t); break; case 2: dma_k<SO, 2>(c, t); break; default: dma_k<SO, 3>(c, t); break; } } while (0)
#define DMA_V(c, SO, t, j) do { switch (j) { case 0: dma_v<SO, 0>(c, t); break; case 1: dma_v<SO, 1>(c, t); break; case 2: dma_v<SO, 2>(c, t); break; default: dma_v<SO, 3>(c, t); break; } } while (0)
DI bf16x8 kfrag(const AttnCtx& c, int so, int n) { return ld128(c.lds + c.ka[n >> 2] + (so + KS0 + (n & 3) * 512)); }
DI bf16x4 vtr(const AttnCtx& c, int so, int i, int dt) { return ldtr(c.lds + c.va[i] + (so + VS0 + (dt >> 1) * 256 + (dt & 1) * 32)); }
DI float xmax(float m) {
    auto a = __builtin_amdgcn_permlane32_swap(__float_as_uint(m), __float_as_uint(m), false, false);
    m = fmaxf(__uint_as_float(a[0]), __uint_as_float(a[1]));
    auto b = __builtin_amdgcn_permlane16_swap(__float_as_uint(m), __float_as_uint(m), false, false);
    return fmaxf(__uint_as_float(b[0]), __uint_as_float(b[1]));
}
DI float xsum(float m) {
    auto a = __builtin_amdgcn_permlane32_swap(__float_as_uint(m), __float_as_uint(m), false, false);
    m = __uint_as_float(a[0]) + __uint_as_float(a[1]);
    auto b = __builtin_amdgcn_permlane16_swap(__float_as_uint(m), __float_as_uint(m), false, false);
    return __uint_as_float(b[0]) + __uint_as_float(b[1]);
}
#define EXP4(v) do { v[0] = __builtin_amdgcn_exp2f(v[0]); v[1] = __builtin_amdgcn_exp2f(v[1]); v[2] = __builtin_amdgcn_exp2f(v[2]); v[3] = __builtin_amdgcn_exp2f(v[3]); PIN(v); } while (0)
#define SUMPK(v, ls, d) do { ls += v[0]; ls += v[1]; ls += v[2]; ls += v[3]; d = u32x2{cvtpk(v[0], v[1]), cvtpk(v[2], v[3])}; PIN(ls); PIN(d); } while (0)

struct QCtx {
    lds_cptr lds; unsigned ldsx, ldsw, ldsx4; int w;
    unsigned xo[5], wv[4], xa_own, xa_x, wa, wax;
    const char* xp; const char* wp;
};
template <int J, int SO> DI void q_dma(const QCtx& q, const int c) {
    if (J < 4) DMAP("", q.ldsw, SO + (J & 3) * 1040, q.wv[J & 3], q.wp + (size_t)c * (64 * HD * 2), 0);
    else if (J < 8) DMAP("", q.ldsx, SO + (J & 3) * 1024, q.xo[J & 3], q.xp + c * 128, 0);
    else
        asm volatile("s_add_u32 m0, %0, %3\n\ts_mov_b32 exec_hi, 0\n\tglobal_load_lds_dwordx4 %1, %2\n\ts_mov_b32 exec_hi, -1"
                     :: "s"(q.ldsx4), "v"(q.xo[4]), "s"(q.xp + c * 128), "i"(SO) : "memory", "scc");
}
template <int SO> DI void q_dma_rt(const QCtx& q, const int j, const int c) {
    switch (j) {
        case 0: q_dma<0, SO>(q, c); break; case 1: q_dma<1, SO>(q, c); break; case 2: q_dma<2, SO>(q, c); break;
        case 3: q_dma<3, SO>(q, c); break; case 4: q_dma<4, SO>(q, c); break; case 5: q_dma<5, SO>(q, c); break;
        case 6: q_dma<6, SO>(q, c); break; case 7: q_dma<7, SO>(q, c); break; default: q_dma<8, SO>(q, c); break;
    }
}
DI int opaque_lane() { int l; asm volatile("v_mbcnt_lo_u32_b32 %0, -1, 0\n\tv_mbcnt_hi_u32_b32 %0, -1, %0" : "=v"(l)); return l; }
DI void q_dma_setup(QCtx& q) {
    const int l = opaque_lane(), g = l >> 4, q4 = (l & 15) >> 2, p = l & 3, row8 = (l >> 2) & 7, hf = l >> 5, w = q.w;
#pragma unroll
    for (int j = 0; j < 4; ++j) {
        q.xo[j] = (8 * (4 * w + j) + row8) * (DM * 2) + hf * 64 + ((p ^ fsw(((j & 1) << 1) | (row8 >> 2))) * 16);
        q.wv[j] = (4 * (4 * w + j) + q4) * (HD * 2) + g * 64 + p * 16;
    }
    q.xo[4] = (8 * (16 + (w >> 1)) + row8) * (DM * 2) + (w & 1) * 64 + ((p ^ fsw(((w >> 1) << 1) | (row8 >> 2))) * 16);
}
template <int SLOT, bool FIRST, bool LAST>
DI void fast_tile(const int t, const AttnCtx& c, const bf16x8 (&qf)[3][4], f32x4 (&o)[3][8], f32x4 (&negm)[3], float (&lsum)[3], bf16x8 (&kf)[4],
                  const QCtx& nq, const bool more) {
    constexpr int SO = SLOT * STG, SN = (SLOT ^ 1) * STG;
    const f32x4 z4 = {0.f, 0.f, 0.f, 0.f};
    f32x4 s[2][4], sx; u32x2 ph[2][4], px; bf16x4 vf[4][2];
#pragma unroll
    for (int n = 0; n < 16; ++n) {
        const int i = n >> 2, ks = n & 3;
        s[0][i] = MFMA32(kf[ks], qf[0][ks], ks ? s[0][i] : ((FIRST && i == 0) ? z4 : negm[0]));
        s[1][i] = MFMA32(kf[ks], qf[1][ks], ks ? s[1][i] : ((FIRST && i == 0) ? z4 : negm[1]));
        if (i == 0) sx = MFMA32(kf[ks], qf[2][ks], ks ? sx : (FIRST ? z4 : negm[2]));
        if (n < 12) kf[ks] = kfrag(c, SO, n + 4);
        if (n >= 14) { vf[n - 14][0] = vtr(c, SO, 0, n - 14); vf[n - 14][1] = vtr(c, SO, 1, n - 14); }
        if (i > 0) {
            if (ks == 1) { EXP4(s[0][i - 1]); }
            if (ks == 2) { EXP4(s[1][i - 1]); SUMPK(s[0][i - 1], lsum[0], ph[0][i - 1]); }
            if (ks == 3) { SUMPK(s[1][i - 1], lsum[1], ph[1][i - 1]); }
            if (i == 2 && ks == 0) { EXP4(sx); }
            if (i == 3 && ks == 0) { SUMPK(sx, lsum[2], px); }
        }
        if (!FIRST && n < 4) { if (t < 7) DMA_V(c, SN, t + 1, n); }
        if (LAST && n >= 2 && n < 11) { if (more) q_dma_rt<SN>(nq, n == 2 ? 8 : n - 3, 0); }
        SB();
        if (FIRST && n == 3) {
            const float m0 = xmax(fmaxf(fmaxf(s[0][0][0], s[0][0][1]), fmaxf(s[0][0][2], s[0][0][3])));
            const float m1 = xmax(fmaxf(fmaxf(s[1][0][0], s[1][0][1]), fmaxf(s[1][0][2], s[1][0][3])));
            const float m2 = xmax(fmaxf(fmaxf(sx[0], sx[1]), fmaxf(sx[2], sx[3])));
            negm[0] = f32x4{-m0, -m0, -m0, -m0}; negm[1] = f32x4{-m1, -m1, -m1, -m1}; negm[2] = f32x4{-m2, -m2, -m2, -m2};
            s[0][0] -= m0; s[1][0] -= m1; sx -= m2;
            SB();
        }
        if (n == 13) { if (LAST) { if (more) WAIT_BAR(9); else WAIT_BAR(0); } else if (FIRST) WAIT_BAR(10); else WAIT_BAR(8); SB(); }
    }
#pragma unroll
    for (int m = 0; m < 16; ++m) {
        const int pr = m >> 3, dt = m & 7;
        const bf16x8 a = cat(vf[m & 3][0], vf[m & 3][1]);
        const u32x4 p0 = __builtin_shufflevector(ph[0][2 * pr], ph[0][2 * pr + 1], 0, 1, 2, 3), p1 = __builtin_shufflevector(ph[1][2 * pr], ph[1][2 * pr + 1], 0, 1, 2, 3);
        if (FIRST && pr == 0) { MFMA32Z(o[0][dt], a, p0); MFMA32Z(o[1][dt], a, p1); MFMA16Z(o[2][dt], vf[m & 3][0], px); }
        else { MFMA32T(o[0][dt], a, p0); MFMA32T(o[1][dt], a, p1); if (pr == 0) MFMA16T(o[2][dt], vf[m & 3][0], px); }
        if (m == 0) {
#pragma unroll
            for (int e = 2; e < 4; ++e) { vf[e][0] = vtr(c, SO, 0, e); vf[e][1] = vtr(c, SO, 1, e); }
        }
        if (m < 12) { const int mm = m + 4; vf[m & 3][0] = vtr(c, SO, 2 * (mm >> 3), mm & 7); vf[m & 3][1] = vtr(c, SO, 2 * (mm >> 3) + 1, mm & 7); }
        if (!LAST && m >= 14) { kf[2 * (m - 14)] = kfrag(c, SN, 2 * (m - 14)); kf[2 * (m - 14) + 1] = kfrag(c, SN, 2 * (m - 14) + 1); }
        if (m == 1) { EXP4(s[0][3]); }
        if (m == 2) { EXP4(s[1][3]); SUMPK(s[0][3], lsum[0], ph[0][3]); }
        if (m == 3) { SUMPK(s[1][3], lsum[1], ph[1][3]); }
        if (!LAST && m >= 4 && m < 12 && (m & 1) == 0) { if (t < 6) DMA_K(c, SO, t + 2, (m - 4) >> 1); }
        SB();
        if (!LAST && m == 13) { if (FIRST) WAIT_BAR(10); else if (t == 6) WAIT_BAR(4); else WAIT_BAR(8); SB(); }
    }
}

DI void safe_tile(const int t, const AttnCtx& c, const bf16x8 (&qf)[3][4], f32x4 (&o)[3][8], float (&mref)[3], float (&lsum)[3]) {
    asm volatile("s_waitcnt lgkmcnt(0)\n\ts_barrier" ::: "memory");
    DMA_K(c, STG, t, 0); DMA_K(c, STG, t, 1); DMA_K(c, STG, t, 2); DMA_K(c, STG, t, 3);
    DMA_V(c, STG, t, 0); DMA_V(c, STG, t, 1); DMA_V(c, STG, t, 2); DMA_V(c, STG, t, 3);
    WAIT_BAR(0);
    const f32x4 z4 = {0.f, 0.f, 0.f, 0.f};
#pragma unroll
    for (int a = 0; a < 3; ++a) {
        f32x4 s[4];
#pragma unroll
        for (int i = 0; i < (a < 2 ? 4 : 1); ++i) {
            s[i] = z4;
#pragma unroll
            for (int ks = 0; ks < 4; ++ks) s[i] = MFMA32(kfrag(c, STG, i * 4 + ks), qf[a][ks], s[i]);
        }
        float m = fmaxf(fmaxf(s[0][0], s[0][1]), fmaxf(s[0][2], s[0][3]));
        if (a < 2) {
#pragma unroll
            for (int i = 1; i < 4; ++i) m = fmaxf(m, fmaxf(fmaxf(s[i][0], s[i][1]), fmaxf(s[i][2], s[i][3])));
        }
        m = fmaxf(xmax(m), mref[a]);
        const float f = __builtin_amdgcn_exp2f(mref[a] - m);
        mref[a] = m; lsum[a] *= f;
        u32x2 ph[4];
#pragma unroll
        for (int i = 0; i < (a < 2 ? 4 : 1); ++i) { s[i] -= m; EXP4(s[i]); SUMPK(s[i], lsum[a], ph[i]); }
#pragma unroll
        for (int dt = 0; dt < 8; ++dt) {
            o[a][dt] *= f;
            if (a < 2) {
                o[a][dt] = MFMA32(cat(vtr(c, STG, 0, dt), vtr(c, STG, 1, dt)), __builtin_bit_cast(bf16x8, __builtin_shufflevector(ph[0], ph[1], 0, 1, 2, 3)), o[a][dt]);
                o[a][dt] = MFMA32(cat(vtr(c, STG, 2, dt), vtr(c, STG, 3, dt)), __builtin_bit_cast(bf16x8, __builtin_shufflevector(ph[2], ph[3], 0, 1, 2, 3)), o[a][dt]);
            } else {
                o[a][dt] = MFMA16(vtr(c, STG, 0, dt), __builtin_bit_cast(bf16x4, ph[0]), o[a][dt]);
            }
        }
        SB();
    }
}

DI bf16x8 qfrag(const f32x4 t0, const f32x4 t1) {
    unsigned a0 = cvtpk(t0[0] * QSCALE, t0[1] * QSCALE), a1 = cvtpk(t0[2] * QSCALE, t0[3] * QSCALE);
    unsigned b0 = cvtpk(t1[0] * QSCALE, t1[1] * QSCALE), b1 = cvtpk(t1[2] * QSCALE, t1[3] * QSCALE);
    auto r0 = __builtin_amdgcn_permlane32_swap(a0, b0, false, false);
    auto r1 = __builtin_amdgcn_permlane32_swap(a1, b1, false, false);
    auto s0 = __builtin_amdgcn_permlane16_swap(r0[0], r0[1], false, false);
    auto s1 = __builtin_amdgcn_permlane16_swap(r1[0], r1[1], false, false);
    u32x4 u = {s0[0], s1[0], s0[1], s1[1]};
    return __builtin_bit_cast(bf16x8, u);
}

DI bf16x8 wfrag(const QCtx& q, const int so, const int s, const int j) {
    const int im = so + XW_W + 8 * s * 1040 + (j < 8 ? (j >> 1) * 256 + (j & 1) * 32 : (j & 1) * 32);
    const unsigned b = j < 8 ? q.wa : q.wax;
    return cat(ldtr(q.lds + b + im), ldtr(q.lds + b + im + 1040));
}
DI void xfrags(const QCtx& q, const int so, const int s, bf16x8 (&xb)[3]) {
    xb[0] = ld128(q.lds + q.xa_own + (so + s * 512)); xb[1] = ld128(q.lds + q.xa_own + (so + 2048 + s * 512)); xb[2] = ld128(q.lds + q.xa_x + (so + s * 512));
}
template <int CS, int MODE, int PREV>
DI void q_chunk(const QCtx& q, const AttnCtx& ac, const int c, f32x4 (&qa)[2][8], f32x4 (&qx)[2], bf16x8 (&af)[10], bf16x8 (&xb0)[3]) {
    constexpr int SO = CS * STG, SN = (CS ^ 1) * STG;
    bf16x8 xb1[3];
#pragma unroll
    for (int j = 0; j < 9; ++j) {
        if (PREV == 3) {
            if (j < 8) { MFMA32Z(qa[0][j], af[j], xb0[0]); MFMA32Z(qa[1][j], af[j], xb0[1]); } else { MFMA32Z(qx[0], af[8], xb0[2]); MFMA32Z(qx[1], af[9], xb0[2]); }
        } else {
            if (j < 8) { MFMA32T(qa[0][j], af[j], xb0[0]); MFMA32T(qa[1][j], af[j], xb0[1]); } else { MFMA32T(qx[0], af[8], xb0[2]); MFMA32T(qx[1], af[9], xb0[2]); }
        }
        if (j < 8) af[j] = wfrag(q, SO, 1, j); else { af[8] = wfrag(q, SO, 1, 8); af[9] = wfrag(q, SO, 1, 9); }
        if (j == 0) xfrags(q, SO, 1, xb1);
        if (PREV == 0) {
            if (j == 0) q_dma<5, SN>(q, c + 1);
            if (j == 1) q_dma<6, SN>(q, c + 1);
            if (j == 2) q_dma<7, SN>(q, c + 1);
            if (j == 3) q_dma<8, SN>(q, c + 1);
        }
        if (PREV == 1) { if (j < 4) DMA_V(ac, 0, 0, j); }
        SB();
    }
#pragma unroll
    for (int j = 0; j < 9; ++j) {
        if (j < 8) { MFMA32T(qa[0][j], af[j], xb1[0]); MFMA32T(qa[1][j], af[j], xb1[1]); }
        else { MFMA32T(qx[0], af[8], xb1[2]); MFMA32T(qx[1], af[9], xb1[2]); }
        if (MODE != 2) {
            if (j == 2) { xfrags(q, SN, 0, xb0); af[0] = wfrag(q, SN, 0, 0); af[1] = wfrag(q, SN, 0, 1); af[2] = wfrag(q, SN, 0, 2); }
            if (j > 2 && j < 8) af[j] = wfrag(q, SN, 0, j);
            if (j == 8) { af[8] = wfrag(q, SN, 0, 8); af[9] = wfrag(q, SN, 0, 9); }
        }
        if (MODE == 0) {
            if (j == 3) q_dma<0, SO>(q, c + 2);
            if (j == 4) q_dma<1, SO>(q, c + 2);
            if (j == 5) q_dma<2, SO>(q, c + 2);
            if (j == 7) q_dma<3, SO>(q, c + 2);
            if (j == 8) q_dma<4, SO>(q, c + 2);
        }
        if (MODE == 1) { if (j >= 3 && j < 7) DMA_K(ac, 0, 0, j - 3); }
        SB();
        if (MODE != 2 && j == 1) { WAIT_BAR(0); SB(); }
    }
}

#define OPOL " sc1"
DI void ostore(char* p, const u32x4 v) { asm volatile("global_store_dwordx4 %0, %1, off" OPOL "\n\ts_nop 1" :: "v"(p), "v"(v) : "memory"); }
#define STAGGER 7000
DI void l2_touch(const char* xp, const char* wp, const int wpart, const int xpart, const int w, const unsigned ldsd) {
    const int l = opaque_lane();
    const int i0 = wpart * 160 + w * 40 + (l < 40 ? l : 39);
    const int i1 = xpart * 180 + w * 45 + (l < 45 ? l : 44);
    asm volatile("s_mov_b32 m0, %4\n\ts_nop 0\n\tglobal_load_lds_dword %0, %2\n\tglobal_load_lds_dword %1, %3"
                 :: "v"((i0 >> 1) * (HD * 2) + (i0 & 1) * 128), "v"(i1 * 128), "s"(wp), "s"(xp), "s"(ldsd) : "memory");
}

__global__ __launch_bounds__(256, 2) void k1_qattn(const K1Args args) {
    extern __shared__ __attribute__((aligned(16))) char smem[];
    const lds_cptr lds = (lds_cptr)smem;
    const unsigned ldsb = (unsigned)(uintptr_t)smem;
    const int w = rfl(threadIdx.x >> 6);
    const int xcd = blockIdx.x & 7, idx = blockIdx.x >> 3;
    const int br = xcd >> 2, b = ((xcd >> 1) & 1) * 8 + (idx & 7), h0 = (xcd & 1) * 16 + (idx >> 3);
#define DIAGP args.ows
    QCtx q;
    q.lds = lds; q.ldsx = ldsb + w * 4096; q.ldsw = ldsb + XW_W + w * 4160; q.ldsx4 = ldsb + 16384 + w * 512; q.w = w;
    const unsigned ldsd = ldsb + ST_OFF + 768;
    q.xp = args.x[br] + (size_t)b * (SQ * DM * 2);
    q.wp = args.wq[br] + h0 * (DH * 2);
    if (STAGGER > 0 && blockIdx.x >= 256) {
        const unsigned long long t0 = __builtin_amdgcn_s_memtime();
        l2_touch(q.xp, q.wp - 4 * (DH * 2), idx & 7, (idx >> 3) & 3, w, ldsd);
        l2_touch(q.xp, q.wp, idx & 7, ((idx >> 3) & 3) + 4, w, ldsd);
        while (__builtin_amdgcn_s_memtime() - t0 < (unsigned long long)STAGGER) __builtin_amdgcn_s_sleep(8);
    }
    q_dma_setup(q);
    q_dma<8, 0>(q, 0); q_dma<0, 0>(q, 0); q_dma<1, 0>(q, 0); q_dma<2, 0>(q, 0); q_dma<3, 0>(q, 0); q_dma<4, 0>(q, 0); q_dma<5, 0>(q, 0); q_dma<6, 0>(q, 0); q_dma<7, 0>(q, 0);

    for (int it = 0; it < 2; ++it) {
        const int h = h0 + 8 * it;
        const bool more = (it == 0);
        STAMP(4 * it + 0);
        const int l = opaque_lane(), r = l & 15, g = l >> 4, q4 = r >> 2, p = l & 3, row8 = (l >> 2) & 7, hf = l >> 5;
        AttnCtx ac;
        ac.lds = lds; ac.ldsk = ldsb + KS0 + w * 4096; ac.ldsv = ldsb + VS0 + w * 4224;
        ac.kp = args.k[br] + (size_t)(b * NH + h) * (SK * DH * 2) + w * 4096;
        ac.vp = args.v[br] + (size_t)(b * NH + h) * (SK * DH * 2) + w * 4096;
#pragma unroll
        for (int j = 0; j < 2; ++j) ac.kvo[j] = row8 * 256 + hf * 64 + ((p ^ fsw((j << 1) | (row8 >> 2))) * 16);
        ac.vvo = q4 * 256 + g * 64 + p * 16;
        f32x4 qa[2][8], qx[2];
        {
            q_dma_setup(q);
            const unsigned xa = (r >> 3) * 1024 + (r & 7) * 64 + ((g ^ fsw((r >> 2) & 3)) * 16);
            q.xa_own = xa + w * 4096; q.xa_x = xa + 8 * 2048;
            q.wa = g * 2080 + q4 * 64 + p * 8; q.wax = q.wa + w * 256;
            SB();
            if (it == 0) WAIT_BAR(0); else WAIT_BAR(9);
            bf16x8 af[10], xb0[3];
            xfrags(q, 0, 0, xb0);
#pragma unroll
            for (int j = 0; j < 10; ++j) af[j] = wfrag(q, 0, 0, j);
            q_dma<0, STG>(q, 1); q_dma<1, STG>(q, 1); q_dma<2, STG>(q, 1); q_dma<3, STG>(q, 1); q_dma<4, STG>(q, 1); q_dma<5, STG>(q, 1); q_dma<6, STG>(q, 1); q_dma<7, STG>(q, 1); q_dma<8, STG>(q, 1);
            SB();
            q_chunk<0, 0, 3>(q, ac, 0, qa, qx, af, xb0);
            for (int c = 1; c < 7; c += 2) { q_chunk<1, 0, 0>(q, ac, c, qa, qx, af, xb0); q_chunk<0, 0, 0>(q, ac, c + 1, qa, qx, af, xb0); }
            q_chunk<1, 0, 0>(q, ac, 7, qa, qx, af, xb0);
            q_chunk<0, 1, 0>(q, ac, 8, qa, qx, af, xb0);
            q_chunk<1, 2, 1>(q, ac, 9, qa, qx, af, xb0);
            MFMA_PAD(); SB();
        }
        bf16x8 qf[3][4];
        *LDSP(bf16x8, (lds_ptr)lds + QX_OFF + w * 1024 + l * 16) = qfrag(qx[0], qx[1]);
#pragma unroll
        for (int ks = 0; ks < 4; ++ks) { qf[0][ks] = qfrag(qa[0][2 * ks], qa[0][2 * ks + 1]); qf[1][ks] = qfrag(qa[1][2 * ks], qa[1][2 * ks + 1]); }
#pragma unroll
        for (int i = 0; i < 4; ++i) {
            const unsigned kt = (unsigned)((i + w) & 3);
            ac.ka[i] = kt * 4096 + (r >> 3) * 2048 + (r & 7) * 64 + ((g ^ fsw((r >> 2) & 3)) * 16);
            ac.va[i] = (kt * 4 + g) * 1056 + q4 * 64 + p * 8;
        }
        SB();
        WAIT_BAR(4);
        bf16x8 kf[4];
#pragma unroll
        for (int n = 0; n < 4; ++n) kf[n] = kfrag(ac, 0, n);
#pragma unroll
        for (int ks = 0; ks < 4; ++ks) qf[2][ks] = ld128(lds + QX_OFF + ks * 1024 + l * 16);
        DMA_K(ac, STG, 1, 0); DMA_K(ac, STG, 1, 1); DMA_K(ac, STG, 1, 2); DMA_K(ac, STG, 1, 3);
        DMA_V(ac, STG, 1, 0); DMA_V(ac, STG, 1, 1); DMA_V(ac, STG, 1, 2); DMA_V(ac, STG, 1, 3);
        q.wp += 8 * (DH * 2);
        {
            const char* wn = more ? q.wp : q.wp - 8 * (DH * 2);
            l2_touch(q.xp, wn, idx & 7, idx >> 3, w, ldsd);
        }
        SB();
        STAMP(4 * it + 1);
        PRIO(1);
        f32x4 o[3][8], negm[3]; float lsum[3] = {0.f, 0.f, 0.f};
        fast_tile<0, true, false>(0, ac, qf, o, negm, lsum, kf, q, more);
        for (int t = 1; t < 7; t += 2) {
            fast_tile<1, false, false>(t, ac, qf, o, negm, lsum, kf, q, more);
            fast_tile<0, false, false>(t + 1, ac, qf, o, negm, lsum, kf, q, more);
        }
        q_dma_setup(q);
        SB();
        fast_tile<1, false, true>(7, ac, qf, o, negm, lsum, kf, q, more);
        MFMA_PAD(); SB();
        PRIO(0);
        STAMP(4 * it + 2);
        float mref[3] = {-negm[0][0], -negm[1][0], -negm[2][0]};
        {
            const bool bad = !(lsum[0] < 4096.f) || !(lsum[1] < 4096.f) || !(lsum[2] < 4096.f);
            const unsigned anybad = __any(bad) ? 1u : 0u;
            if (l == 0) *LDSP(unsigned, (lds_ptr)lds + ST_OFF + 512 + w * 4) = anybad;
            if (g == 0) *LDSP(float, (lds_ptr)lds + ST_OFF + w * 64 + r * 4) = mref[2];
        }
        asm volatile("s_waitcnt lgkmcnt(0)\n\ts_barrier" ::: "memory");
        {
            const u32x4 fl = *LDSP(const u32x4, lds + ST_OFF + 512);
            if (__builtin_expect(rfl(fl[0] | fl[1] | fl[2] | fl[3]) != 0, 0)) {
#pragma unroll
                for (int a = 0; a < 3; ++a) {
                    mref[a] = -3.0e38f; lsum[a] = 0.f;
#pragma unroll
                    for (int dt = 0; dt < 8; ++dt) o[a][dt] = f32x4{0, 0, 0, 0};
                }
                for (int t = 0; t < 8; ++t) safe_tile(t, ac, qf, o, mref, lsum);
                if (g == 0) *LDSP(float, (lds_ptr)lds + ST_OFF + w * 64 + r * 4) = mref[2];
                asm volatile("s_waitcnt lgkmcnt(0)\n\ts_barrier" ::: "memory");
            }
        }
        float mo[4];
#pragma unroll
        for (int i = 0; i < 4; ++i) mo[i] = *LDSP(const float, lds + ST_OFF + i * 64 + r * 4);
        const float mall = fmaxf(fmaxf(mo[0], mo[1]), fmaxf(mo[2], mo[3]));
        const float fx = __builtin_amdgcn_exp2f(mref[2] - mall);
#pragma unroll
        for (int dt = 0; dt < 8; ++dt) *LDSP(f32x4, (lds_ptr)lds + STG + w * 8192 + dt * 1024 + l * 16) = o[2][dt] * fx;
        const float lx = xsum(lsum[2]) * fx;
        if (g == 0) *LDSP(float, (lds_ptr)lds + ST_OFF + 256 + w * 64 + r * 4) = lx;
        char* ob = args.ows + ((size_t)((br * NB + b) * 9) * NH + h) * 4096 + l * 16;
#pragma unroll
        for (int a = 0; a < 2; ++a) {
            const float il = __builtin_amdgcn_rcpf(xsum(lsum[a]));
#pragma unroll
            for (int s = 0; s < 4; ++s) {
                const f32x4 u = o[a][2 * s] * il, v = o[a][2 * s + 1] * il;
                const u32x4 pk = {cvtpk(u[0], u[1]), cvtpk(u[2], u[3]), cvtpk(v[0], v[1]), cvtpk(v[2], v[3])};
                ostore(ob + (size_t)(2 * w + a) * (NH * 4096) + s * 1024, pk);
            }
        }
        asm volatile("s_waitcnt lgkmcnt(0)\n\ts_barrier" ::: "memory");
        f32x4 u = {0, 0, 0, 0}, v = {0, 0, 0, 0}; float lt = 0.f;
#pragma unroll
        for (int i = 0; i < 4; ++i) {
            u += *LDSP(const f32x4, lds + STG + i * 8192 + (2 * w) * 1024 + l * 16);
            v += *LDSP(const f32x4, lds + STG + i * 8192 + (2 * w + 1) * 1024 + l * 16);
            lt += *LDSP(const float, lds + ST_OFF + 256 + i * 64 + r * 4);
        }
        const float il = __builtin_amdgcn_rcpf(lt);
        u *= il; v *= il;
        const u32x4 pk = {cvtpk(u[0], u[1]), cvtpk(u[2], u[3]), cvtpk(v[0], v[1]), cvtpk(v[2], v[3])};
        ostore(ob + (size_t)8 * (NH * 4096) + w * 1024, pk);
        SB();
        STAMP(4 * it + 3);
    }
#undef DIAGP
}

constexpr int K2_STG = 32768, K2_NS = 4, K2_LDS = 147456;
struct K2Args { const char* ows; const char* wo[2]; char* out; };

__global__ __launch_bounds__(512, 2) void k2_oproj(const K2Args args) {
    extern __shared__ __attribute__((aligned(16))) char smem[];
    const lds_cptr lds = (lds_cptr)smem;
    const unsigned ldsb = (unsigned)(uintptr_t)smem;
    const int tid = threadIdx.x, l = tid & 63, r = l & 15, g = l >> 4, q4 = r >> 2, p = l & 3;
    const int w = rfl(tid >> 6), kq = w & 3, mh = w >> 2;
    const int xcd = blockIdx.x & 7, idx = blockIdx.x >> 3;
    const int br = xcd >> 2, mt = (xcd & 3) * 6 + idx / 5, nt = idx % 5;
    const int row8 = (l >> 2) & 7, hf = l >> 5;
    const char* wp = args.wo[br] + (size_t)(kq * 1024 + mh * 16) * (DM * 2) + nt * 256 - BIAS;
    const char* ap = args.ows + ((size_t)(br * 144 + mt * 6 + mh * 3) * NH + kq * 8) * 4096;
    unsigned wv[4];
#pragma unroll
    for (int j = 0; j < 4; ++j) wv[j] = BIAS + (8 * (j >> 1) + row8) * (DM * 2) + (j & 1) * 128 + hf * 64 + ((p ^ (2 * (row8 >> 2))) * 16) - j * 1024;
    const unsigned av = l * 16;
    unsigned ba[2];
#pragma unroll
    for (int a = 0; a < 2; ++a) ba[a] = kq * 8192 + (g >> 1) * 2048 + (4 * (g & 1) + q4) * 64 + ((a ^ (g & 1)) * 32) + p * 8;
    f32x4 acc[3][8];
#pragma unroll
    for (int i = 0; i < 3; ++i)
#pragma unroll
        for (int n = 0; n < 8; ++n) acc[i][n] = f32x4{0, 0, 0, 0};
    u32x4 af[4][3];
#define K2_ISSUE(st, SET) do { const int st_ = (st) < 32 ? (st) : 31;                                                              \
        DMA4("", ldsb + (SET) * K2_STG + kq * 8192 + mh * 4096, wv[0], wv[1], wv[2], wv[3], wp + (size_t)st_ * (32 * DM * 2));     \
        const char* a_ = ap + st_ * 1024;                                                                                          \
        asm volatile("global_load_dwordx4 %0, %3, %4\n\tglobal_load_dwordx4 %1, %3, %5\n\tglobal_load_dwordx4 %2, %3, %6"          \
                     : "=&v"(af[SET][0]), "=&v"(af[SET][1]), "=&v"(af[SET][2])                                                     \
                     : "v"(av), "s"(a_), "s"(a_ + NH * 4096), "s"(a_ + 2 * NH * 4096) : "memory"); } while (0)
#define K2_STEP(st, SET) do {                                                                                                      \
        asm volatile("s_waitcnt vmcnt(14) lgkmcnt(0)\n\ts_barrier" : "+v"(af[SET][0]), "+v"(af[SET][1]), "+v"(af[SET][2]) :: "memory"); \
        K2_ISSUE((st) + 3, ((SET) + 3) & 3);                                                                                       \
        _Pragma("unroll") for (int n = 0; n < 8; ++n) {                                                                            \
            const int im = (SET) * K2_STG + (n >> 1) * 512;                                                                        \
            const bf16x8 bw = cat(ldtr(lds + ba[n & 1] + im), ldtr(lds + ba[n & 1] + im + 4096));                                  \
            _Pragma("unroll") for (int i = 0; i < 3; ++i) acc[i][n] = MFMA32(bw, __builtin_bit_cast(bf16x8, af[SET][i]), acc[i][n]); \
        } } while (0)
    K2_ISSUE(0, 0); K2_ISSUE(1, 1); K2_ISSUE(2, 2);
    for (int st = 0; st < 32; st += 4) { K2_STEP(st, 0); K2_STEP(st + 1, 1); K2_STEP(st + 2, 2); K2_STEP(st + 3, 3); }
    asm volatile("s_waitcnt vmcnt(0) lgkmcnt(0)\n\ts_barrier" : "+v"(af[0][0]), "+v"(af[0][1]), "+v"(af[0][2]), "+v"(af[1][0]), "+v"(af[1][1]), "+v"(af[1][2]),
                 "+v"(af[2][0]), "+v"(af[2][1]), "+v"(af[2][2]) :: "memory");
#pragma unroll
    for (int d = 1; d < 4; ++d) {
#pragma unroll
        for (int dd = 0; dd < 4; ++dd) {
            if ((kq ^ d) == dd) {
#pragma unroll
                for (int i = 0; i < 3; ++i)
#pragma unroll
                    for (int a = 0; a < 2; ++a)
                        *LDSP(f32x4, (lds_ptr)lds + (mh * 4 + dd) * 18432 + (d - 1) * 6144 + (i * 2 + a) * 1024 + l * 16) = acc[i][2 * dd + a];
            }
        }
    }
    asm volatile("s_waitcnt lgkmcnt(0)\n\ts_barrier" ::: "memory");
    f32x4 res[3][2];
#pragma unroll
    for (int dd = 0; dd < 4; ++dd) {
        if (kq == dd) {
#pragma unroll
            for (int i = 0; i < 3; ++i) { res[i][0] = acc[i][2 * dd]; res[i][1] = acc[i][2 * dd + 1]; }
        }
    }
#pragma unroll
    for (int d = 0; d < 3; ++d)
#pragma unroll
        for (int i = 0; i < 3; ++i)
#pragma unroll
            for (int a = 0; a < 2; ++a) res[i][a] += *LDSP(const f32x4, lds + w * 18432 + d * 6144 + (i * 2 + a) * 1024 + l * 16);
#pragma unroll
    for (int i = 0; i < 3; ++i) {
        unsigned a0 = cvtpk(res[i][0][0], res[i][0][1]), a1 = cvtpk(res[i][0][2], res[i][0][3]);
        unsigned b0 = cvtpk(res[i][1][0], res[i][1][1]), b1 = cvtpk(res[i][1][2], res[i][1][3]);
        auto r0 = __builtin_amdgcn_permlane32_swap(a0, b0, false, false);
        auto r1 = __builtin_amdgcn_permlane32_swap(a1, b1, false, false);
        auto s0 = __builtin_amdgcn_permlane16_swap(r0[0], r0[1], false, false);
        auto s1 = __builtin_amdgcn_permlane16_swap(r1[0], r1[1], false, false);
        const u32x4 pk = {s0[0], s1[0], s0[1], s1[1]};
        const int row = (mt * 6 + mh * 3 + i) * 16 + r;
        *(u32x4*)(args.out + (size_t)row * (2 * DM * 2) + br * (DM * 2) + nt * 256 + kq * 64 + g * 16) = pk;
    }
}

extern "C" void kernel_launch(void* const* d_in, const int* in_sizes, int n_in, void* d_out, int out_size, void* d_ws, size_t ws_size,
                              hipStream_t stream) {
    (void)in_sizes; (void)n_in; (void)out_size; (void)ws_size;
    (void)hipFuncSetAttribute((const void*)k1_qattn, hipFuncAttributeMaxDynamicSharedMemorySize, K1_LDS);
    (void)hipFuncSetAttribute((const void*)k2_oproj, hipFuncAttributeMaxDynamicSharedMemorySize, K2_LDS);
    K1Args a1;
    a1.x[0] = (const char*)d_in[0]; a1.k[0] = (const char*)d_in[1]; a1.v[0] = (const char*)d_in[2]; a1.wq[0] = (const char*)d_in[3];
    a1.x[1] = (const char*)d_in[5]; a1.k[1] = (const char*)d_in[6]; a1.v[1] = (const char*)d_in[7]; a1.wq[1] = (const char*)d_in[8];
    a1.ows = (char*)d_ws;
    K2Args a2; a2.ows = (const char*)d_ws; a2.wo[0] = (const char*)d_in[4]; a2.wo[1] = (const char*)d_in[9]; a2.out = (char*)d_out;
    hipLaunchKernelGGL(k1_qattn, dim3(512), dim3(256), K1_LDS, stream, a1);
    hipLaunchKernelGGL(k2_oproj, dim3(240), dim3(512), K2_LDS, stream, a2);
}
```

```cpp
#include <hip/hip_runtime.h>
#include <hip/hip_bf16.h>

typedef short bf16x8 __attribute__((ext_vector_type(8)));
typedef short bf16x4 __attribute__((ext_vector_type(4)));
typedef float f32x4 __attribute__((ext_vector_type(4)));
typedef float f32x2 __attribute__((ext_vector_type(2)));
typedef __bf16 bf2_t __attribute__((ext_vector_type(2)));
typedef unsigned u32x4 __attribute__((ext_vector_type(4)));
typedef unsigned u32x2 __attribute__((ext_vector_type(2)));
typedef __attribute__((address_space(3))) char* lds_ptr;
typedef __attribute__((address_space(3))) const char* lds_cptr;
#define DI __device__ __forceinline__
#define LDSP(T, p) ((__attribute__((address_space(3))) T*)(p))

constexpr int NB = 16, SQ = 144, DM = 640, NH = 32, SK = 512, DH = 128, HD = NH * DH;
constexpr float QSCALE = 0.12751743082459868f;
constexpr float THR = 10.0f;

#define MFMA32(a, b, c) __builtin_amdgcn_mfma_f32_16x16x32_bf16((a), (b), (c), 0, 0, 0)
#define MFMA16(a, b, c) __builtin_amdgcn_mfma_f32_16x16x16bf16_1k((a), (b), (c), 0, 0, 0)

#define MFMA32T(acc, a, b) asm volatile("v_mfma_f32_16x16x32_bf16 %0, %1, %2, %0" : "+v"(acc) : "v"(a), "v"(b))
#define MFMA32Z(acc, a, b) asm volatile("v_mfma_f32_16x16x32_bf16 %0, %1, %2, 0" : "=&v"(acc) : "v"(a), "v"(b))
#define MFMA16T(acc, a, b) asm volatile("v_mfma_f32_16x16x16_bf16 %0, %1, %2, %0" : "+v"(acc) : "v"(a), "v"(b))
#define MFMA16Z(acc, a, b) asm volatile("v_mfma_f32_16x16x16_bf16 %0, %1, %2, 0" : "=&v"(acc) : "v"(a), "v"(b))
#define MFMA_PAD() asm volatile("s_nop 7\n\ts_nop 3" ::: "memory")
DI unsigned cvtpk(float lo, float hi) { f32x2 v = {lo, hi}; bf2_t b = __builtin_convertvector(v, bf2_t); return __builtin_bit_cast(unsigned, b); }
DI bf16x8 ld128(lds_cptr p) { return *LDSP(const bf16x8, p); }
DI bf16x4 ldtr(lds_cptr p) { return __builtin_amdgcn_ds_read_tr16_b64_v4i16(LDSP(bf16x4, (lds_ptr)p)); }
DI bf16x8 cat(bf16x4 lo, bf16x4 hi) { return __builtin_shufflevector(lo, hi, 0, 1, 2, 3, 4, 5, 6, 7); }
DI int fsw(int u) { return (((u ^ (u >> 1)) & 1) << 1) | ((u >> 1) & 1); }
DI unsigned rfl(unsigned x) { return __builtin_amdgcn_readfirstlane(x); }

#define DMA4(POL, m0v, v0, v1, v2, v3, sb)                                                                                   \
    asm volatile("s_mov_b32 m0, %0\n\ts_nop 0\n\t"                                                                            \
                 "global_load_lds_dwordx4 %1, %5" POL "\n\t"                                                                  \
                 "global_load_lds_dwordx4 %2, %5 offset:1024" POL "\n\t"                                                      \
                 "global_load_lds_dwordx4 %3, %5 offset:2048" POL "\n\t"                                                      \
                 "global_load_lds_dwordx4 %4, %5 offset:3072" POL                                                             \
                 :: "s"(m0v), "v"(v0), "v"(v1), "v"(v2), "v"(v3), "s"(sb) : "memory")
#define DMA1(POL, m0v, v0, sb)                                                                                               \
    asm volatile("s_mov_b32 m0, %0\n\ts_nop 0\n\tglobal_load_lds_dwordx4 %1, %2" POL :: "s"(m0v), "v"(v0), "s"(sb) : "memory")
#define DIAG_LDS 75264
#define WAIT_BAR(N) asm volatile("s_waitcnt vmcnt(" #N ") lgkmcnt(0)\n\ts_barrier" ::: "memory")

#define STAMP(i)
constexpr int XW_W = 18432;
constexpr int STG = XW_W + 16 * 1040;
constexpr int KS0 = 0, VS0 = 16384;
constexpr int QX_OFF = 2 * STG;
constexpr int ST_OFF = QX_OFF + 4096;
constexpr int K1_LDS = ST_OFF + 1024;
constexpr int BIAS = 3072;
#define PRIOV 1
#define PRIO(x) do { if (PRIOV) __builtin_amdgcn_s_setprio(x); } while (0)
#define SB() __builtin_amdgcn_sched_barrier(0)
#define PIN(x) asm volatile("" : "+v"(x))
#define DMAP(POL, m0b, M0OFF, vo, sb, IMM) \
    asm volatile("s_add_u32 m0, %0, %4\n\ts_nop 0\n\tglobal_load_lds_dwordx4 %1, %2 offset:%3" POL :: "s"(m0b), "v"(vo), "s"(sb), "i"(IMM), "i"(M0OFF) : "memory", "scc")

struct K1Args { const char* x[2]; const char* k[2]; const char* v[2]; const char* wq[2]; const char* wo[2]; char* ows; };

struct AttnCtx {
    lds_cptr lds; unsigned ldsk, ldsv;
    unsigned ka[4], va[4], kvo[2], vvo;
    const char* kp; const char* vp;
};
template <int SO, int J> DI void dma_k(const AttnCtx& c, const int t) { DMAP(" nt", c.ldsk, SO + (J & 1) * 896, c.kvo[J >> 1], c.kp + t * 16384, (J >> 1) * 2048 + (J & 1) * 128); }
template <int SO, int J> DI void dma_v(const AttnCtx& c, const int t) { DMAP(" nt", c.ldsv, SO + J * 32, c.vvo, c.vp + t * 16384, J * 1024); }
#define DMA_K(c, SO, t, j) do { switch (j) { case 0: dma_k<SO, 0>(c, t); break; case 1: dma_k<SO, 1>(c, t); break; case 2: dma_k<SO, 2>(c, t); break; default: dma_k<SO, 3>(c, t); break; } } while (0)
#define DMA_V(c, SO, t, j) do { switch (j) { case 0: dma_v<SO, 0>(c, t); break; case 1: dma_v<SO, 1>(c, t); break; case 2: dma_v<SO, 2>(c, t); break; default: dma_v<SO, 3>(c, t); break; } } while (0)
DI bf16x8 kfrag(const AttnCtx& c, int so, int n) { return ld128(c.lds + c.ka[n >> 2] + (so + KS0 + (n & 3) * 512)); }
DI bf16x4 vtr(const AttnCtx& c, int so, int i, int dt) { return ldtr(c.lds + c.va[i] + (so + VS0 + (dt >> 1) * 256 + (dt & 1) * 32)); }
DI float xmax(float m) {
    auto a = __builtin_amdgcn_permlane32_swap(__float_as_uint(m), __float_as_uint(m), false, false);
    m = fmaxf(__uint_as_float(a[0]), __uint_as_float(a[1]));
    auto b = __builtin_amdgcn_permlane16_swap(__float_as_uint(m), __float_as_uint(m), false, false);
    return fmaxf(__uint_as_float(b[0]), __uint_as_float(b[1]));
}
DI float xsum(float m) {
    auto a = __builtin_amdgcn_permlane32_swap(__float_as_uint(m), __float_as_uint(m), false, false);
    m = __uint_as_float(a[0]) + __uint_as_float(a[1]);
    auto b = __builtin_amdgcn_permlane16_swap(__float_as_uint(m), __float_as_uint(m), false, false);
    return __uint_as_float(b[0]) + __uint_as_float(b[1]);
}
#define EXP4(v) do { v[0] = __builtin_amdgcn_exp2f(v[0]); v[1] = __builtin_amdgcn_exp2f(v[1]); v[2] = __builtin_amdgcn_exp2f(v[2]); v[3] = __builtin_amdgcn_exp2f(v[3]); PIN(v); } while (0)
#define SUMPK(v, ls, d) do { ls += v[0]; ls += v[1]; ls += v[2]; ls += v[3]; d = u32x2{cvtpk(v[0], v[1]), cvtpk(v[2], v[3])}; PIN(ls); PIN(d); } while (0)

struct QCtx {
    lds_cptr lds; unsigned ldsx, ldsw, ldsx4; int w;
    unsigned xo[5], wv[4], xa_own, xa_x, wa, wax;
    const char* xp; const char* wp;
};
template <int J, int SO> DI void q_dma(const QCtx& q, const int c) {
    if (J < 4) DMAP("", q.ldsw, SO + (J & 3) * 1040, q.wv[J & 3], q.wp + (size_t)c * (64 * HD * 2), 0);
    else if (J < 8) DMAP("", q.ldsx, SO + (J & 3) * 1024, q.xo[J & 3], q.xp + c * 128, 0);
    else
        asm volatile("s_add_u32 m0, %0, %3\n\ts_mov_b32 exec_hi, 0\n\tglobal_load_lds_dwordx4 %1, %2\n\ts_mov_b32 exec_hi, -1"
                     :: "s"(q.ldsx4), "v"(q.xo[4]), "s"(q.xp + c * 128), "i"(SO) : "memory", "scc");
}
template <int SO> DI void q_dma_rt(const QCtx& q, const int j, const int c) {
    switch (j) {
        case 0: q_dma<0, SO>(q, c); break; case 1: q_dma<1, SO>(q, c); break; case 2: q_dma<2, SO>(q, c); break;
        case 3: q_dma<3, SO>(q, c); break; case 4: q_dma<4, SO>(q, c); break; case 5: q_dma<5, SO>(q, c); break;
        case 6: q_dma<6, SO>(q, c); break; case 7: q_dma<7, SO>(q, c); break; default: q_dma<8, SO>(q, c); break;
    }
}
DI int opaque_lane() { int l; asm volatile("v_mbcnt_lo_u32_b32 %0, -1, 0\n\tv_mbcnt_hi_u32_b32 %0, -1, %0" : "=v"(l)); return l; }
DI void q_dma_setup(QCtx& q) {
    const int l = opaque_lane(), g = l >> 4, q4 = (l & 15) >> 2, p = l & 3, row8 = (l >> 2) & 7, hf = l >> 5, w = q.w;
#pragma unroll
    for (int j = 0; j < 4; ++j) {
        q.xo[j] = (8 * (4 * w + j) + row8) * (DM * 2) + hf * 64 + ((p ^ fsw(((j & 1) << 1) | (row8 >> 2))) * 16);
        q.wv[j] = (4 * (4 * w + j) + q4) * (HD * 2) + g * 64 + p * 16;
    }
    q.xo[4] = (8 * (16 + (w >> 1)) + row8) * (DM * 2) + (w & 1) * 64 + ((p ^ fsw(((w >> 1) << 1) | (row8 >> 2))) * 16);
}
template <int SLOT, bool FIRST, bool LAST>
DI void fast_tile(const int t, const AttnCtx& c, const bf16x8 (&qf)[3][4], f32x4 (&o)[3][8], f32x4 (&negm)[3], float (&lsum)[3], bf16x8 (&kf)[4],
                  const QCtx& nq, const bool more) {
    constexpr int SO = SLOT * STG, SN = (SLOT ^ 1) * STG;
    const f32x4 z4 = {0.f, 0.f, 0.f, 0.f};
    f32x4 s[2][4], sx; u32x2 ph[2][4], px; bf16x4 vf[4][2];
#pragma unroll
    for (int n = 0; n < 16; ++n) {
        const int i = n >> 2, ks = n & 3;
        s[0][i] = MFMA32(kf[ks], qf[0][ks], ks ? s[0][i] : ((FIRST && i == 0) ? z4 : negm[0]));
        s[1][i] = MFMA32(kf[ks], qf[1][ks], ks ? s[1][i] : ((FIRST && i == 0) ? z4 : negm[1]));
        if (i == 0) sx = MFMA32(kf[ks], qf[2][ks], ks ? sx : (FIRST ? z4 : negm[2]));
        if (n < 12) kf[ks] = kfrag(c, SO, n + 4);
        if (n >= 14) { vf[n - 14][0] = vtr(c, SO, 0, n - 14); vf[n - 14][1] = vtr(c, SO, 1, n - 14); }
        if (i > 0) {
            if (ks == 1) { EXP4(s[0][i - 1]); }
            if (ks == 2) { EXP4(s[1][i - 1]); SUMPK(s[0][i - 1], lsum[0], ph[0][i - 1]); }
            if (ks == 3) { SUMPK(s[1][i - 1], lsum[1], ph[1][i - 1]); }
            if (i == 2 && ks == 0) { EXP4(sx); }
            if (i == 3 && ks == 0) { SUMPK(sx, lsum[2], px); }
        }
        if (!FIRST && n < 4) { if (t < 7) DMA_V(c, SN, t + 1, n); }
        if (LAST && n >= 2 && n < 11) { if (more) q_dma_rt<SN>(nq, n == 2 ? 8 : n - 3, 0); }
        SB();
        if (FIRST && n == 3) {
            const float m0 = xmax(fmaxf(fmaxf(s[0][0][0], s[0][0][1]), fmaxf(s[0][0][2], s[0][0][3])));
            const float m1 = xmax(fmaxf(fmaxf(s[1][0][0], s[1][0][1]), fmaxf(s[1][0][2], s[1][0][3])));
            const float m2 = xmax(fmaxf(fmaxf(sx[0], sx[1]), fmaxf(sx[2], sx[3])));
            negm[0] = f32x4{-m0, -m0, -m0, -m0}; negm[1] = f32x4{-m1, -m1, -m1, -m1}; negm[2] = f32x4{-m2, -m2, -m2, -m2};
            s[0][0] -= m0; s[1][0] -= m1; sx -= m2;
            SB();
        }
        if (n == 13) { if (LAST) { if (more) WAIT_BAR(9); else WAIT_BAR(0); } else if (FIRST) WAIT_BAR(10); else WAIT_BAR(8); SB(); }
    }
#pragma unroll
    for (int m = 0; m < 16; ++m) {
        const int pr = m >> 3, dt = m & 7;
        const bf16x8 a = cat(vf[m & 3][0], vf[m & 3][1]);
        const u32x4 p0 = __builtin_shufflevector(ph[0][2 * pr], ph[0][2 * pr + 1], 0, 1, 2, 3), p1 = __builtin_shufflevector(ph[1][2 * pr], ph[1][2 * pr + 1], 0, 1, 2, 3);
        if (FIRST && pr == 0) { MFMA32Z(o[0][dt], a, p0); MFMA32Z(o[1][dt], a, p1); MFMA16Z(o[2][dt], vf[m & 3][0], px); }
        else { MFMA32T(o[0][dt], a, p0); MFMA32T(o[1][dt], a, p1); if (pr == 0) MFMA16T(o[2][dt], vf[m & 3][0], px); }
        if (m == 0) {
#pragma unroll
            for (int e = 2; e < 4; ++e) { vf[e][0] = vtr(c, SO, 0, e); vf[e][1] = vtr(c, SO, 1, e); }
        }
        if (m < 12) { const int mm = m + 4; vf[m & 3][0] = vtr(c, SO, 2 * (mm >> 3), mm & 7); vf[m & 3][1] = vtr(c, SO, 2 * (mm >> 3) + 1, mm & 7); }
        if (!LAST && m >= 14) { kf[2 * (m - 14)] = kfrag(c, SN, 2 * (m - 14)); kf[2 * (m - 14) + 1] = kfrag(c, SN, 2 * (m - 14) + 1); }
        if (m == 1) { EXP4(s[0][3]); }
        if (m == 2) { EXP4(s[1][3]); SUMPK(s[0][3], lsum[0], ph[0][3]); }
        if (m == 3) { SUMPK(s[1][3], lsum[1], ph[1][3]); }
        if (!LAST && m >= 4 && m < 12 && (m & 1) == 0) { if (t < 6) DMA_K(c, SO, t + 2, (m - 4) >> 1); }
        SB();
        if (!LAST && m == 13) { if (FIRST) WAIT_BAR(10); else if (t == 6) WAIT_BAR(4); else WAIT_BAR(8); SB(); }
    }
}

DI void safe_tile(const int t, const AttnCtx& c, const bf16x8 (&qf)[3][4], f32x4 (&o)[3][8], float (&mref)[3], float (&lsum)[3]) {
    asm volatile("s_waitcnt lgkmcnt(0)\n\ts_barrier" ::: "memory");
    DMA_K(c, STG, t, 0); DMA_K(c, STG, t, 1); DMA_K(c, STG, t, 2); DMA_K(c, STG, t, 3);
    DMA_V(c, STG, t, 0); DMA_V(c, STG, t, 1); DMA_V(c, STG, t, 2); DMA_V(c, STG, t, 3);
    WAIT_BAR(0);
    const f32x4 z4 = {0.f, 0.f, 0.f, 0.f};
#pragma unroll
    for (int a = 0; a < 3; ++a) {
        f32x4 s[4];
#pragma unroll
        for (int i = 0; i < (a < 2 ? 4 : 1); ++i) {
            s[i] = z4;
#pragma unroll
            for (int ks = 0; ks < 4; ++ks) s[i] = MFMA32(kfrag(c, STG, i * 4 + ks), qf[a][ks], s[i]);
        }
        float m = fmaxf(fmaxf(s[0][0], s[0][1]), fmaxf(s[0][2], s[0][3]));
        if (a < 2) {
#pragma unroll
            for (int i = 1; i < 4; ++i) m = fmaxf(m, fmaxf(fmaxf(s[i][0], s[i][1]), fmaxf(s[i][2], s[i][3])));
        }
        m = fmaxf(xmax(m), mref[a]);
        const float f = __builtin_amdgcn_exp2f(mref[a] - m);
        mref[a] = m; lsum[a] *= f;
        u32x2 ph[4];
#pragma unroll
        for (int i = 0; i < (a < 2 ? 4 : 1); ++i) { s[i] -= m; EXP4(s[i]); SUMPK(s[i], lsum[a], ph[i]); }
#pragma unroll
        for (int dt = 0; dt < 8; ++dt) {
            o[a][dt] *= f;
            if (a < 2) {
                o[a][dt] = MFMA32(cat(vtr(c, STG, 0, dt), vtr(c, STG, 1, dt)), __builtin_bit_cast(bf16x8, __builtin_shufflevector(ph[0], ph[1], 0, 1, 2, 3)), o[a][dt]);
                o[a][dt] = MFMA32(cat(vtr(c, STG, 2, dt), vtr(c, STG, 3, dt)), __builtin_bit_cast(bf16x8, __builtin_shufflevector(ph[2], ph[3], 0, 1, 2, 3)), o[a][dt]);
            } else {
                o[a][dt] = MFMA16(vtr(c, STG, 0, dt), __builtin_bit_cast(bf16x4, ph[0]), o[a][dt]);
            }
        }
        SB();
    }
}

DI bf16x8 qfrag(const f32x4 t0, const f32x4 t1) {
    unsigned a0 = cvtpk(t0[0] * QSCALE, t0[1] * QSCALE), a1 = cvtpk(t0[2] * QSCALE, t0[3] * QSCALE);
    unsigned b0 = cvtpk(t1[0] * QSCALE, t1[1] * QSCALE), b1 = cvtpk(t1[2] * QSCALE, t1[3] * QSCALE);
    auto r0 = __builtin_amdgcn_permlane32_swap(a0, b0, false, false);
    auto r1 = __builtin_amdgcn_permlane32_swap(a1, b1, false, false);
    auto s0 = __builtin_amdgcn_permlane16_swap(r0[0], r0[1], false, false);
    auto s1 = __builtin_amdgcn_permlane16_swap(r1[0], r1[1], false, false);
    u32x4 u = {s0[0], s1[0], s0[1], s1[1]};
    return __builtin_bit_cast(bf16x8, u);
}

DI bf16x8 wfrag(const QCtx& q, const int so, const int s, const int j) {
    const int im = so + XW_W + 8 * s * 1040 + (j < 8 ? (j >> 1) * 256 + (j & 1) * 32 : (j & 1) * 32);
    const unsigned b = j < 8 ? q.wa : q.wax;
    return cat(ldtr(q.lds + b + im), ldtr(q.lds + b + im + 1040));
}
DI void xfrags(const QCtx& q, const int so, const int s, bf16x8 (&xb)[3]) {
    xb[0] = ld128(q.lds + q.xa_own + (so + s * 512)); xb[1] = ld128(q.lds + q.xa_own + (so + 2048 + s * 512)); xb[2] = ld128(q.lds + q.xa_x + (so + s * 512));
}
template <int CS, int MODE, int PREV>
DI void q_chunk(const QCtx& q, const AttnCtx& ac, const int c, f32x4 (&qa)[2][8], f32x4 (&qx)[2], bf16x8 (&af)[10], bf16x8 (&xb0)[3]) {
    constexpr int SO = CS * STG, SN = (CS ^ 1) * STG;
    bf16x8 xb1[3];
#pragma unroll
    for (int j = 0; j < 9; ++j) {
        if (PREV == 3) {
            if (j < 8) { MFMA32Z(qa[0][j], af[j], xb0[0]); MFMA32Z(qa[1][j], af[j], xb0[1]); } else { MFMA32Z(qx[0], af[8], xb0[2]); MFMA32Z(qx[1], af[9], xb0[2]); }
        } else {
            if (j < 8) { MFMA32T(qa[0][j], af[j], xb0[0]); MFMA32T(qa[1][j], af[j], xb0[1]); } else { MFMA32T(qx[0], af[8], xb0[2]); MFMA32T(qx[1], af[9], xb0[2]); }
        }
        if (j < 8) af[j] = wfrag(q, SO, 1, j); else { af[8] = wfrag(q, SO, 1, 8); af[9] = wfrag(q, SO, 1, 9); }
        if (j == 0) xfrags(q, SO, 1, xb1);
        if (PREV == 0) {
            if (j == 0) q_dma<5, SN>(q, c + 1);
            if (j == 1) q_dma<6, SN>(q, c + 1);
            if (j == 2) q_dma<7, SN>(q, c + 1);
            if (j == 3) q_dma<8, SN>(q, c + 1);
        }
        if (PREV == 1) { if (j < 4) DMA_V(ac, 0, 0, j); }
        SB();
    }
#pragma unroll
    for (int j = 0; j < 9; ++j) {
        if (j < 8) { MFMA32T(qa[0][j], af[j], xb1[0]); MFMA32T(qa[1][j], af[j], xb1[1]); }
        else { MFMA32T(qx[0], af[8], xb1[2]); MFMA32T(qx[1], af[9], xb1[2]); }
        if (MODE != 2) {
            if (j == 2) { xfrags(q, SN, 0, xb0); af[0] = wfrag(q, SN, 0, 0); af[1] = wfrag(q, SN, 0, 1); af[2] = wfrag(q, SN, 0, 2); }
            if (j > 2 && j < 8) af[j] = wfrag(q, SN, 0, j);
            if (j == 8) { af[8] = wfrag(q, SN, 0, 8); af[9] = wfrag(q, SN, 0, 9); }
        }
        if (MODE == 0) {
            if (j == 3) q_dma<0, SO>(q, c + 2);
            if (j == 4) q_dma<1, SO>(q, c + 2);
            if (j == 5) q_dma<2, SO>(q, c + 2);
            if (j == 7) q_dma<3, SO>(q, c + 2);
            if (j == 8) q_dma<4, SO>(q, c + 2);
        }
        if (MODE == 1) { if (j >= 3 && j < 7) DMA_K(ac, 0, 0, j - 3); }
        SB();
        if (MODE != 2 && j == 1) { WAIT_BAR(0); SB(); }
    }
}

#define OPOL " sc1"
DI void ostore(char* p, const u32x4 v) { asm volatile("global_store_dwordx4 %0, %1, off" OPOL "\n\ts_nop 1" :: "v"(p), "v"(v) : "memory"); }
#define STAGGER 7000
DI void l2_touch(const char* xp, const char* wp, const int wpart, const int xpart, const int w, const unsigned ldsd) {
    const int l = opaque_lane();
    const int i0 = wpart * 160 + w * 40 + (l < 40 ? l : 39);
    const int i1 = xpart * 180 + w * 45 + (l < 45 ? l : 44);
    asm volatile("s_mov_b32 m0, %4\n\ts_nop 0\n\tglobal_load_lds_dword %0, %2\n\tglobal_load_lds_dword %1, %3"
                 :: "v"((i0 >> 1) * (HD * 2) + (i0 & 1) * 128), "v"(i1 * 128), "s"(wp), "s"(xp), "s"(ldsd) : "memory");
}

__global__ __launch_bounds__(256, 2) void k1_qattn(const K1Args args) {
    extern __shared__ __attribute__((aligned(16))) char smem[];
    const lds_cptr lds = (lds_cptr)smem;
    const unsigned ldsb = (unsigned)(uintptr_t)smem;
    const int w = rfl(threadIdx.x >> 6);
    const int xcd = blockIdx.x & 7, idx = blockIdx.x >> 3;
    const int br = xcd >> 2, b = ((xcd >> 1) & 1) * 8 + (idx & 7), h0 = (xcd & 1) * 16 + (idx >> 3);
#define DIAGP args.ows
    QCtx q;
    q.lds = lds; q.ldsx = ldsb + w * 4096; q.ldsw = ldsb + XW_W + w * 4160; q.ldsx4 = ldsb + 16384 + w * 512; q.w = w;
    const unsigned ldsd = ldsb + ST_OFF + 768;
    q.xp = args.x[br] + (size_t)b * (SQ * DM * 2);
    q.wp = args.wq[br] + h0 * (DH * 2);
    if (STAGGER > 0 && blockIdx.x >= 256) {
        const unsigned long long t0 = __builtin_amdgcn_s_memtime();
        l2_touch(q.xp, q.wp - 4 * (DH * 2), idx & 7, (idx >> 3) & 3, w, ldsd);
        l2_touch(q.xp, q.wp, idx & 7, ((idx >> 3) & 3) + 4, w, ldsd);
        while (__builtin_amdgcn_s_memtime() - t0 < (unsigned long long)STAGGER) __builtin_amdgcn_s_sleep(8);
    }
    q_dma_setup(q);
    q_dma<8, 0>(q, 0); q_dma<0, 0>(q, 0); q_dma<1, 0>(q, 0); q_dma<2, 0>(q, 0); q_dma<3, 0>(q, 0); q_dma<4, 0>(q, 0); q_dma<5, 0>(q, 0); q_dma<6, 0>(q, 0); q_dma<7, 0>(q, 0);

    for (int it = 0; it < 2; ++it) {
        const int h = h0 + 8 * it;
        const bool more = (it == 0);
        STAMP(4 * it + 0);
        const int l = opaque_lane(), r = l & 15, g = l >> 4, q4 = r >> 2, p = l & 3, row8 = (l >> 2) & 7, hf = l >> 5;
        AttnCtx ac;
        ac.lds = lds; ac.ldsk = ldsb + KS0 + w * 4096; ac.ldsv = ldsb + VS0 + w * 4224;
        ac.kp = args.k[br] + (size_t)(b * NH + h) * (SK * DH * 2) + w * 4096;
        ac.vp = args.v[br] + (size_t)(b * NH + h) * (SK * DH * 2) + w * 4096;
#pragma unroll
        for (int j = 0; j < 2; ++j) ac.kvo[j] = row8 * 256 + hf * 64 + ((p ^ fsw((j << 1) | (row8 >> 2))) * 16);
        ac.vvo = q4 * 256 + g * 64 + p * 16;
        f32x4 qa[2][8], qx[2];
        {
            q_dma_setup(q);
            const unsigned xa = (r >> 3) * 1024 + (r & 7) * 64 + ((g ^ fsw((r >> 2) & 3)) * 16);
            q.xa_own = xa + w * 4096; q.xa_x = xa + 8 * 2048;
            q.wa = g * 2080 + q4 * 64 + p * 8; q.wax = q.wa + w * 256;
            SB();
            if (it == 0) WAIT_BAR(0); else WAIT_BAR(9);
            bf16x8 af[10], xb0[3];
            xfrags(q, 0, 0, xb0);
#pragma unroll
            for (int j = 0; j < 10; ++j) af[j] = wfrag(q, 0, 0, j);
            q_dma<0, STG>(q, 1); q_dma<1, STG>(q, 1); q_dma<2, STG>(q, 1); q_dma<3, STG>(q, 1); q_dma<4, STG>(q, 1); q_dma<5, STG>(q, 1); q_dma<6, STG>(q, 1); q_dma<7, STG>(q, 1); q_dma<8, STG>(q, 1);
            SB();
            q_chunk<0, 0, 3>(q, ac, 0, qa, qx, af, xb0);
            for (int c = 1; c < 7; c += 2) { q_chunk<1, 0, 0>(q, ac, c, qa, qx, af, xb0); q_chunk<0, 0, 0>(q, ac, c + 1, qa, qx, af, xb0); }
            q_chunk<1, 0, 0>(q, ac, 7, qa, qx, af, xb0);
            q_chunk<0, 1, 0>(q, ac, 8, qa, qx, af, xb0);
            q_chunk<1, 2, 1>(q, ac, 9, qa, qx, af, xb0);
            MFMA_PAD(); SB();
        }
        bf16x8 qf[3][4];
        *LDSP(bf16x8, (lds_ptr)lds + QX_OFF + w * 1024 + l * 16) = qfrag(qx[0], qx[1]);
#pragma unroll
        for (int ks = 0; ks < 4; ++ks) { qf[0][ks] = qfrag(qa[0][2 * ks], qa[0][2 * ks + 1]); qf[1][ks] = qfrag(qa[1][2 * ks], qa[1][2 * ks + 1]); }
#pragma unroll
        for (int i = 0; i < 4; ++i) {
            const unsigned kt = (unsigned)((i + w) & 3);
            ac.ka[i] = kt * 4096 + (r >> 3) * 2048 + (r & 7) * 64 + ((g ^ fsw((r >> 2) & 3)) * 16);
            ac.va[i] = (kt * 4 + g) * 1056 + q4 * 64 + p * 8;
        }
        SB();
        WAIT_BAR(4);
        bf16x8 kf[4];
#pragma unroll
        for (int n = 0; n < 4; ++n) kf[n] = kfrag(ac, 0, n);
#pragma unroll
        for (int ks = 0; ks < 4; ++ks) qf[2][ks] = ld128(lds + QX_OFF + ks * 1024 + l * 16);
        DMA_K(ac, STG, 1, 0); DMA_K(ac, STG, 1, 1); DMA_K(ac, STG, 1, 2); DMA_K(ac, STG, 1, 3);
        DMA_V(ac, STG, 1, 0); DMA_V(ac, STG, 1, 1); DMA_V(ac, STG, 1, 2); DMA_V(ac, STG, 1, 3);
        q.wp += 8 * (DH * 2);
        {
            if (more) l2_touch(q.xp, q.wp, idx & 7, idx >> 3, w, ldsd);
            else {
                const int i0 = ((xcd & 3) * 64 + idx) * 160 + w * 40 + (l < 20 ? l : 19);
                asm volatile("s_mov_b32 m0, %3\n\ts_nop 0\n\tglobal_load_lds_dword %0, %2\n\tglobal_load_lds_dword %1, %2"
                             :: "v"(i0 * 128), "v"(i0 * 128 + 2560), "s"(args.wo[br]), "s"(ldsd) : "memory");
            }
        }
        SB();
        STAMP(4 * it + 1);
        PRIO(1);
        f32x4 o[3][8], negm[3]; float lsum[3] = {0.f, 0.f, 0.f};
        fast_tile<0, true, false>(0, ac, qf, o, negm, lsum, kf, q, more);
        for (int t = 1; t < 7; t += 2) {
            fast_tile<1, false, false>(t, ac, qf, o, negm, lsum, kf, q, more);
            fast_tile<0, false, false>(t + 1, ac, qf, o, negm, lsum, kf, q, more);
        }
        q_dma_setup(q);
        SB();
        fast_tile<1, false, true>(7, ac, qf, o, negm, lsum, kf, q, more);
        MFMA_PAD(); SB();
        STAMP(4 * it + 2);
        float mref[3] = {-negm[0][0], -negm[1][0], -negm[2][0]};
        {
            const bool bad = !(lsum[0] < 4096.f) || !(lsum[1] < 4096.f) || !(lsum[2] < 4096.f);
            const unsigned anybad = __any(bad) ? 1u : 0u;
            if (l == 0) *LDSP(unsigned, (lds_ptr)lds + ST_OFF + 512 + w * 4) = anybad;
            if (g == 0) *LDSP(float, (lds_ptr)lds + ST_OFF + w * 64 + r * 4) = mref[2];
        }
        asm volatile("s_waitcnt lgkmcnt(0)\n\ts_barrier" ::: "memory");
        {
            const u32x4 fl = *LDSP(const u32x4, lds + ST_OFF + 512);
            if (__builtin_expect(rfl(fl[0] | fl[1] | fl[2] | fl[3]) != 0, 0)) {
#pragma unroll
                for (int a = 0; a < 3; ++a) {
                    mref[a] = -3.0e38f; lsum[a] = 0.f;
#pragma unroll
                    for (int dt = 0; dt < 8; ++dt) o[a][dt] = f32x4{0, 0, 0, 0};
                }
                for (int t = 0; t < 8; ++t) safe_tile(t, ac, qf, o, mref, lsum);
                if (g == 0) *LDSP(float, (lds_ptr)lds + ST_OFF + w * 64 + r * 4) = mref[2];
                asm volatile("s_waitcnt lgkmcnt(0)\n\ts_barrier" ::: "memory");
            }
        }
        float mo[4];
#pragma unroll
        for (int i = 0; i < 4; ++i) mo[i] = *LDSP(const float, lds + ST_OFF + i * 64 + r * 4);
        const float mall = fmaxf(fmaxf(mo[0], mo[1]), fmaxf(mo[2], mo[3]));
        const float fx = __builtin_amdgcn_exp2f(mref[2] - mall);
#pragma unroll
        for (int dt = 0; dt < 8; ++dt) *LDSP(f32x4, (lds_ptr)lds + STG + w * 8192 + dt * 1024 + l * 16) = o[2][dt] * fx;
        const float lx = xsum(lsum[2]) * fx;
        if (g == 0) *LDSP(float, (lds_ptr)lds + ST_OFF + 256 + w * 64 + r * 4) = lx;
        char* ob = args.ows + ((size_t)((br * NB + b) * 9) * NH + h) * 4096 + l * 16;
#pragma unroll
        for (int a = 0; a < 2; ++a) {
            const float il = __builtin_amdgcn_rcpf(xsum(lsum[a]));
#pragma unroll
            for (int s = 0; s < 4; ++s) {
                const f32x4 u = o[a][2 * s] * il, v = o[a][2 * s + 1] * il;
                const u32x4 pk = {cvtpk(u[0], u[1]), cvtpk(u[2], u[3]), cvtpk(v[0], v[1]), cvtpk(v[2], v[3])};
                ostore(ob + (size_t)(2 * w + a) * (NH * 4096) + s * 1024, pk);
            }
        }
        asm volatile("s_waitcnt lgkmcnt(0)\n\ts_barrier" ::: "memory");
        f32x4 u = {0, 0, 0, 0}, v = {0, 0, 0, 0}; float lt = 0.f;
#pragma unroll
        for (int i = 0; i < 4; ++i) {
            u += *LDSP(const f32x4, lds + STG + i * 8192 + (2 * w) * 1024 + l * 16);
            v += *LDSP(const f32x4, lds + STG + i * 8192 + (2 * w + 1) * 1024 + l * 16);
            lt += *LDSP(const float, lds + ST_OFF + 256 + i * 64 + r * 4);
        }
        const float il = __builtin_amdgcn_rcpf(lt);
        u *= il; v *= il;
        const u32x4 pk = {cvtpk(u[0], u[1]), cvtpk(u[2], u[3]), cvtpk(v[0], v[1]), cvtpk(v[2], v[3])};
        ostore(ob + (size_t)8 * (NH * 4096) + w * 1024, pk);
        SB();
        STAMP(4 * it + 3);
        PRIO(0);
    }
#undef DIAGP
}

constexpr int K2_Q = 8 * 1056, K2_STG = 4 * K2_Q, K2_LDS = 147456 + 512;
struct K2Args { const char* ows; const char* wo[2]; char* out; };
#define TOUCH 0
#define K2_TSET 2
#define K2_TAHEAD 2
#define WOPOL ""
struct K2Ctx { lds_cptr lds; unsigned ldsw, ldsd, wv[4], av, ba[2][4]; const char* wp; const char* ap; const char* tb; int w, part; };
DI void k2_touch(const K2Ctx& c, const int G) {
    if ((c.w & 3) == 3) return;
    const int j = (c.w & 3) * 64 + opaque_lane();
    unsigned off;
    if (c.w < 4) { int u = 6 * j + c.part; u = u < 1024 ? u : 1023; off = (unsigned)(((u >> 6) & 3) * 1024 + (4 * G + (u >> 8)) * 32 + ((u >> 1) & 31)) * (DM * 2) + (u & 1) * 128; }
    else { int v = 5 * j + c.part; v = v < 768 ? v : 767; off = (unsigned)(v >> 7) * (NH * 4096) + ((v >> 5) & 3) * 32768 + G * 4096 + (v & 31) * 128; }
    asm volatile("s_mov_b32 m0, %2\n\ts_nop 0\n\tglobal_load_lds_dword %0, %1" :: "v"(off), "s"(c.tb), "s"(c.ldsd) : "memory");
}
template <int SET, int J> DI void k2_dma(const K2Ctx& c, const int st) { DMAP(WOPOL, c.ldsw, SET * K2_STG + J * 1056, c.wv[J], c.wp + (size_t)st * (32 * DM * 2), 0); }
template <int I> DI void k2_lda(const K2Ctx& c, const int st, u32x4& d) {
    asm volatile("global_load_dwordx4 %0, %1, %2" : "=v"(d) : "v"(c.av), "s"(c.ap + st * 1024 + I * (NH * 4096)) : "memory");
}
template <int SET> DI bf16x8 k2_bfrag(const K2Ctx& c, const int n) {
    const unsigned b = c.ba[SET >> 1][n >> 1]; const int im = (SET & 1) * K2_STG + (n & 1) * 32;
    return cat(ldtr(c.lds + b + im), ldtr(c.lds + b + im + 4 * 1056));
}
template <int SET, int MODE>
DI void k2_step(const K2Ctx& c, const int st, f32x4 (&acc)[3][8], u32x4 (&af)[4][3], bf16x8 (&bw)[8]) {
    constexpr int SN = (SET + 1) & 3, SA = (SET + 3) & 3;
#pragma unroll
    for (int n = 0; n < 8; ++n) {
        MFMA32T(acc[0][n], bw[n], af[SET][0]); MFMA32T(acc[1][n], bw[n], af[SET][1]); MFMA32T(acc[2][n], bw[n], af[SET][2]);
        if (MODE != 4) {
            if (n == 2) { bw[0] = k2_bfrag<SN>(c, 0); bw[1] = k2_bfrag<SN>(c, 1); bw[2] = k2_bfrag<SN>(c, 2); }
            if (n > 2) bw[n] = k2_bfrag<SN>(c, n);
        }
        if (MODE == 0 && SET == K2_TSET && n == 2) { if (TOUCH && (st >> 2) + K2_TAHEAD < 8) k2_touch(c, (st >> 2) + K2_TAHEAD); }
        if (MODE <= 1) {
            if (n == 2) k2_lda<0>(c, st + 3, af[SA][0]);
            if (n == 4) k2_lda<1>(c, st + 3, af[SA][1]);
            if (n == 6) k2_lda<2>(c, st + 3, af[SA][2]);
        }
        if (MODE == 0) {
            if (n == 3) k2_dma<SET, 0>(c, st + 4);
            if (n == 5) k2_dma<SET, 1>(c, st + 4);
            if (n == 7) { k2_dma<SET, 2>(c, st + 4); k2_dma<SET, 3>(c, st + 4); }
        }
        SB();
        if (n == 1 && MODE != 4) {
            if (MODE <= 1) asm volatile("s_waitcnt vmcnt(7) lgkmcnt(0)\n\ts_barrier" : "+v"(af[SN][0]), "+v"(af[SN][1]), "+v"(af[SN][2]) :: "memory");
            else if (MODE == 2) asm volatile("s_waitcnt vmcnt(3) lgkmcnt(0)\n\ts_barrier" : "+v"(af[SN][0]), "+v"(af[SN][1]), "+v"(af[SN][2]) :: "memory");
            else asm volatile("s_waitcnt vmcnt(0) lgkmcnt(0)\n\ts_barrier" : "+v"(af[SN][0]), "+v"(af[SN][1]), "+v"(af[SN][2]) :: "memory");
            SB();
        }
    }
}

__global__ __launch_bounds__(512, 2) void k2_oproj(const K2Args args) {
    extern __shared__ __attribute__((aligned(16))) char smem[];
    const lds_cptr lds = (lds_cptr)smem;
    const unsigned ldsb = (unsigned)(uintptr_t)smem;
    const int tid = threadIdx.x, l = tid & 63, r = l & 15, g = l >> 4, q4 = r >> 2, p = l & 3;
    const int w = rfl(tid >> 6), kq = w & 3, mh = w >> 2;
    const int xcd = blockIdx.x & 7, idx = blockIdx.x >> 3;
    const int br = xcd >> 2, mt = (xcd & 3) * 6 + idx / 5, nt = idx % 5;
    K2Ctx c;
    c.lds = lds; c.ldsw = ldsb + kq * K2_Q + mh * 4224;
    c.wp = args.wo[br] + (size_t)(kq * 1024 + mh * 16) * (DM * 2) + nt * 256;
    c.ap = args.ows + ((size_t)(br * 144 + mt * 6 + mh * 3) * NH + kq * 8) * 4096;
#pragma unroll
    for (int j = 0; j < 4; ++j) {
        c.wv[j] = (4 * j + q4) * (DM * 2) + g * 64 + p * 16;
        c.ba[0][j] = kq * K2_Q + g * 1056 + ((kq + j) & 3) * 256 + q4 * 64 + p * 8; c.ba[1][j] = c.ba[0][j] + 2 * K2_STG;
    }
    c.av = l * 16;
    c.ldsd = ldsb + 4 * K2_STG; c.w = w; c.part = w < 4 ? idx / 5 : nt;
    c.tb = w < 4 ? args.wo[br] + nt * 256 : args.ows + (size_t)(br * 144 + mt * 6) * (NH * 4096);
    f32x4 acc[3][8]; u32x4 af[4][3]; bf16x8 bw[8];
#pragma unroll
    for (int i = 0; i < 3; ++i)
#pragma unroll
        for (int n = 0; n < 8; ++n) { acc[i][n] = f32x4{0, 0, 0, 0}; PIN(acc[i][n]); }
    k2_dma<0, 0>(c, 0); k2_dma<0, 1>(c, 0); k2_dma<0, 2>(c, 0); k2_dma<0, 3>(c, 0);
    k2_lda<0>(c, 0, af[0][0]); k2_lda<1>(c, 0, af[0][1]); k2_lda<2>(c, 0, af[0][2]);
    k2_dma<1, 0>(c, 1); k2_dma<1, 1>(c, 1); k2_dma<1, 2>(c, 1); k2_dma<1, 3>(c, 1);
    k2_lda<0>(c, 1, af[1][0]); k2_lda<1>(c, 1, af[1][1]); k2_lda<2>(c, 1, af[1][2]);
    k2_dma<2, 0>(c, 2); k2_dma<2, 1>(c, 2); k2_dma<2, 2>(c, 2); k2_dma<2, 3>(c, 2);
    k2_lda<0>(c, 2, af[2][0]); k2_lda<1>(c, 2, af[2][1]); k2_lda<2>(c, 2, af[2][2]);
    k2_dma<3, 0>(c, 3); k2_dma<3, 1>(c, 3); k2_dma<3, 2>(c, 3); k2_dma<3, 3>(c, 3);
    if (TOUCH) k2_touch(c, 1);
    asm volatile("s_waitcnt vmcnt(18)\n\ts_barrier" : "+v"(af[0][0]), "+v"(af[0][1]), "+v"(af[0][2]) :: "memory");
#pragma unroll
    for (int n = 0; n < 8; ++n) bw[n] = k2_bfrag<0>(c, n);
    SB();
    for (int st = 0; st < 28; st += 4) {
        k2_step<0, 0>(c, st, acc, af, bw); k2_step<1, 0>(c, st + 1, acc, af, bw); k2_step<2, 0>(c, st + 2, acc, af, bw); k2_step<3, 0>(c, st + 3, acc, af, bw);
    }
    k2_step<0, 1>(c, 28, acc, af, bw); k2_step<1, 2>(c, 29, acc, af, bw); k2_step<2, 3>(c, 30, acc, af, bw); k2_step<3, 4>(c, 31, acc, af, bw);
    MFMA_PAD();
    asm volatile("s_waitcnt lgkmcnt(0)\n\ts_barrier" ::: "memory");
    SB();
#pragma unroll
    for (int d = 1; d < 4; ++d)
#pragma unroll
        for (int i = 0; i < 3; ++i)
#pragma unroll
            for (int a = 0; a < 2; ++a)
                *LDSP(f32x4, (lds_ptr)lds + (mh * 4 + ((kq + d) & 3)) * 18432 + ((d - 1) * 6144 + (i * 2 + a) * 1024) + l * 16) = acc[i][2 * d + a];
    asm volatile("s_waitcnt lgkmcnt(0)\n\ts_barrier" ::: "memory");
#pragma unroll
    for (int d = 0; d < 3; ++d)
#pragma unroll
        for (int i = 0; i < 3; ++i)
#pragma unroll
            for (int a = 0; a < 2; ++a) acc[i][a] += *LDSP(const f32x4, lds + w * 18432 + (d * 6144 + (i * 2 + a) * 1024) + l * 16);
#pragma unroll
    for (int i = 0; i < 3; ++i) {
        unsigned a0 = cvtpk(acc[i][0][0], acc[i][0][1]), a1 = cvtpk(acc[i][0][2], acc[i][0][3]);
        unsigned b0 = cvtpk(acc[i][1][0], acc[i][1][1]), b1 = cvtpk(acc[i][1][2], acc[i][1][3]);
        auto r0 = __builtin_amdgcn_permlane32_swap(a0, b0, false, false);
        auto r1 = __builtin_amdgcn_permlane32_swap(a1, b1, false, false);
        auto s0 = __builtin_amdgcn_permlane16_swap(r0[0], r0[1], false, false);
        auto s1 = __builtin_amdgcn_permlane16_swap(r1[0], r1[1], false, false);
        const u32x4 pk = {s0[0], s1[0], s0[1], s1[1]};
        const int row = (mt * 6 + mh * 3 + i) * 16 + r;
        *(u32x4*)(args.out + (size_t)row * (2 * DM * 2) + br * (DM * 2) + nt * 256 + kq * 64 + g * 16) = pk;
    }
}

extern "C" void kernel_launch(void* const* d_in, const int* in_sizes, int n_in, void* d_out, int out_size, void* d_ws, size_t ws_size,
                              hipStream_t stream) {
    (void)in_sizes; (void)n_in; (void)out_size; (void)ws_size;
    (void)hipFuncSetAttribute((const void*)k1_qattn, hipFuncAttributeMaxDynamicSharedMemorySize, K1_LDS);
    (void)hipFuncSetAttribute((const void*)k2_oproj, hipFuncAttributeMaxDynamicSharedMemorySize, K2_LDS);
    K1Args a1;
    a1.x[0] = (const char*)d_in[0]; a1.k[0] = (const char*)d_in[1]; a1.v[0] = (const char*)d_in[2]; a1.wq[0] = (const char*)d_in[3];
    a1.x[1] = (const char*)d_in[5]; a1.k[1] = (const char*)d_in[6]; a1.v[1] = (const char*)d_in[7]; a1.wq[1] = (const char*)d_in[8];
    a1.wo[0] = (const char*)d_in[4]; a1.wo[1] = (const char*)d_in[9]; a1.ows = (char*)d_ws;
    K2Args a2; a2.ows = (const char*)d_ws; a2.wo[0] = (const char*)d_in[4]; a2.wo[1] = (const char*)d_in[9]; a2.out = (char*)d_out;
    hipLaunchKernelGGL(k1_qattn, dim3(512), dim3(256), K1_LDS, stream, a1);
    hipLaunchKernelGGL(k2_oproj, dim3(240), dim3(512), K2_LDS, stream, a2);
}
```

```cpp
#include <hip/hip_runtime.h>
#include <hip/hip_bf16.h>

typedef short bf16x8 __attribute__((ext_vector_type(8)));
typedef short bf16x4 __attribute__((ext_vector_type(4)));
typedef float f32x4 __attribute__((ext_vector_type(4)));
typedef float f32x2 __attribute__((ext_vector_type(2)));
typedef __bf16 bf2_t __attribute__((ext_vector_type(2)));
typedef unsigned u32x4 __attribute__((ext_vector_type(4)));
typedef unsigned u32x2 __attribute__((ext_vector_type(2)));
typedef __attribute__((address_space(3))) char* lds_ptr;
typedef __attribute__((address_space(3))) const char* lds_cptr;
#define DI __device__ __forceinline__
#define LDSP(T, p) ((__attribute__((address_space(3))) T*)(p))

constexpr int NB = 16, SQ = 144, DM = 640, NH = 32, SK = 512, DH = 128, HD = NH * DH;
constexpr float QSCALE = 0.12751743082459868f;

#define MFMA32(a, b, c) __builtin_amdgcn_mfma_f32_16x16x32_bf16((a), (b), (c), 0, 0, 0)
#define MFMA16(a, b, c) __builtin_amdgcn_mfma_f32_16x16x16bf16_1k((a), (b), (c), 0, 0, 0)

#define MFMA32T(acc, a, b) asm volatile("v_mfma_f32_16x16x32_bf16 %0, %1, %2, %0" : "+v"(acc) : "v"(a), "v"(b))
#define MFMA32Z(acc, a, b) asm volatile("v_mfma_f32_16x16x32_bf16 %0, %1, %2, 0" : "=&v"(acc) : "v"(a), "v"(b))
#define MFMA16T(acc, a, b) asm volatile("v_mfma_f32_16x16x16_bf16 %0, %1, %2, %0" : "+v"(acc) : "v"(a), "v"(b))
#define MFMA16Z(acc, a, b) asm volatile("v_mfma_f32_16x16x16_bf16 %0, %1, %2, 0" : "=&v"(acc) : "v"(a), "v"(b))
#define MFMA_PAD() asm volatile("s_nop 7\n\ts_nop 3" ::: "memory")
DI unsigned cvtpk(float lo, float hi) { f32x2 v = {lo, hi}; bf2_t b = __builtin_convertvector(v, bf2_t); return __builtin_bit_cast(unsigned, b); }
DI bf16x8 ld128(lds_cptr p) { return *LDSP(const bf16x8, p); }
DI bf16x4 ldtr(lds_cptr p) { return __builtin_amdgcn_ds_read_tr16_b64_v4i16(LDSP(bf16x4, (lds_ptr)p)); }
DI bf16x8 cat(bf16x4 lo, bf16x4 hi) { return __builtin_shufflevector(lo, hi, 0, 1, 2, 3, 4, 5, 6, 7); }
DI int fsw(int u) { return (((u ^ (u >> 1)) & 1) << 1) | ((u >> 1) & 1); }
DI unsigned rfl(unsigned x) { return __builtin_amdgcn_readfirstlane(x); }

#define DIAG_LDS 160768
#define WAIT_BAR(N) asm volatile("s_waitcnt vmcnt(" #N ") lgkmcnt(0)\n\ts_barrier" ::: "memory")
#define WAIT_BARL(N, L) asm volatile("s_waitcnt vmcnt(" #N ") lgkmcnt(" #L ")\n\ts_barrier" ::: "memory")
#define STAMP(i)
constexpr int RG = 16896, HB = 4 * RG, KE = 0, KO = RG, VE = 2 * RG, VO = 3 * RG;
constexpr int X0 = 2 * HB;
constexpr int X1 = HB + VO;
constexpr int X1X = X0 + 18432;
constexpr int PXO = X1X + 2048;
constexpr int STO = PXO + 4096;
constexpr int K1_LDS = STO + 1024;
#define SB() __builtin_amdgcn_sched_barrier(0)
#define PPONG 1
#define QPRIO 1
#define APRIO 1
#define PRIO(x) __builtin_amdgcn_s_setprio(x)
#define PIN(x) asm volatile("" : "+v"(x))
#define DMAP(POL, m0b, M0OFF, vo, sb, IMM) \
    asm volatile("s_add_u32 m0, %0, %4\n\ts_nop 0\n\tglobal_load_lds_dwordx4 %1, %2 offset:%3" POL :: "s"(m0b), "v"(vo), "s"(sb), "i"(IMM), "i"(M0OFF) : "memory", "scc")
#define QPOL ""

struct K1Args { const char* x[2]; const char* k[2]; const char* v[2]; const char* wq[2]; const char* wo[2]; char* ows; };

struct AttnCtx {
    lds_cptr lds; unsigned ldk, ldv;
    unsigned ka[4], va[4], vax, pxr, pxw, kvo[2], vvo;
    const char* kp; const char* vp; const char* knext;
};
#define KVPOL " nt"
template <int SO, int J> DI void dma_k(const AttnCtx& c, const char* kt) { DMAP(KVPOL, c.ldk, SO + (J & 1) * 896, c.kvo[J >> 1], kt, (J >> 1) * 2048 + (J & 1) * 128); }
template <int SO, int J> DI void dma_v(const AttnCtx& c, const char* vt) { DMAP(KVPOL, c.ldv, SO + J * 32, c.vvo, vt, J * 1024); }
#define DMA_K(c, SO, t, j) do { switch (j) { case 0: dma_k<SO, 0>(c, t); break; case 1: dma_k<SO, 1>(c, t); break; case 2: dma_k<SO, 2>(c, t); break; default: dma_k<SO, 3>(c, t); break; } } while (0)
#define DMA_V(c, SO, t, j) do { switch (j) { case 0: dma_v<SO, 0>(c, t); break; case 1: dma_v<SO, 1>(c, t); break; case 2: dma_v<SO, 2>(c, t); break; default: dma_v<SO, 3>(c, t); break; } } while (0)
DI bf16x8 kfrag(const AttnCtx& c, int so, int n) { return ld128(c.lds + c.ka[n >> 2] + (so + (n & 3) * 512)); }
DI bf16x4 vtr(const AttnCtx& c, int so, int i, int dt) { return ldtr(c.lds + c.va[i] + (so + (dt >> 1) * 256 + (dt & 1) * 32)); }
DI bf16x4 vxtr(const AttnCtx& c, int so, int kt, int d) { return ldtr(c.lds + c.vax + (so + kt * 4224 + d * 32)); }
DI float xmax(float m) {
    auto a = __builtin_amdgcn_permlane32_swap(__float_as_uint(m), __float_as_uint(m), false, false);
    m = fmaxf(__uint_as_float(a[0]), __uint_as_float(a[1]));
    auto b = __builtin_amdgcn_permlane16_swap(__float_as_uint(m), __float_as_uint(m), false, false);
    return fmaxf(__uint_as_float(b[0]), __uint_as_float(b[1]));
}
DI float xsum(float m) {
    auto a = __builtin_amdgcn_permlane32_swap(__float_as_uint(m), __float_as_uint(m), false, false);
    m = __uint_as_float(a[0]) + __uint_as_float(a[1]);
    auto b = __builtin_amdgcn_permlane16_swap(__float_as_uint(m), __float_as_uint(m), false, false);
    return __uint_as_float(b[0]) + __uint_as_float(b[1]);
}
DI float max4(const f32x4 v) { return fmaxf(fmaxf(v[0], v[1]), fmaxf(v[2], v[3])); }
#define EXP4(v) do { v[0] = __builtin_amdgcn_exp2f(v[0]); v[1] = __builtin_amdgcn_exp2f(v[1]); v[2] = __builtin_amdgcn_exp2f(v[2]); v[3] = __builtin_amdgcn_exp2f(v[3]); PIN(v); } while (0)
#define EXP2(v, h) do { v[2 * (h)] = __builtin_amdgcn_exp2f(v[2 * (h)]); v[2 * (h) + 1] = __builtin_amdgcn_exp2f(v[2 * (h) + 1]); PIN(v); } while (0)
#define SUM4(v, ls) do { ls += v[0]; ls += v[1]; ls += v[2]; ls += v[3]; PIN(ls); } while (0)
#define PK4(v, d) do { d = u32x2{cvtpk(v[0], v[1]), cvtpk(v[2], v[3])}; PIN(d); } while (0)
#define SUMPK(v, ls, d) do { ls += v[0]; ls += v[1]; ls += v[2]; ls += v[3]; d = u32x2{cvtpk(v[0], v[1]), cvtpk(v[2], v[3])}; PIN(ls); PIN(d); } while (0)

struct QCtx {
    lds_cptr lds; unsigned ldx, ldxx, ldw; int w;
    unsigned xo[3], wv, xa_own, xa_x, wa, wax;
    unsigned wk, xk;
    const char* xp; const char* wp;
};
constexpr int wslot(int sl) { return sl < 2 ? VE + sl * 8320 : KO + (sl - 2) * 8320; }
template <int SL, int J> DI void w_dma(QCtx& q) {
    DMAP(QPOL, q.ldw, wslot(SL) + J * 1040, q.wv, q.wp + q.wk + J * (4 * HD * 2), 0);
    if (J == 1) { q.wk += 32 * HD * 2; if (q.wk >= DM * HD * 2) q.wk = 0; }
}
template <int ST, int J> DI void x_dma(QCtx& q) {
    if (J < 2) DMAP(QPOL, q.ldx, (ST ? 0 : X0 - X1) + J * 1024, q.xo[J], q.xp + q.xk, 0);
    else {
        asm volatile("s_add_u32 m0, %0, %3\n\ts_nop 0\n\tglobal_load_lds_dword %1, %2" QPOL :: "s"(q.ldxx), "v"(q.xo[2]), "s"(q.xp + q.xk), "i"(ST * 2048) : "memory", "scc");
        q.xk += 128; if (q.xk >= DM * 2) q.xk = 0;
    }
}
DI int opaque_lane() { int l; asm volatile("v_mbcnt_lo_u32_b32 %0, -1, 0\n\tv_mbcnt_hi_u32_b32 %0, -1, %0" : "=v"(l)); return l; }
DI void x_setup(QCtx& q) {
    const int l = opaque_lane(), p = l & 3, row8 = (l >> 2) & 7, hf = l >> 5, w = q.w;
#pragma unroll
    for (int j = 0; j < 2; ++j) q.xo[j] = (16 * w + 8 * j + row8) * (DM * 2) + hf * 64 + ((p ^ fsw((j << 1) | (row8 >> 2))) * 16);
    q.xo[2] = (128 + 8 * (w >> 2) + 4 * (w & 1) + (l >> 4)) * (DM * 2) + ((w >> 1) & 1) * 64 + (((row8 & 3) ^ fsw(2 * (w >> 2) + (w & 1))) * 16) + p * 4;
}
DI void w_setup(QCtx& q) {
    const int l = opaque_lane();
    q.wv = (8 * (q.w & 3) + ((l & 15) >> 2)) * (HD * 2) + (l >> 4) * 64 + (l & 3) * 16;
}
template <bool NT> DI void touch(const char* p, const unsigned off, const unsigned ldsd) {
    if (NT) asm volatile("s_mov_b32 m0, %2\n\ts_nop 0\n\tglobal_load_lds_dword %0, %1 nt" :: "v"(off), "s"(p), "s"(ldsd) : "memory");
    else asm volatile("s_mov_b32 m0, %2\n\ts_nop 0\n\tglobal_load_lds_dword %0, %1" :: "v"(off), "s"(p), "s"(ldsd) : "memory");
}
struct Seam { unsigned ldsd, to[2], kl; const char* tb[2]; };

template <int SLOT, bool FIRST, bool LAST>
DI void fast_tile(const int t, const AttnCtx& c, const bf16x8 (&qf)[3][4], f32x4 (&o)[2][8], f32x4 (&ox)[2], f32x4 (&negm)[3], float (&lsum)[3], bf16x8 (&kf)[4],
                  QCtx& nq, const Seam& sm) {
    constexpr int SO = SLOT * RG, SN = (SLOT ^ 1) * RG;
    const f32x4 z4 = {0.f, 0.f, 0.f, 0.f};
    f32x4 s[2][4], sx, sr; u32x2 ph[2][4], px; bf16x4 vf[5][2]; bf16x8 pxf[2], kr[4]; bf16x4 vx[2][2];
    if (FIRST) {
#pragma unroll
        for (int ks = 0; ks < 4; ++ks) kr[ks] = ld128(c.lds + sm.kl + ks * 512);
    }
    const char* kn = (SLOT == 0 && !FIRST && t == 6) ? c.knext : c.kp + (t + 2) * 16384;
    const char* vn = c.vp + (t + 1) * 16384;
#pragma unroll
    for (int n = 0; n < 16; ++n) {
        const int i = n >> 2, ks = n & 3;
        s[0][i] = MFMA32(kf[ks], qf[0][ks], ks ? s[0][i] : ((FIRST && i == 0) ? z4 : negm[0]));
        if (n == 4) EXP2(sx, 0);
        if (n == 5) EXP2(s[0][0], 0);
        if (n == 6) EXP2(s[1][0], 0);
        if (n == 7) SUM4(sx, lsum[2]);
        if (n == 8) SUM4(s[0][0], lsum[0]);
        if (n == 9) EXP2(s[0][1], 0);
        if (n == 10) SUM4(s[1][0], lsum[1]);
        if (n == 11) EXP2(s[1][1], 0);
        if (n == 12) SUM4(s[0][1], lsum[0]);
        if (n == 13) SUM4(s[1][1], lsum[1]);
        if (n >= 14) { vf[n - 14][0] = vtr(c, SO, 0, n - 14); vf[n - 14][1] = vtr(c, SO, 1, n - 14); }
        SB();
        s[1][i] = MFMA32(kf[ks], qf[1][ks], ks ? s[1][i] : ((FIRST && i == 0) ? z4 : negm[1]));
        if (i == 0) { SB(); sx = MFMA32(kf[ks], qf[2][ks], ks ? sx : (FIRST ? z4 : negm[2])); if (FIRST) sr = MFMA32(kr[ks], qf[2][ks], ks ? sr : z4); }
        if (n < 12) kf[ks] = kfrag(c, SO, n + 4);
        if (n == 4) EXP2(sx, 1);
        if (n == 5) EXP2(s[0][0], 1);
        if (n == 6) EXP2(s[1][0], 1);
        if (n == 7) { PK4(sx, px); *LDSP(u32x2, (lds_ptr)c.lds + c.pxw) = px; }
        if (n == 8) PK4(s[0][0], ph[0][0]);
        if (n == 9) EXP2(s[0][1], 1);
        if (n == 10) PK4(s[1][0], ph[1][0]);
        if (n == 11) EXP2(s[1][1], 1);
        if (n == 12) PK4(s[0][1], ph[0][1]);
        if (n == 13) PK4(s[1][1], ph[1][1]);
        if (n == 14) { EXP2(s[0][2], 0); vf[2][0] = vtr(c, SO, 0, 2); }
        if (n == 15) { EXP2(s[0][2], 1); vf[2][1] = vtr(c, SO, 1, 2); }
        if (LAST && n == 12) *LDSP(float, (lds_ptr)c.lds + STO + nq.w * 64 + ((c.pxr >> 2) & 60)) = xsum(lsum[2]);
        if (FIRST && n < 2) touch<true>(sm.tb[n], sm.to[n], sm.ldsd);
        if (!FIRST && !LAST && n < 4) DMA_V(c, SN, vn, n);
        if (LAST) {
            if (n == 0) w_dma<0, 0>(nq); if (n == 1) w_dma<0, 1>(nq); if (n == 2) x_dma<0, 0>(nq); if (n == 3) x_dma<0, 1>(nq);
            if (n == 7) x_dma<0, 2>(nq); if (n == 8) w_dma<1, 0>(nq); if (n == 10) w_dma<1, 1>(nq);
        }
        if (APRIO) { if (n == 1) PRIO(2); if (n == 5) PRIO(1); if (n == 9) PRIO(0); }
        SB();
        if (FIRST && n == 3) {
            const float m0 = xmax(max4(s[0][0])), m1 = xmax(max4(s[1][0])), m2 = xmax(max4(sr));
            negm[0] = f32x4{-m0, -m0, -m0, -m0}; negm[1] = f32x4{-m1, -m1, -m1, -m1}; negm[2] = f32x4{-m2, -m2, -m2, -m2};
            s[0][0] -= m0; s[1][0] -= m1; sx -= m2;
            SB();
        }
        if (n == 13) { if (FIRST) WAIT_BAR(10); else if (LAST) WAIT_BAR(11); else WAIT_BAR(8); if (APRIO) PRIO(3); SB(); }
    }
#pragma unroll
    for (int m = 0; m < 16; ++m) {
        const int pr = m >> 3, dt = m & 7, mm = m + 4;
        const bf16x8 a = cat(vf[m % 5][0], vf[m % 5][1]);
        const u32x4 p0 = __builtin_shufflevector(ph[0][2 * pr], ph[0][2 * pr + 1], 0, 1, 2, 3), p1 = __builtin_shufflevector(ph[1][2 * pr], ph[1][2 * pr + 1], 0, 1, 2, 3);
        if (FIRST && pr == 0) MFMA32Z(o[0][dt], a, p0); else MFMA32T(o[0][dt], a, p0);
        if (m == 0) vf[3][0] = vtr(c, SO, 0, 3);
        if (m < 12) vf[mm % 5][0] = vtr(c, SO, 2 * (mm >> 3), mm & 7);
        if (m == 0) EXP2(s[1][2], 0);
        if (m == 1) EXP2(s[0][3], 0);
        if (m == 2) EXP2(s[1][3], 0);
        if (m == 3) SUM4(s[0][2], lsum[0]);
        if (m == 4) SUM4(s[1][2], lsum[1]);
        if (m == 5) PK4(s[0][3], ph[0][3]);
        if (m == 6) PK4(s[1][3], ph[1][3]);
        if (m == 7 || m == 8) vx[m - 7][0] = vxtr(c, SO, 0, m - 7);
        if (m == 10 || m == 11) vx[m - 10][0] = vxtr(c, SO, 2, m - 10);
        if (!LAST && m >= 14) kf[2 * (m - 14)] = kfrag(c, SN, 2 * (m - 14));
        SB();
        if (FIRST && pr == 0) MFMA32Z(o[1][dt], a, p1); else MFMA32T(o[1][dt], a, p1);
        if (m == 0) vf[3][1] = vtr(c, SO, 1, 3);
        if (m < 12) vf[mm % 5][1] = vtr(c, SO, 2 * (mm >> 3) + 1, mm & 7);
        if (m == 0) EXP2(s[1][2], 1);
        if (m == 1) EXP2(s[0][3], 1);
        if (m == 2) EXP2(s[1][3], 1);
        if (m == 3) PK4(s[0][2], ph[0][2]);
        if (m == 4) PK4(s[1][2], ph[1][2]);
        if (m == 5) SUM4(s[0][3], lsum[0]);
        if (m == 6) SUM4(s[1][3], lsum[1]);
        if (m == 5 || m == 6) pxf[m - 5] = ld128(c.lds + c.pxr + (m - 5) * 1024);
        if (m == 7 || m == 8) vx[m - 7][1] = vxtr(c, SO, 1, m - 7);
        if (m == 10 || m == 11) vx[m - 10][1] = vxtr(c, SO, 3, m - 10);
        if (!LAST && m >= 14) kf[2 * (m - 14) + 1] = kfrag(c, SN, 2 * (m - 14) + 1);
        if (m == 9 || m == 10) { SB(); if (FIRST) MFMA32Z(ox[m - 9], cat(vx[m - 9][0], vx[m - 9][1]), pxf[0]); else MFMA32T(ox[m - 9], cat(vx[m - 9][0], vx[m - 9][1]), pxf[0]); }
        if (m == 13 || m == 14) { SB(); MFMA32T(ox[m - 13], cat(vx[m - 13][0], vx[m - 13][1]), pxf[1]); }
        if (!LAST && (m == 7 || m == 9 || m == 12 || m == 13)) DMA_K(c, SO, kn, m == 7 ? 0 : (m == 9 ? 1 : m - 10));
        if (LAST) {
            if (m == 7) w_dma<2, 0>(nq); if (m == 9) w_dma<2, 1>(nq);
            if (m == 11 || m == 12) touch<false>(sm.tb[0], sm.to[m - 11], sm.ldsd);
            if (m == 14) x_dma<1, 0>(nq); if (m == 15) { x_dma<1, 1>(nq); x_dma<1, 2>(nq); }
        }
        if (APRIO) { if (m == 1) PRIO(2); if (m == 5) PRIO(1); if (m == 9) PRIO(0); }
        SB();
        if (m == 13) { if (FIRST) WAIT_BAR(10); else if (LAST) WAIT_BAR(6); else WAIT_BAR(8); if (APRIO) PRIO(3); SB(); }
    }
}

#define OPOL " sc1"
DI void ostore(char* p, const u32x4 v) { asm volatile("global_store_dwordx4 %0, %1, off" OPOL "\n\ts_nop 1" :: "v"(p), "v"(v) : "memory"); }
struct Pend { u32x4 pk[9]; char* ob[3]; unsigned lo; };
template <int I> DI void pstore(const Pend& p) { asm volatile("global_store_dwordx4 %0, %1, %2 offset:%3" OPOL "\n\ts_nop 1" :: "v"(p.lo), "v"(p.pk[I]), "s"(p.ob[I >> 2]), "i"((I & 3) * 1024) : "memory"); }
DI u32x4 opack(const f32x4 u, const f32x4 v, const float il) {
    return u32x4{cvtpk(u[0] * il, u[1] * il), cvtpk(u[2] * il, u[3] * il), cvtpk(v[0] * il, v[1] * il), cvtpk(v[2] * il, v[3] * il)};
}
DI void slow_frag(const bf16x8 (&tq)[4], const char* kp, const char* vp, char* dst) {
    const int l = opaque_lane(), r = l & 15, g = l >> 4;
    float m = -3.0e38f, ls = 0.f; f32x4 acc[2] = {{0, 0, 0, 0}, {0, 0, 0, 0}};
#pragma unroll 1
    for (int kt = 0; kt < 32; ++kt) {
        f32x4 s = {0, 0, 0, 0};
#pragma unroll
        for (int ks = 0; ks < 4; ++ks) s = MFMA32(*(const bf16x8*)(kp + (kt * 16 + r) * 256 + ks * 64 + g * 16), tq[ks], s);
        const float mn = fmaxf(m, xmax(max4(s))), f = __builtin_amdgcn_exp2f(m - mn);
        m = mn; s -= mn; ls *= f; u32x2 pk;
        EXP4(s); SUMPK(s, ls, pk);
#pragma unroll
        for (int d = 0; d < 2; ++d) {
            const short* vq = (const short*)(vp + (kt * 16 + 4 * g) * 256 + (d * 16 + r) * 2);
            const bf16x4 a = {vq[0], vq[128], vq[256], vq[384]};
            acc[d] = MFMA16(a, __builtin_bit_cast(bf16x4, pk), acc[d] * f);
        }
    }
    ostore(dst + l * 16, opack(acc[0], acc[1], __builtin_amdgcn_rcpf(xsum(ls))));
}

DI bf16x8 qfrag(const f32x4 t0, const f32x4 t1) {
    unsigned a0 = cvtpk(t0[0] * QSCALE, t0[1] * QSCALE), a1 = cvtpk(t0[2] * QSCALE, t0[3] * QSCALE);
    unsigned b0 = cvtpk(t1[0] * QSCALE, t1[1] * QSCALE), b1 = cvtpk(t1[2] * QSCALE, t1[3] * QSCALE);
    auto r0 = __builtin_amdgcn_permlane32_swap(a0, b0, false, false);
    auto r1 = __builtin_amdgcn_permlane32_swap(a1, b1, false, false);
    auto s0 = __builtin_amdgcn_permlane16_swap(r0[0], r0[1], false, false);
    auto s1 = __builtin_amdgcn_permlane16_swap(r1[0], r1[1], false, false);
    u32x4 u = {s0[0], s1[0], s0[1], s1[1]};
    return __builtin_bit_cast(bf16x8, u);
}

DI bf16x4 wtr(const QCtx& q, const int sl, const int j, const int hi) {
    const int im = wslot(sl) + ((j == 0 || j == 9) ? (j & 1) * 32 : ((j - 1) >> 1) * 256 + ((j - 1) & 1) * 32) + hi * 1040;
    return ldtr(q.lds + ((j == 0 || j == 9) ? q.wax : q.wa) + im);
}
DI bf16x8 xfrag(const QCtx& q, const int sl, const int i) {
    const int so = ((sl >> 1) ? 0 : X0 - X1) + (sl & 1) * 512;
    return i < 2 ? ld128(q.lds + q.xa_own + (so + i * 2048)) : ld128(q.lds + q.xa_x + ((sl >> 1) * 2048 + (sl & 1) * 512));
}
DI void q_first(const QCtx& q, bf16x4 (&af)[10][2], bf16x8 (&xb)[3]) {
    xb[2] = xfrag(q, 0, 2);
#pragma unroll
    for (int j = 0; j < 10; ++j) { const int jj = j == 0 ? 0 : (j == 1 ? 9 : j - 1); if (jj != 8) { af[jj][0] = wtr(q, 0, jj, 0); af[jj][1] = wtr(q, 0, jj, 1); } if (j == 1) { xb[0] = xfrag(q, 0, 0); xb[1] = xfrag(q, 0, 1); } }
}
template <int S>
DI void q_step(QCtx& q, const AttnCtx& ac, const Pend& pd, f32x4 (&qa)[2][8], f32x4 (&qx)[2], bf16x4 (&af)[10][2], const bf16x8 (&xc)[3], bf16x8 (&xn)[3],
               bf16x8 (&qf)[3][4], bf16x8 (&kf)[4], const unsigned qxa) {
    constexpr int SL = S & 3, SN = (SL + 1) & 3, SP = (SL + 3) & 3, BJ = S == 19 ? 3 : 1;
#pragma unroll
    for (int j = 0; j < 9; ++j) {
        const bf16x8 a0 = cat(af[j][0], af[j][1]);
        if (j > 0) { if (S == 0) MFMA32Z(qa[0][j - 1], a0, xc[0]); else MFMA32T(qa[0][j - 1], a0, xc[0]); }
        else { if (S == 0) MFMA32Z(qx[0], a0, xc[2]); else MFMA32T(qx[0], a0, xc[2]); }
        if (j == 0) af[8][0] = wtr(q, SL, 8, 0);
        if (S < 19) {
            if (j == 2) { af[0][0] = wtr(q, SN, 0, 0); af[0][1] = wtr(q, SN, 0, 1); }
            if (j == 3) { af[1][0] = wtr(q, SN, 1, 0); af[1][1] = wtr(q, SN, 1, 1); }
            if (j == 4) { af[2][0] = wtr(q, SN, 2, 0); af[2][1] = wtr(q, SN, 2, 1); }
            if (j == 5) { af[3][0] = wtr(q, SN, 3, 0); af[3][1] = wtr(q, SN, 3, 1); }
            if (j == 6) { af[4][0] = wtr(q, SN, 4, 0); af[4][1] = wtr(q, SN, 4, 1); }
            if (j == 7) { af[6][0] = wtr(q, SN, 6, 0); }
            if (j == 8) { af[7][0] = wtr(q, SN, 7, 0); }
        } else {
            if (j == 2) *LDSP(bf16x8, (lds_ptr)q.lds + qxa + q.w * 1024) = qfrag(qx[0], qx[1]);
            if (j == 4 || j == 6 || j == 8) qf[0][(j - 4) >> 1] = qfrag(qa[0][j - 4], qa[0][j - 3]);
            if (j >= 4 && j < 8) qf[2][j - 4] = ld128(q.lds + qxa + (q.w >> 2) * 4096 + (j - 4) * 1024);
        }
        SB();
        if (j > 0) { if (S == 0) MFMA32Z(qa[1][j - 1], a0, xc[1]); else MFMA32T(qa[1][j - 1], a0, xc[1]); }
        else { const bf16x8 a1 = cat(af[9][0], af[9][1]); if (S == 0) MFMA32Z(qx[1], a1, xc[2]); else MFMA32T(qx[1], a1, xc[2]); }
        if (j == 0) af[8][1] = wtr(q, SL, 8, 1);
        if (S < 19) {
            if (j == 2) { af[9][0] = wtr(q, SN, 9, 0); af[9][1] = wtr(q, SN, 9, 1); }
            if (j == 3) xn[2] = xfrag(q, SN, 2);
            if (j == 4 || j == 5) xn[j - 4] = xfrag(q, SN, j - 4);
            if (j == 6) { af[5][0] = wtr(q, SN, 5, 0); af[5][1] = wtr(q, SN, 5, 1); }
            if (j == 7) { af[6][1] = wtr(q, SN, 6, 1); }
            if (j == 8) { af[7][1] = wtr(q, SN, 7, 1); }
        } else {
            if (j == 4 || j == 6 || j == 8) qf[1][(j - 4) >> 1] = qfrag(qa[1][j - 4], qa[1][j - 3]);
            if (j >= 5) kf[j - 5] = kfrag(ac, 0, j - 5);
        }
        if (S < 17) {
            if (SL & 1) {
                if (j == 3) w_dma<SP, 0>(q);
                if (j == 4) w_dma<SP, 1>(q);
                if (j == 5) x_dma<(SL >> 1), 0>(q);
                if (j == 7) x_dma<(SL >> 1), 1>(q);
                if (j == 8) x_dma<(SL >> 1), 2>(q);
            } else {
                if (j == 3) w_dma<SP, 0>(q);
                if (j == 5) w_dma<SP, 1>(q);
                if (j == 7) pstore<(S >> 1)>(pd);
            }
        }
        if (S == 17 && j >= 2 && (j & 1) == 0) DMA_V(ac, 0, ac.vp, (j - 2) >> 1);
        if (S == 19 && (j == 5 || j == 7)) { DMA_K(ac, KO, ac.kp + 16384, j - 5); DMA_K(ac, KO, ac.kp + 16384, j - 4); }
        if (QPRIO) { if (j == 3) PRIO(2); if (j == 5) PRIO(1); if (j == 7) PRIO(0); }
        SB();
        if (j == BJ) {
            if (S == 0) WAIT_BARL(7, 15);
            else if (S == 17) WAIT_BAR(3);
            else if (S == 18) WAIT_BARL(5, 15);
            else if (S == 19) WAIT_BAR(5);
            else if (SL & 1) WAIT_BARL(3, 8);
            else WAIT_BARL(6, 15);
            if (QPRIO) PRIO(3);
            SB();
        }
    }
}

constexpr size_t DISCARD = (size_t)64 << 20;
#define KROT 1
__global__ __launch_bounds__(512, 2) void k1_qattn(const K1Args args) {
    extern __shared__ __attribute__((aligned(16))) char smem[];
    const lds_cptr lds = (lds_cptr)smem;
    const unsigned ldsb = (unsigned)(uintptr_t)smem;
    const int w = rfl(threadIdx.x >> 6), hd = w >> 2, wq = w & 3, hb = (hd ^ 1) * HB;
    const int xcd = blockIdx.x & 7, idx = blockIdx.x >> 3;
    const int br = xcd >> 2, b = ((xcd >> 1) & 1) * 8 + (idx & 7), h0 = (xcd & 1) * 16 + (idx >> 3) * 2 + hd;
    QCtx q;
    q.lds = lds; q.ldx = ldsb + X1 + w * 2048; q.ldxx = ldsb + X0 + 16384 + w * 256; q.ldw = ldsb + hb + wq * 2080; q.w = w;
    q.xp = args.x[br] + (size_t)b * (SQ * DM * 2);
    q.wp = args.wq[br] + h0 * (DH * 2);
    {
        const int c0 = KROT ? ((idx & 7) * 5 / 4 + (idx >> 3) * 5 / 2) % 10 : 0;
        q.wk = c0 * (64 * HD * 2); q.xk = c0 * 128;
    }
    const size_t hoff = (size_t)(b * NH + h0) * (SK * DH * 2);
    constexpr size_t HSTEP = (size_t)8 * SK * DH * 2;
    char* const ob0 = args.ows + ((size_t)((br * NB + b) * 9) * NH + h0) * 4096;
    AttnCtx ac;
    ac.lds = lds; ac.ldk = ldsb + hb + wq * 4096; ac.ldv = ldsb + hb + VE + wq * 4224;
    Seam sm; sm.ldsd = ldsb + STO + 512;
    Pend pd;
    bf16x4 af[10][2]; bf16x8 xb0[3];
    {
        x_setup(q); w_setup(q);
        const int l = opaque_lane(), r = l & 15, g = l >> 4, q4 = r >> 2, p = l & 3, row8 = (l >> 2) & 7, hf = l >> 5;
#pragma unroll
        for (int j = 0; j < 2; ++j) ac.kvo[j] = row8 * 256 + hf * 64 + ((p ^ fsw((j << 1) | (row8 >> 2))) * 16);
        const char* k0 = args.k[br] + hoff + wq * 4096;
        DMA_K(ac, KE, k0, 0); DMA_K(ac, KE, k0, 1); DMA_K(ac, KE, k0, 2); DMA_K(ac, KE, k0, 3);
        w_dma<0, 0>(q); w_dma<0, 1>(q); x_dma<0, 0>(q); x_dma<0, 1>(q); x_dma<0, 2>(q); w_dma<1, 0>(q); w_dma<1, 1>(q); w_dma<2, 0>(q); w_dma<2, 1>(q);
        const unsigned po = (l & 1) * (unsigned)HSTEP + (l & 2) * 32768;
        touch<true>(args.k[br] + hoff, po + 16384, sm.ldsd); touch<true>(args.v[br] + hoff, po, sm.ldsd);
        x_dma<1, 0>(q); x_dma<1, 1>(q); x_dma<1, 2>(q);
#pragma unroll
        for (int i = 0; i < 9; ++i) pd.pk[i] = u32x4{0, 0, 0, 0};
        pd.ob[0] = ob0 + 8 * 4096 + (size_t)(2 * wq) * (NH * 4096); pd.ob[1] = pd.ob[0] + NH * 4096; pd.ob[2] = ob0 + 8 * 4096 + (size_t)8 * (NH * 4096) + wq * 1024; pd.lo = l * 16;
        const unsigned xa = (r >> 3) * 1024 + (r & 7) * 64 + ((g ^ fsw((r >> 2) & 3)) * 16);
        q.xa_own = X1 + xa + wq * 4096; q.xa_x = X0 + 16384 + xa;
        q.wa = hb + g * 2080 + q4 * 64 + p * 8; q.wax = q.wa + wq * 256;
        SB();
        WAIT_BAR(9);
        q_first(q, af, xb0);
    }
    for (int it = 0; it < 2; ++it) {
        STAMP(4 * it + 0);
        const int l = opaque_lane(), r = l & 15, g = l >> 4, q4 = r >> 2, p = l & 3, row8 = (l >> 2) & 7, hf = l >> 5;
        const char* kh = args.k[br] + hoff + it * HSTEP;
        const char* vh = args.v[br] + hoff + it * HSTEP;
        ac.kp = kh + wq * 4096; ac.vp = vh + wq * 4096; ac.knext = ac.kp + (it ? 7 * 16384 : HSTEP);
#pragma unroll
        for (int j = 0; j < 2; ++j) ac.kvo[j] = row8 * 256 + hf * 64 + ((p ^ fsw((j << 1) | (row8 >> 2))) * 16);
        ac.vvo = q4 * 256 + g * 64 + p * 16;
        sm.kl = hb + (r >> 3) * 2048 + (r & 7) * 64 + ((g ^ fsw((r >> 2) & 3)) * 16);
        ac.ka[0] = sm.kl + wq * 4096;
        bf16x8 qf[3][4], kf[4];
        {
            f32x4 qa[2][8], qx[2]; bf16x8 xb1[3];
            const unsigned qxa = X0 + l * 16;
            SB();
#define QS2(S) q_step<S>(q, ac, pd, qa, qx, af, xb0, xb1, qf, kf, qxa); q_step<S + 1>(q, ac, pd, qa, qx, af, xb1, xb0, qf, kf, qxa)
            QS2(0); QS2(2);
            STAMP(12);
            QS2(4); QS2(6); QS2(8); QS2(10); QS2(12); QS2(14);
            STAMP(13);
            QS2(16); QS2(18);
            DMA_V(ac, RG, ac.vp + 16384, 0); DMA_V(ac, RG, ac.vp + 16384, 1); DMA_V(ac, RG, ac.vp + 16384, 2); DMA_V(ac, RG, ac.vp + 16384, 3);
            MFMA_PAD(); SB();
            qf[0][3] = qfrag(qa[0][6], qa[0][7]); qf[1][3] = qfrag(qa[1][6], qa[1][7]);
        }
        q.wp += (1 - it) * (8 * DH * 2);
        const unsigned vl = hb + VE + g * 1056 + q4 * 64 + p * 8;
#pragma unroll
        for (int i = 0; i < 4; ++i) { const unsigned kt = (unsigned)((i + wq) & 3); ac.ka[i] = sm.kl + kt * 4096; ac.va[i] = vl + kt * 4224; }
        ac.vax = vl + wq * 256; ac.pxr = PXO + hd * 2048 + l * 16; ac.pxw = ac.pxr + (wq >> 1) * 1024 + (wq & 1) * 8;
        {
            const int i0 = (idx * 8 + w) * 40 + (l < 40 ? l : 39);
            sm.to[0] = (unsigned)((i0 >> 4) * (HD * 2) + (i0 & 15) * 128); sm.to[1] = (unsigned)(((idx >> 3) * 8 + w) * 45 + (l < 45 ? l : 44)) * 128;
            sm.tb[0] = args.wq[br] + ((xcd & 1) * 16 + 8) * (DH * 2); sm.tb[1] = q.xp;
        }
        f32x4 o[2][8], ox[2], negm[3]; float lsum[3] = {0.f, 0.f, 0.f};
        SB();
        if (PPONG && hd) asm volatile("s_barrier" ::: "memory");
        STAMP(4 * it + 1);
        fast_tile<0, true, false>(0, ac, qf, o, ox, negm, lsum, kf, q, sm);
        STAMP(14);
        for (int t = 1; t < 7; t += 2) {
            fast_tile<1, false, false>(t, ac, qf, o, ox, negm, lsum, kf, q, sm);
            fast_tile<0, false, false>(t + 1, ac, qf, o, ox, negm, lsum, kf, q, sm);
        }
        STAMP(15);
        x_setup(q); w_setup(q);
        {
            const unsigned ml = 0u - (unsigned)it;
            sm.to[0] = (unsigned)(((idx * 8 + w) * 40 + (l < 20 ? l : 19)) * 128) & ml; sm.to[1] = sm.to[0] + (2560 & ml);
            sm.tb[0] = it ? args.wo[br] + (size_t)(xcd & 3) * (32 * 320 * 128) : q.xp;
        }
        SB();
        fast_tile<1, false, true>(7, ac, qf, o, ox, negm, lsum, kf, q, sm);
        STAMP(4 * it + 2);
        {
            const unsigned xa = (r >> 3) * 1024 + (r & 7) * 64 + ((g ^ fsw((r >> 2) & 3)) * 16);
            q.xa_own = X1 + xa + wq * 4096; q.xa_x = X0 + 16384 + xa;
            q.wa = hb + g * 2080 + q4 * 64 + p * 8; q.wax = q.wa + wq * 256;
        }
        if (!PPONG) q_first(q, af, xb0);
        MFMA_PAD(); SB();
        char* ob = ob0 + it * (8 * 4096);
        float lt[3];
        lt[0] = xsum(lsum[0]); lt[1] = xsum(lsum[1]); lt[2] = 0.f;
#pragma unroll
        for (int i = 0; i < 4; ++i) lt[2] += *LDSP(const float, lds + STO + hd * 256 + i * 64 + r * 4);
#pragma unroll
        for (int a = 0; a < 2; ++a) {
            const float il = __builtin_amdgcn_rcpf(lt[a]);
#pragma unroll
            for (int s = 0; s < 4; ++s) pd.pk[4 * a + s] = opack(o[a][2 * s], o[a][2 * s + 1], il);
        }
        pd.pk[8] = opack(ox[0], ox[1], __builtin_amdgcn_rcpf(lt[2]));
        const unsigned bad = (__any(!(lt[0] < 4096.f)) ? 1u : 0u) | (__any(!(lt[1] < 4096.f)) ? 2u : 0u) | (__any(!(lt[2] < 4096.f)) ? 4u : 0u);
        pd.ob[0] = ob + ((bad & 1) ? DISCARD : 0) + (size_t)(2 * wq) * (NH * 4096); pd.ob[1] = ob + ((bad & 2) ? DISCARD : 0) + (size_t)(2 * wq + 1) * (NH * 4096);
        pd.ob[2] = ob + ((bad & 4) ? DISCARD : 0) + (size_t)8 * (NH * 4096) + wq * 1024; pd.lo = l * 16;
        if (__builtin_expect(bad != 0, 0)) {
#pragma unroll 1
            for (int job = 0; job < 9; ++job) {
                const int a = job >> 2, s = job < 8 ? (job & 3) : wq;
                if (!((bad >> a) & 1)) continue;
                bf16x8 tq[4];
#pragma unroll
                for (int ks = 0; ks < 4; ++ks) tq[ks] = a == 0 ? qf[0][ks] : (a == 1 ? qf[1][ks] : qf[2][ks]);
                slow_frag(tq, kh, vh + s * 64, ob + (size_t)(a < 2 ? 2 * wq + a : 8) * (NH * 4096) + s * 1024);
            }
            asm volatile("s_waitcnt vmcnt(0)" ::: "memory");
        }
        SB();
        if (PPONG) { if (!hd) asm volatile("s_barrier" ::: "memory"); q_first(q, af, xb0); }
        STAMP(4 * it + 3);
    }
    pstore<0>(pd); pstore<1>(pd); pstore<2>(pd); pstore<3>(pd); pstore<4>(pd); pstore<5>(pd); pstore<6>(pd); pstore<7>(pd); pstore<8>(pd);
    asm volatile("s_waitcnt vmcnt(0)" ::: "memory");
}

constexpr int K2_Q = 8 * 1056, K2_STG = 4 * K2_Q, K2_LDS = 147456 + 512;
struct K2Args { const char* ows; const char* wo[2]; char* out; };
#define TOUCH 0
#define K2_TSET 2
#define K2_TAHEAD 2
#define WOPOL ""
struct K2Ctx { lds_cptr lds; unsigned ldsw, ldsd, wv[4], av, ba[2][4]; const char* wp; const char* ap; const char* tb; int w, part; };
DI void k2_touch(const K2Ctx& c, const int G) {
    if ((c.w & 3) == 3) return;
    const int j = (c.w & 3) * 64 + opaque_lane();
    unsigned off;
    if (c.w < 4) { int u = 6 * j + c.part; u = u < 1024 ? u : 1023; off = (unsigned)(((u >> 6) & 3) * 1024 + (4 * G + (u >> 8)) * 32 + ((u >> 1) & 31)) * (DM * 2) + (u & 1) * 128; }
    else { int v = 5 * j + c.part; v = v < 768 ? v : 767; off = (unsigned)(v >> 7) * (NH * 4096) + ((v >> 5) & 3) * 32768 + G * 4096 + (v & 31) * 128; }
    asm volatile("s_mov_b32 m0, %2\n\ts_nop 0\n\tglobal_load_lds_dword %0, %1" :: "v"(off), "s"(c.tb), "s"(c.ldsd) : "memory");
}
template <int SET, int J> DI void k2_dma(const K2Ctx& c, const int st) { DMAP(WOPOL, c.ldsw, SET * K2_STG + J * 1056, c.wv[J], c.wp + (size_t)st * (32 * DM * 2), 0); }
template <int I> DI void k2_lda(const K2Ctx& c, const int st, u32x4& d) {
    asm volatile("global_load_dwordx4 %0, %1, %2" : "=v"(d) : "v"(c.av), "s"(c.ap + st * 1024 + I * (NH * 4096)) : "memory");
}
template <int SET> DI bf16x8 k2_bfrag(const K2Ctx& c, const int n) {
    const unsigned b = c.ba[SET >> 1][n >> 1]; const int im = (SET & 1) * K2_STG + (n & 1) * 32;
    return cat(ldtr(c.lds + b + im), ldtr(c.lds + b + im + 4 * 1056));
}
template <int SET, int MODE>
DI void k2_step(const K2Ctx& c, const int st, f32x4 (&acc)[3][8], u32x4 (&af)[4][3], bf16x8 (&bw)[8]) {
    constexpr int SN = (SET + 1) & 3, SA = (SET + 3) & 3;
#pragma unroll
    for (int n = 0; n < 8; ++n) {
        MFMA32T(acc[0][n], bw[n], af[SET][0]); MFMA32T(acc[1][n], bw[n], af[SET][1]); MFMA32T(acc[2][n], bw[n], af[SET][2]);
        if (MODE != 4) {
            if (n == 2) { bw[0] = k2_bfrag<SN>(c, 0); bw[1] = k2_bfrag<SN>(c, 1); bw[2] = k2_bfrag<SN>(c, 2); }
            if (n > 2) bw[n] = k2_bfrag<SN>(c, n);
        }
        if (MODE == 0 && SET == K2_TSET && n == 2) { if (TOUCH && (st >> 2) + K2_TAHEAD < 8) k2_touch(c, (st >> 2) + K2_TAHEAD); }
        if (MODE <= 1) {
            if (n == 2) k2_lda<0>(c, st + 3, af[SA][0]);
            if (n == 4) k2_lda<1>(c, st + 3, af[SA][1]);
            if (n == 6) k2_lda<2>(c, st + 3, af[SA][2]);
        }
        if (MODE == 0) {
            if (n == 3) k2_dma<SET, 0>(c, st + 4);
            if (n == 5) k2_dma<SET, 1>(c, st + 4);
            if (n == 7) { k2_dma<SET, 2>(c, st + 4); k2_dma<SET, 3>(c, st + 4); }
        }
        SB();
        if (n == 1 && MODE != 4) {
            if (MODE <= 1) asm volatile("s_waitcnt vmcnt(7) lgkmcnt(0)\n\ts_barrier" : "+v"(af[SN][0]), "+v"(af[SN][1]), "+v"(af[SN][2]) :: "memory");
            else if (MODE == 2) asm volatile("s_waitcnt vmcnt(3) lgkmcnt(0)\n\ts_barrier" : "+v"(af[SN][0]), "+v"(af[SN][1]), "+v"(af[SN][2]) :: "memory");
            else asm volatile("s_waitcnt vmcnt(0) lgkmcnt(0)\n\ts_barrier" : "+v"(af[SN][0]), "+v"(af[SN][1]), "+v"(af[SN][2]) :: "memory");
            SB();
        }
    }
}

__global__ __launch_bounds__(512, 2) void k2_oproj(const K2Args args) {
    extern __shared__ __attribute__((aligned(16))) char smem[];
    const lds_cptr lds = (lds_cptr)smem;
    const unsigned ldsb = (unsigned)(uintptr_t)smem;
    const int tid = threadIdx.x, l = tid & 63, r = l & 15, g = l >> 4, q4 = r >> 2, p = l & 3;
    const int w = rfl(tid >> 6), kq = w & 3, mh = w >> 2;
    const int xcd = blockIdx.x & 7, idx = blockIdx.x >> 3;
    const int br = xcd >> 2, mt = (xcd & 3) * 6 + idx / 5, nt = idx % 5;
    K2Ctx c;
    c.lds = lds; c.ldsw = ldsb + kq * K2_Q + mh * 4224;
    c.wp = args.wo[br] + (size_t)(kq * 1024 + mh * 16) * (DM * 2) + nt * 256;
    c.ap = args.ows + ((size_t)(br * 144 + mt * 6 + mh * 3) * NH + kq * 8) * 4096;
#pragma unroll
    for (int j = 0; j < 4; ++j) {
        c.wv[j] = (4 * j + q4) * (DM * 2) + g * 64 + p * 16;
        c.ba[0][j] = kq * K2_Q + g * 1056 + ((kq + j) & 3) * 256 + q4 * 64 + p * 8; c.ba[1][j] = c.ba[0][j] + 2 * K2_STG;
    }
    c.av = l * 16;
    c.ldsd = ldsb + 4 * K2_STG; c.w = w; c.part = w < 4 ? idx / 5 : nt;
    c.tb = w < 4 ? args.wo[br] + nt * 256 : args.ows + (size_t)(br * 144 + mt * 6) * (NH * 4096);
    f32x4 acc[3][8]; u32x4 af[4][3]; bf16x8 bw[8];
#pragma unroll
    for (int i = 0; i < 3; ++i)
#pragma unroll
        for (int n = 0; n < 8; ++n) { acc[i][n] = f32x4{0, 0, 0, 0}; PIN(acc[i][n]); }
    k2_dma<0, 0>(c, 0); k2_dma<0, 1>(c, 0); k2_dma<0, 2>(c, 0); k2_dma<0, 3>(c, 0);
    k2_lda<0>(c, 0, af[0][0]); k2_lda<1>(c, 0, af[0][1]); k2_lda<2>(c, 0, af[0][2]);
    k2_dma<1, 0>(c, 1); k2_dma<1, 1>(c, 1); k2_dma<1, 2>(c, 1); k2_dma<1, 3>(c, 1);
    k2_lda<0>(c, 1, af[1][0]); k2_lda<1>(c, 1, af[1][1]); k2_lda<2>(c, 1, af[1][2]);
    k2_dma<2, 0>(c, 2); k2_dma<2, 1>(c, 2); k2_dma<2, 2>(c, 2); k2_dma<2, 3>(c, 2);
    k2_lda<0>(c, 2, af[2][0]); k2_lda<1>(c, 2, af[2][1]); k2_lda<2>(c, 2, af[2][2]);
    k2_dma<3, 0>(c, 3); k2_dma<3, 1>(c, 3); k2_dma<3, 2>(c, 3); k2_dma<3, 3>(c, 3);
    if (TOUCH) k2_touch(c, 1);
    asm volatile("s_waitcnt vmcnt(18)\n\ts_barrier" : "+v"(af[0][0]), "+v"(af[0][1]), "+v"(af[0][2]) :: "memory");
#pragma unroll
    for (int n = 0; n < 8; ++n) bw[n] = k2_bfrag<0>(c, n);
    SB();
    for (int st = 0; st < 28; st += 4) {
        k2_step<0, 0>(c, st, acc, af, bw); k2_step<1, 0>(c, st + 1, acc, af, bw); k2_step<2, 0>(c, st + 2, acc, af, bw); k2_step<3, 0>(c, st + 3, acc, af, bw);
    }
    k2_step<0, 1>(c, 28, acc, af, bw); k2_step<1, 2>(c, 29, acc, af, bw); k2_step<2, 3>(c, 30, acc, af, bw); k2_step<3, 4>(c, 31, acc, af, bw);
    MFMA_PAD();
    asm volatile("s_waitcnt lgkmcnt(0)\n\ts_barrier" ::: "memory");
    SB();
#pragma unroll
    for (int d = 1; d < 4; ++d)
#pragma unroll
        for (int i = 0; i < 3; ++i)
#pragma unroll
            for (int a = 0; a < 2; ++a)
                *LDSP(f32x4, (lds_ptr)lds + (mh * 4 + ((kq + d) & 3)) * 18432 + ((d - 1) * 6144 + (i * 2 + a) * 1024) + l * 16) = acc[i][2 * d + a];
    asm volatile("s_waitcnt lgkmcnt(0)\n\ts_barrier" ::: "memory");
#pragma unroll
    for (int d = 0; d < 3; ++d)
#pragma unroll
        for (int i = 0; i < 3; ++i)
#pragma unroll
            for (int a = 0; a < 2; ++a) acc[i][a] += *LDSP(const f32x4, lds + w * 18432 + (d * 6144 + (i * 2 + a) * 1024) + l * 16);
#pragma unroll
    for (int i = 0; i < 3; ++i) {
        unsigned a0 = cvtpk(acc[i][0][0], acc[i][0][1]), a1 = cvtpk(acc[i][0][2], acc[i][0][3]);
        unsigned b0 = cvtpk(acc[i][1][0], acc[i][1][1]), b1 = cvtpk(acc[i][1][2], acc[i][1][3]);
        auto r0 = __builtin_amdgcn_permlane32_swap(a0, b0, false, false);
        auto r1 = __builtin_amdgcn_permlane32_swap(a1, b1, false, false);
        auto s0 = __builtin_amdgcn_permlane16_swap(r0[0], r0[1], false, false);
        auto s1 = __builtin_amdgcn_permlane16_swap(r1[0], r1[1], false, false);
        const u32x4 pk = {s0[0], s1[0], s0[1], s1[1]};
        const int row = (mt * 6 + mh * 3 + i) * 16 + r;
        *(u32x4*)(args.out + (size_t)row * (2 * DM * 2) + br * (DM * 2) + nt * 256 + kq * 64 + g * 16) = pk;
    }
}

extern "C" void kernel_launch(void* const* d_in, const int* in_sizes, int n_in, void* d_out, int out_size, void* d_ws, size_t ws_size,
                              hipStream_t stream) {
    (void)in_sizes; (void)n_in; (void)out_size; (void)ws_size;
    (void)hipFuncSetAttribute((const void*)k1_qattn, hipFuncAttributeMaxDynamicSharedMemorySize, K1_LDS);
    (void)hipFuncSetAttribute((const void*)k2_oproj, hipFuncAttributeMaxDynamicSharedMemorySize, K2_LDS);
    K1Args a1;
    a1.x[0] = (const char*)d_in[0]; a1.k[0] = (const char*)d_in[1]; a1.v[0] = (const char*)d_in[2]; a1.wq[0] = (const char*)d_in[3];
    a1.x[1] = (const char*)d_in[5]; a1.k[1] = (const char*)d_in[6]; a1.v[1] = (const char*)d_in[7]; a1.wq[1] = (const char*)d_in[8];
    a1.wo[0] = (const char*)d_in[4]; a1.wo[1] = (const char*)d_in[9]; a1.ows = (char*)d_ws;
    K2Args a2; a2.ows = (const char*)d_ws; a2.wo[0] = (const char*)d_in[4]; a2.wo[1] = (const char*)d_in[9]; a2.out = (char*)d_out;
    hipLaunchKernelGGL(k1_qattn, dim3(256), dim3(512), K1_LDS, stream, a1);
    hipLaunchKernelGGL(k2_oproj, dim3(240), dim3(512), K2_LDS, stream, a2);
}
```

```cpp
#include <hip/hip_runtime.h>
#include <hip/hip_bf16.h>

typedef short bf16x8 __attribute__((ext_vector_type(8)));
typedef short bf16x4 __attribute__((ext_vector_type(4)));
typedef float f32x4 __attribute__((ext_vector_type(4)));
typedef float f32x2 __attribute__((ext_vector_type(2)));
typedef __bf16 bf2_t __attribute__((ext_vector_type(2)));
typedef unsigned u32x4 __attribute__((ext_vector_type(4)));
typedef unsigned u32x2 __attribute__((ext_vector_type(2)));
typedef __attribute__((address_space(3))) char* lds_ptr;
typedef __attribute__((address_space(3))) const char* lds_cptr;
#define DI __device__ __forceinline__
#define LDSP(T, p) ((__attribute__((address_space(3))) T*)(p))

constexpr int NB = 16, SQ = 144, DM = 640, NH = 32, SK = 512, DH = 128, HD = NH * DH;
constexpr float QSCALE = 0.12751743082459868f;

#define MFMA32(a, b, c) __builtin_amdgcn_mfma_f32_16x16x32_bf16((a), (b), (c), 0, 0, 0)
#define MFMA16(a, b, c) __builtin_amdgcn_mfma_f32_16x16x16bf16_1k((a), (b), (c), 0, 0, 0)

#define MFMA32T(acc, a, b) asm volatile("v_mfma_f32_16x16x32_bf16 %0, %1, %2, %0" : "+v"(acc) : "v"(a), "v"(b))
#define MFMA32Z(acc, a, b) asm volatile("v_mfma_f32_16x16x32_bf16 %0, %1, %2, 0" : "=&v"(acc) : "v"(a), "v"(b))
#define MFMA16T(acc, a, b) asm volatile("v_mfma_f32_16x16x16_bf16 %0, %1, %2, %0" : "+v"(acc) : "v"(a), "v"(b))
#define MFMA16Z(acc, a, b) asm volatile("v_mfma_f32_16x16x16_bf16 %0, %1, %2, 0" : "=&v"(acc) : "v"(a), "v"(b))
#define MFMA_PAD() asm volatile("s_nop 7\n\ts_nop 3" ::: "memory")
DI unsigned cvtpk(float lo, float hi) { f32x2 v = {lo, hi}; bf2_t b = __builtin_convertvector(v, bf2_t); return __builtin_bit_cast(unsigned, b); }
DI bf16x8 ld128(lds_cptr p) { return *LDSP(const bf16x8, p); }
DI bf16x4 ldtr(lds_cptr p) { return __builtin_amdgcn_ds_read_tr16_b64_v4i16(LDSP(bf16x4, (lds_ptr)p)); }
DI bf16x8 cat(bf16x4 lo, bf16x4 hi) { return __builtin_shufflevector(lo, hi, 0, 1, 2, 3, 4, 5, 6, 7); }
DI int fsw(int u) { return (((u ^ (u >> 1)) & 1) << 1) | ((u >> 1) & 1); }
DI unsigned rfl(unsigned x) { return __builtin_amdgcn_readfirstlane(x); }

#define DIAG_LDS 160768
#define WAIT_BARL(N, L) asm volatile("s_waitcnt vmcnt(%1) lgkmcnt(%0)\n\ts_barrier" :: "i"(L), "i"(N) : "memory")
#define DQ(x)
#define WAIT_BAR(N) WAIT_BARL(N, 0)
#define STAMP(i)
constexpr int RG = 16896, HB = 4 * RG, KE = 0, KO = RG, VE = 2 * RG, VO = 3 * RG;
constexpr int X0 = 2 * HB;
constexpr int X1 = VO + 8448;
constexpr int X1X = X0 + 18432;
constexpr int PXO = X1X + 2048;
constexpr int STO = PXO + 4096;
constexpr int K1_LDS = STO + 1024;
#define SB() __builtin_amdgcn_sched_barrier(0)
#define PPONG 0
#define QPRIO 1
#define APRIO 1
#define PRIO(x) __builtin_amdgcn_s_setprio(x)
#define TS0 1
#define PFM 0
#define VPOS 0
#define KSLOT(m) ((m) == 0 ? 0 : (m) == 4 ? 1 : (m) == 8 ? 2 : (m) == 12 ? 3 : -1)
#define PIN(x) asm volatile("" : "+v"(x))
#define DMAP(POL, m0b, M0OFF, vo, sb, IMM) \
    asm volatile("s_add_u32 m0, %0, %4\n\ts_nop 0\n\tglobal_load_lds_dwordx4 %1, %2 offset:%3" POL :: "s"(m0b), "v"(vo), "s"(sb), "i"(IMM), "i"(M0OFF) : "memory", "scc")
#define QPOL ""

struct K1Args { const char* x[2]; const char* k[2]; const char* v[2]; const char* wq[2]; const char* wo[2]; char* ows; };

struct AttnCtx {
    lds_cptr lds; unsigned ldk, ldv;
    unsigned ka[4], va[4], vax, pxr, pxw, kvo[2], vvo, pwk, pwv;
    const char* kp; const char* vp; const char* knext;
    int rot;
};
DI int toff(const AttnCtx& c, const int t) { return ((t + c.rot) & 7) * 16384; }
#define KVPOL " nt"
template <int SO, int J> DI void dma_k(const AttnCtx& c, const char* kt) { DMAP(KVPOL, c.ldk, SO + (J & 1) * 896, c.kvo[J >> 1], kt, (J >> 1) * 2048 + (J & 1) * 128); }
template <int SO, int J> DI void dma_v(const AttnCtx& c, const char* vt) { DMAP(KVPOL, c.ldv, SO + J * 32, c.vvo, vt, J * 1024); }
#define DMA_K(c, SO, t, j) do { switch (j) { case 0: dma_k<SO, 0>(c, t); break; case 1: dma_k<SO, 1>(c, t); break; case 2: dma_k<SO, 2>(c, t); break; default: dma_k<SO, 3>(c, t); break; } } while (0)
#define DMA_V(c, SO, t, j) do { switch (j) { case 0: dma_v<SO, 0>(c, t); break; case 1: dma_v<SO, 1>(c, t); break; case 2: dma_v<SO, 2>(c, t); break; default: dma_v<SO, 3>(c, t); break; } } while (0)
DI bf16x8 kfrag(const AttnCtx& c, int so, int n) { return ld128(c.lds + c.ka[n >> 2] + (so + (n & 3) * 512)); }
DI bf16x4 vtr(const AttnCtx& c, int so, int i, int dt) { return ldtr(c.lds + c.va[i] + (so + (dt >> 1) * 256 + (dt & 1) * 32)); }
DI bf16x4 vxtr(const AttnCtx& c, int so, int kt, int d) { return ldtr(c.lds + c.vax + (so + kt * 4224 + d * 32)); }
DI float xmax(float m) {
    auto a = __builtin_amdgcn_permlane32_swap(__float_as_uint(m), __float_as_uint(m), false, false);
    m = fmaxf(__uint_as_float(a[0]), __uint_as_float(a[1]));
    auto b = __builtin_amdgcn_permlane16_swap(__float_as_uint(m), __float_as_uint(m), false, false);
    return fmaxf(__uint_as_float(b[0]), __uint_as_float(b[1]));
}
DI float xsum(float m) {
    auto a = __builtin_amdgcn_permlane32_swap(__float_as_uint(m), __float_as_uint(m), false, false);
    m = __uint_as_float(a[0]) + __uint_as_float(a[1]);
    auto b = __builtin_amdgcn_permlane16_swap(__float_as_uint(m), __float_as_uint(m), false, false);
    return __uint_as_float(b[0]) + __uint_as_float(b[1]);
}
DI float max4(const f32x4 v) { return fmaxf(fmaxf(v[0], v[1]), fmaxf(v[2], v[3])); }
#define EXP4(v) do { v[0] = __builtin_amdgcn_exp2f(v[0]); v[1] = __builtin_amdgcn_exp2f(v[1]); v[2] = __builtin_amdgcn_exp2f(v[2]); v[3] = __builtin_amdgcn_exp2f(v[3]); PIN(v); } while (0)
#define EXP2(v, h) do { v[2 * (h)] = __builtin_amdgcn_exp2f(v[2 * (h)]); v[2 * (h) + 1] = __builtin_amdgcn_exp2f(v[2 * (h) + 1]); PIN(v); } while (0)
#define SUM4(v, ls) do { ls += v[0]; ls += v[1]; ls += v[2]; ls += v[3]; PIN(ls); } while (0)
#define PK4(v, d) do { d = u32x2{cvtpk(v[0], v[1]), cvtpk(v[2], v[3])}; PIN(d); } while (0)
#define SUMPK(v, ls, d) do { ls += v[0]; ls += v[1]; ls += v[2]; ls += v[3]; d = u32x2{cvtpk(v[0], v[1]), cvtpk(v[2], v[3])}; PIN(ls); PIN(d); } while (0)

struct QCtx {
    lds_cptr lds; unsigned ldx[2], ldxx, ldw; int w;
    unsigned xo[3], wv, xa_own[2], xa_x, wa, wax;
    unsigned wk, xk;
    const char* xp; const char* wp;
};
constexpr int wslot(int sl) { return sl < 2 ? VE + sl * 8320 : (sl < 4 ? KO + (sl - 2) * 8320 : VO); }
template <int SL, int J> DI void w_dma(QCtx& q) {
    DMAP(QPOL, q.ldw, wslot(SL) + J * 1040, q.wv, q.wp + q.wk + J * (4 * HD * 2), 0);
    if (J == 1) { q.wk += 32 * HD * 2; if (q.wk >= DM * HD * 2) q.wk = 0; }
}
template <int ST, int J> DI void x_dma(QCtx& q) {
    if (J < 2) DMAP(QPOL, q.ldx[ST], J * 1024, q.xo[J], q.xp + q.xk, 0);
    else {
        asm volatile("s_add_u32 m0, %0, %3\n\ts_nop 0\n\tglobal_load_lds_dword %1, %2" QPOL :: "s"(q.ldxx), "v"(q.xo[2]), "s"(q.xp + q.xk), "i"(ST * 2048) : "memory", "scc");
        q.xk += 128; if (q.xk >= DM * 2) q.xk = 0;
    }
}
DI int opaque_lane() { int l; asm volatile("v_mbcnt_lo_u32_b32 %0, -1, 0\n\tv_mbcnt_hi_u32_b32 %0, -1, %0" : "=v"(l)); return l; }
DI void x_setup(QCtx& q) {
    const int l = opaque_lane(), p = l & 3, row8 = (l >> 2) & 7, hf = l >> 5, w = q.w;
#pragma unroll
    for (int j = 0; j < 2; ++j) q.xo[j] = (16 * w + 8 * j + row8) * (DM * 2) + hf * 64 + ((p ^ fsw((j << 1) | (row8 >> 2))) * 16);
    q.xo[2] = (128 + 8 * (w >> 2) + 4 * (w & 1) + (l >> 4)) * (DM * 2) + ((w >> 1) & 1) * 64 + (((row8 & 3) ^ fsw(2 * (w >> 2) + (w & 1))) * 16) + p * 4;
}
DI void w_setup(QCtx& q) {
    const int l = opaque_lane();
    q.wv = (8 * (q.w & 3) + ((l & 15) >> 2)) * (HD * 2) + (l >> 4) * 64 + (l & 3) * 16;
}
template <bool NT> DI void touch(const char* p, const unsigned off, const unsigned ldsd) {
    if (NT) asm volatile("s_mov_b32 m0, %2\n\ts_nop 0\n\tglobal_load_lds_dword %0, %1 nt" :: "v"(off), "s"(p), "s"(ldsd) : "memory");
    else asm volatile("s_mov_b32 m0, %2\n\ts_nop 0\n\tglobal_load_lds_dword %0, %1" :: "v"(off), "s"(p), "s"(ldsd) : "memory");
}
struct Seam { unsigned ldsd, to[2], kl; const char* tb; };

template <int SLOT, bool FIRST, bool LAST>
DI void fast_tile(const int t, const AttnCtx& c, const bf16x8 (&qf)[3][4], f32x4 (&o)[2][8], f32x4 (&ox)[2], f32x4 (&negm)[3], float (&lsum)[3], bf16x8 (&kf)[4],
                  QCtx& nq, const Seam& sm) {
    constexpr int SO = SLOT * RG, SN = (SLOT ^ 1) * RG;
    DQ(nq);
    const f32x4 z4 = {0.f, 0.f, 0.f, 0.f};
    f32x4 s[2][4], sx, sr; u32x2 ph[2][4], px; bf16x4 vf[5][2]; bf16x8 pxf[2], kr[4]; bf16x4 vx[2][2];
    if (FIRST) {
#pragma unroll
        for (int ks = 0; ks < 4; ++ks) kr[ks] = ld128(c.lds + sm.kl + ks * 512);
    }
    const char* kn = (SLOT == 0 && !FIRST && t == 6) ? c.knext : c.kp + toff(c, t + 2);
    const char* vn = c.vp + toff(c, t + 1);
#pragma unroll
    for (int n = 0; n < 16; ++n) {
        const int i = n >> 2, ks = n & 3;
        s[0][i] = MFMA32(kf[ks], qf[0][ks], ks ? s[0][i] : ((FIRST && i == 0) ? z4 : negm[0]));
        if (n == 4) EXP2(sx, 0);
        if (n == 5) EXP2(s[0][0], 0);
        if (n == 6) EXP2(s[1][0], 0);
        if (n == 7) SUM4(sx, lsum[2]);
        if (n == 8) SUM4(s[0][0], lsum[0]);
        if (n == 9) EXP2(s[0][1], 0);
        if (n == 10) SUM4(s[1][0], lsum[1]);
        if (n == 11) EXP2(s[1][1], 0);
        if (n == 12) SUM4(s[0][1], lsum[0]);
        if (n == 13) SUM4(s[1][1], lsum[1]);
        if (n >= 14) { vf[n - 14][0] = vtr(c, SO, 0, n - 14); vf[n - 14][1] = vtr(c, SO, 1, n - 14); }
        SB();
        s[1][i] = MFMA32(kf[ks], qf[1][ks], ks ? s[1][i] : ((FIRST && i == 0) ? z4 : negm[1]));
        if (i == 0) { SB(); sx = MFMA32(kf[ks], qf[2][ks], ks ? sx : (FIRST ? z4 : negm[2])); if (FIRST) sr = MFMA32(kr[ks], qf[2][ks], ks ? sr : z4); }
        if (n < 12) kf[ks] = kfrag(c, SO, n + 4);
        if (n == 4) EXP2(sx, 1);
        if (n == 5) EXP2(s[0][0], 1);
        if (n == 6) EXP2(s[1][0], 1);
        if (n == 7) { PK4(sx, px); *LDSP(u32x2, (lds_ptr)c.lds + c.pxw) = px; }
        if (n == 8) PK4(s[0][0], ph[0][0]);
        if (n == 9) EXP2(s[0][1], 1);
        if (n == 10) PK4(s[1][0], ph[1][0]);
        if (n == 11) EXP2(s[1][1], 1);
        if (n == 12) PK4(s[0][1], ph[0][1]);
        if (n == 13) PK4(s[1][1], ph[1][1]);
        if (n == 14) { EXP2(s[0][2], 0); vf[2][0] = vtr(c, SO, 0, 2); }
        if (n == 15) { EXP2(s[0][2], 1); vf[2][1] = vtr(c, SO, 1, 2); }
        if (LAST && n == 12) *LDSP(float, (lds_ptr)c.lds + STO + nq.w * 64 + ((c.pxr >> 2) & 60)) = xsum(lsum[2]);
        if ((!FIRST || !PFM) && !LAST && (n & 3) == VPOS) DMA_V(c, SN, vn, n >> 2);
        if (LAST) {
            if (n == 0) w_dma<0, 0>(nq); if (n == 1) w_dma<0, 1>(nq); if (n == 2) x_dma<0, 0>(nq); if (n == 3) x_dma<0, 1>(nq);
            if (n == 7) x_dma<0, 2>(nq); if (n == 8) w_dma<1, 0>(nq); if (n == 10) w_dma<1, 1>(nq);
        }
        if (APRIO) { if (n == 1) PRIO(2); if (n == 5) PRIO(1); if (n == 9) PRIO(0); }
        SB();
        if (FIRST && n == 3) {
            const float m0 = xmax(max4(s[0][0])), m1 = xmax(max4(s[1][0])), m2 = xmax(max4(sr));
            negm[0] = f32x4{-m0, -m0, -m0, -m0}; negm[1] = f32x4{-m1, -m1, -m1, -m1}; negm[2] = f32x4{-m2, -m2, -m2, -m2};
            s[0][0] -= m0; s[1][0] -= m1; sx -= m2;
            SB();
        }
        if (n == 13) { if (FIRST && PFM) WAIT_BAR(0); else if (LAST) WAIT_BAR(11); else WAIT_BAR(8); if (APRIO) PRIO(3); SB(); }
    }
#pragma unroll
    for (int m = 0; m < 16; ++m) {
        const int pr = m >> 3, dt = m & 7, mm = m + 4;
        const bf16x8 a = cat(vf[m % 5][0], vf[m % 5][1]);
        const u32x4 p0 = __builtin_shufflevector(ph[0][2 * pr], ph[0][2 * pr + 1], 0, 1, 2, 3), p1 = __builtin_shufflevector(ph[1][2 * pr], ph[1][2 * pr + 1], 0, 1, 2, 3);
        if (FIRST && pr == 0) MFMA32Z(o[0][dt], a, p0); else MFMA32T(o[0][dt], a, p0);
        if (m == 0) vf[3][0] = vtr(c, SO, 0, 3);
        if (m < 12) vf[mm % 5][0] = vtr(c, SO, 2 * (mm >> 3), mm & 7);
        if (m == 0) EXP2(s[1][2], 0);
        if (m == 1) EXP2(s[0][3], 0);
        if (m == 2) EXP2(s[1][3], 0);
        if (m == 3) SUM4(s[0][2], lsum[0]);
        if (m == 4) SUM4(s[1][2], lsum[1]);
        if (m == 5) PK4(s[0][3], ph[0][3]);
        if (m == 6) PK4(s[1][3], ph[1][3]);
        if (m == 7 || m == 8) vx[m - 7][0] = vxtr(c, SO, 0, m - 7);
        if (m == 10 || m == 11) vx[m - 10][0] = vxtr(c, SO, 2, m - 10);
        if (!LAST && m >= 14) kf[2 * (m - 14)] = kfrag(c, SN, 2 * (m - 14));
        SB();
        if (FIRST && pr == 0) MFMA32Z(o[1][dt], a, p1); else MFMA32T(o[1][dt], a, p1);
        if (m == 0) vf[3][1] = vtr(c, SO, 1, 3);
        if (m < 12) vf[mm % 5][1] = vtr(c, SO, 2 * (mm >> 3) + 1, mm & 7);
        if (m == 0) EXP2(s[1][2], 1);
        if (m == 1) EXP2(s[0][3], 1);
        if (m == 2) EXP2(s[1][3], 1);
        if (m == 3) PK4(s[0][2], ph[0][2]);
        if (m == 4) PK4(s[1][2], ph[1][2]);
        if (m == 5) SUM4(s[0][3], lsum[0]);
        if (m == 6) SUM4(s[1][3], lsum[1]);
        if (m == 5 || m == 6) pxf[m - 5] = ld128(c.lds + c.pxr + (m - 5) * 1024);
        if (m == 7 || m == 8) vx[m - 7][1] = vxtr(c, SO, 1, m - 7);
        if (m == 10 || m == 11) vx[m - 10][1] = vxtr(c, SO, 3, m - 10);
        if (!LAST && m >= 14) kf[2 * (m - 14) + 1] = kfrag(c, SN, 2 * (m - 14) + 1);
        if (m == 9 || m == 10) { SB(); if (FIRST) MFMA32Z(ox[m - 9], cat(vx[m - 9][0], vx[m - 9][1]), pxf[0]); else MFMA32T(ox[m - 9], cat(vx[m - 9][0], vx[m - 9][1]), pxf[0]); }
        if (m == 13 || m == 14) { SB(); MFMA32T(ox[m - 13], cat(vx[m - 13][0], vx[m - 13][1]), pxf[1]); }
        if (!LAST && KSLOT(m) >= 0) DMA_K(c, SO, kn, KSLOT(m));
        if (LAST) {
            if (m == 4) w_dma<2, 0>(nq); if (m == 7) w_dma<2, 1>(nq); if (m == 8) w_dma<3, 0>(nq); if (m == 9) w_dma<3, 1>(nq);
            if (m == 14) x_dma<1, 0>(nq); if (m == 15) { x_dma<1, 1>(nq); x_dma<1, 2>(nq); }
        }
        if (APRIO) { if (m == 1) PRIO(2); if (m == 5) PRIO(1); if (m == 9) PRIO(0); }
        SB();
        if (m == 13) { if (FIRST && PFM) WAIT_BAR(4); else if (LAST) WAIT_BAR(6); else WAIT_BAR(8); if (APRIO) PRIO(3); SB(); }
    }
}

#define OPOL " sc1"
DI void ostore(char* p, const u32x4 v) { asm volatile("global_store_dwordx4 %0, %1, off" OPOL "\n\ts_nop 1" :: "v"(p), "v"(v) : "memory"); }
struct Pend { u32x4 pk[9]; char* ob[3]; unsigned lo; };
template <int I> DI void pstore(const Pend& p) { asm volatile("global_store_dwordx4 %0, %1, %2 offset:%3" OPOL "\n\ts_nop 1" :: "v"(p.lo), "v"(p.pk[I]), "s"(p.ob[I >> 2]), "i"((I & 3) * 1024) : "memory"); }
DI u32x4 opack(const f32x4 u, const f32x4 v, const float il) {
    return u32x4{cvtpk(u[0] * il, u[1] * il), cvtpk(u[2] * il, u[3] * il), cvtpk(v[0] * il, v[1] * il), cvtpk(v[2] * il, v[3] * il)};
}
DI bf16x8 qfrag(const f32x4 t0, const f32x4 t1) {
    unsigned a0 = cvtpk(t0[0] * QSCALE, t0[1] * QSCALE), a1 = cvtpk(t0[2] * QSCALE, t0[3] * QSCALE);
    unsigned b0 = cvtpk(t1[0] * QSCALE, t1[1] * QSCALE), b1 = cvtpk(t1[2] * QSCALE, t1[3] * QSCALE);
    auto r0 = __builtin_amdgcn_permlane32_swap(a0, b0, false, false);
    auto r1 = __builtin_amdgcn_permlane32_swap(a1, b1, false, false);
    auto s0 = __builtin_amdgcn_permlane16_swap(r0[0], r0[1], false, false);
    auto s1 = __builtin_amdgcn_permlane16_swap(r1[0], r1[1], false, false);
    u32x4 u = {s0[0], s1[0], s0[1], s1[1]};
    return __builtin_bit_cast(bf16x8, u);
}

DI void slow_frag(const bf16x8 (&tq)[4], const char* kp, const char* vp, char* dst) {
    const int l = opaque_lane(), r = l & 15, g = l >> 4;
    float m = -3.0e38f, ls = 0.f; f32x4 acc[2] = {{0, 0, 0, 0}, {0, 0, 0, 0}};
#pragma unroll 1
    for (int kt = 0; kt < 32; ++kt) {
        f32x4 s = {0, 0, 0, 0};
#pragma unroll
        for (int ks = 0; ks < 4; ++ks) s = MFMA32(*(const bf16x8*)(kp + (kt * 16 + r) * 256 + ks * 64 + g * 16), tq[ks], s);
        const float mn = fmaxf(m, xmax(max4(s))), f = __builtin_amdgcn_exp2f(m - mn);
        m = mn; s -= mn; ls *= f; u32x2 pk;
        EXP4(s); SUMPK(s, ls, pk);
#pragma unroll
        for (int d = 0; d < 2; ++d) {
            const short* vq = (const short*)(vp + (kt * 16 + 4 * g) * 256 + (d * 16 + r) * 2);
            const bf16x4 a = {vq[0], vq[128], vq[256], vq[384]};
            acc[d] = MFMA16(a, __builtin_bit_cast(bf16x4, pk), acc[d] * f);
        }
    }
    ostore(dst + l * 16, opack(acc[0], acc[1], __builtin_amdgcn_rcpf(xsum(ls))));
}

DI void slow_q(bf16x8 (&tq)[4], const char* xr, const char* wc) {
    const int l = opaque_lane(), r = l & 15, g = l >> 4;
#pragma unroll 1
    for (int ks = 0; ks < 4; ++ks) {
        f32x4 a0 = {0, 0, 0, 0}, a1 = {0, 0, 0, 0};
#pragma unroll 1
        for (int kk = 0; kk < 20; ++kk) {
            const bf16x8 xb = *(const bf16x8*)(xr + r * (DM * 2) + kk * 64 + g * 16);
            const short* wp = (const short*)(wc + (size_t)(32 * kk + 8 * g) * (HD * 2) + (32 * ks + r) * 2);
            bf16x8 w0, w1;
#pragma unroll
            for (int j = 0; j < 8; ++j) { w0[j] = wp[j * HD]; w1[j] = wp[j * HD + 16]; }
            a0 = MFMA32(w0, xb, a0); a1 = MFMA32(w1, xb, a1);
        }
        tq[0] = tq[1]; tq[1] = tq[2]; tq[2] = tq[3]; tq[3] = qfrag(a0, a1);
    }
}
DI bf16x4 wtr(const QCtx& q, const int sl, const int j, const int hi) {
    const int im = wslot(sl) + ((j == 0 || j == 9) ? (j & 1) * 32 : ((j - 1) >> 1) * 256 + ((j - 1) & 1) * 32) + hi * 1040;
    return ldtr(q.lds + ((j == 0 || j == 9) ? q.wax : q.wa) + im);
}
DI bf16x8 xfrag(const QCtx& q, const int s, const int i) {
    return i < 2 ? ld128(q.lds + q.xa_own[(s >> 1) & 1] + ((s & 1) * 512 + i * 2048)) : ld128(q.lds + q.xa_x + (((s >> 1) & 1) * 2048 + (s & 1) * 512));
}
DI void q_addr(QCtx& q, const int hb) {
    const int l = opaque_lane(), r = l & 15, wq = q.w & 3;
    const unsigned xa = (r >> 3) * 1024 + (r & 7) * 64 + (((l >> 4) ^ fsw((r >> 2) & 3)) * 16);
    q.xa_own[0] = X0 + xa + wq * 4096; q.xa_own[1] = X1 + (wq < 2 ? HB : 0) + xa + (wq & 1) * 4096; q.xa_x = X0 + 16384 + xa;
    q.wa = hb + (l >> 4) * 2080 + (r >> 2) * 64 + (l & 3) * 8; q.wax = q.wa + wq * 256;
}
DI void q_first(const QCtx& q, bf16x4 (&af)[10][2], bf16x8 (&xb)[3]) {
    xb[2] = xfrag(q, 0, 2);
#pragma unroll
    for (int j = 0; j < 10; ++j) { const int jj = j == 0 ? 0 : (j == 1 ? 9 : j - 1); if (jj != 8) { af[jj][0] = wtr(q, 0, jj, 0); af[jj][1] = wtr(q, 0, jj, 1); } if (j == 1) { xb[0] = xfrag(q, 0, 0); xb[1] = xfrag(q, 0, 1); } }
}
template <int I> DI void pf_load(const AttnCtx& c, u32x4& d) {
    if (I < 4 || I >= 8) asm volatile("global_load_dwordx4 %0, %1, %2 offset:%3 nt" : "=v"(d) : "v"(c.vvo), "s"(c.vp + toff(c, I >= 8)), "i"((I & 3) * 1024) : "memory");
    else asm volatile("global_load_dwordx4 %0, %1, %2 offset:%3 nt" : "=v"(d) : "v"(c.kvo[(I & 3) >> 1]), "s"(c.kp + toff(c, 1)), "i"(((I & 3) >> 1) * 2048 + (I & 1) * 128) : "memory");
}
template <int I> DI void pf_put(const unsigned pw, const u32x4& d) {
    asm volatile("ds_write_b128 %0, %1 offset:%2" :: "v"(pw), "v"(d), "i"(I < 4 ? I * 1056 : (I < 8 ? KO + (I & 3) * 1024 : RG + (I & 3) * 1056)) : "memory");
}
constexpr int q_w(int s) { return s >= 0 && s < 16 ? 2 : 0; }
constexpr int q_x(int s) { return s >= 0 && s < 17 && (s & 1) ? 3 : 0; }
constexpr int q_post(int s) { return s < 0 || s > 16 ? 0 : ((s & 1) ? ((PFM || (s >= TS0 && s < TS0 + 4)) ? 2 : 0) : 1); }
constexpr int q_all(int s) { return q_w(s) + q_x(s) + q_post(s); }
constexpr int q_wait(int s) { return (s & 1) ? q_post(s - 2) + q_all(s - 1) : q_x(s - 3) + q_post(s - 3) + q_all(s - 2) + q_all(s - 1); }
template <int N, int L> DI void wait_bar(QCtx& dq) { WAIT_BARL(N, L); }
template <int S>
DI void q_step(QCtx& q, const AttnCtx& ac, const Pend& pd, f32x4 (&qa)[2][8], f32x4 (&qx)[2], bf16x4 (&af)[10][2], const bf16x8 (&xc)[3], bf16x8 (&xn)[3],
               bf16x8 (&qf)[3][4], bf16x8 (&kf)[4], const unsigned qxa, u32x4 (&pf)[12], const Seam& sm) {
    constexpr int SL = S % 5, SN = (S + 1) % 5, SP = (S + 4) % 5, BJ = S == 19 ? 3 : 1, PI = (S & 1) && S >= 5 && S <= 15 ? S - 5 : 0;
#pragma unroll
    for (int j = 0; j < 9; ++j) {
        const bf16x8 a0 = cat(af[j][0], af[j][1]);
        if (j > 0) { if (S == 0) MFMA32Z(qa[0][j - 1], a0, xc[0]); else MFMA32T(qa[0][j - 1], a0, xc[0]); }
        else { if (S == 0) MFMA32Z(qx[0], a0, xc[2]); else MFMA32T(qx[0], a0, xc[2]); }
        if (j == 0) af[8][0] = wtr(q, SL, 8, 0);
        if (S < 19) {
            if (j >= 2 && j < 7) { af[j - 2][0] = wtr(q, SN, j - 2, 0); af[j - 2][1] = wtr(q, SN, j - 2, 1); }
            if (j >= 7) af[j - 1][0] = wtr(q, SN, j - 1, 0);
        } else {
            if (j == 2) *LDSP(bf16x8, (lds_ptr)q.lds + qxa + q.w * 1024) = qfrag(qx[0], qx[1]);
            if (j == 4 || j == 6 || j == 8) qf[0][(j - 4) >> 1] = qfrag(qa[0][j - 4], qa[0][j - 3]);
            if (j >= 4 && j < 8) qf[2][j - 4] = ld128(q.lds + qxa + (q.w >> 2) * 4096 + (j - 4) * 1024);
        }
        SB();
        if (j > 0) { if (S == 0) MFMA32Z(qa[1][j - 1], a0, xc[1]); else MFMA32T(qa[1][j - 1], a0, xc[1]); }
        else { const bf16x8 a1 = cat(af[9][0], af[9][1]); if (S == 0) MFMA32Z(qx[1], a1, xc[2]); else MFMA32T(qx[1], a1, xc[2]); }
        if (j == 0) af[8][1] = wtr(q, SL, 8, 1);
        if (S < 19) {
            if (j == 2) { af[9][0] = wtr(q, SN, 9, 0); af[9][1] = wtr(q, SN, 9, 1); }
            if (j == 3) xn[2] = xfrag(q, S + 1, 2);
            if (j == 4 || j == 5) xn[j - 4] = xfrag(q, S + 1, j - 4);
            if (j == 6) { af[5][0] = wtr(q, SN, 5, 0); af[5][1] = wtr(q, SN, 5, 1); }
            if (j >= 7) af[j - 1][1] = wtr(q, SN, j - 1, 1);
        } else {
            if (j == 4 || j == 6 || j == 8) qf[1][(j - 4) >> 1] = qfrag(qa[1][j - 4], qa[1][j - 3]);
            if (j >= 5) kf[j - 5] = kfrag(ac, 0, j - 5);
        }
        if (S < 16) { if (j == 2) w_dma<SP, 0>(q); if (j == 3) w_dma<SP, 1>(q); }
        if (S < 17 && (S & 1)) {
            if (j == 4) x_dma<((S >> 1) & 1), 0>(q);
            if (j == 5) x_dma<((S >> 1) & 1), 1>(q);
            if (j == 6) x_dma<((S >> 1) & 1), 2>(q);
            if (S >= TS0 && S < TS0 + 4) { if (j >= 7) touch<false>(sm.tb + (S - TS0 + j - 7) * 1280, sm.to[0], sm.ldsd); }
            else if (PFM) { if (j == 7) pf_load<PI>(ac, pf[PI]); if (j == 8) pf_load<PI + 1>(ac, pf[PI + 1]); }
        }
        if (S < 17 && !(S & 1) && j == 6) pstore<(S >> 1)>(pd);
        if (!PFM && S == 18 && j >= 2 && !(j & 1)) DMA_V(ac, 0, ac.vp + toff(ac, 0), (j - 2) >> 1);
        if (!PFM && S == 19 && j >= 5) DMA_K(ac, KO, ac.kp + toff(ac, 1), j - 5);
        if (PFM && S == 18) { if (j == 3) pf_put<0>(ac.pwv, pf[0]); if (j == 4) pf_put<1>(ac.pwv, pf[1]); if (j == 5) pf_put<2>(ac.pwv, pf[2]); if (j == 7) pf_put<3>(ac.pwv, pf[3]); }
        if (PFM && S == 19) {
            if (j == 4) pf_put<4>(ac.pwk, pf[4]); if (j == 5) { pf_put<5>(ac.pwk, pf[5]); pf_put<6>(ac.pwk, pf[6]); } if (j == 6) pf_put<7>(ac.pwk, pf[7]);
            if (j == 7) { pf_put<8>(ac.pwv, pf[8]); pf_put<9>(ac.pwv, pf[9]); } if (j == 8) { pf_put<10>(ac.pwv, pf[10]); pf_put<11>(ac.pwv, pf[11]); }
        }
        if (QPRIO) { if (j == 3) PRIO(2); if (j == 5) PRIO(1); if (j == 7) PRIO(0); }
        SB();
        if (j == BJ) {
            if (S == 0) wait_bar<7, 15>(q);
            else if (S == 2) wait_bar<3 + q_all(0) + q_all(1), 15>(q);
            else if (S == 19) wait_bar<PFM ? 0 : 9, 0>(q);
            else wait_bar<q_wait(S), (S & 1) ? 8 : 15>(q);
            if (QPRIO) PRIO(3);
            SB();
        }
    }
}

constexpr size_t DISCARD = (size_t)128 << 20;
#define TMASK ~0u
#define TROT 0
#define NOCLAIM 0
#define KROT 1
__global__ __launch_bounds__(512, 2) void k1_qattn(const K1Args args) {
    extern __shared__ __attribute__((aligned(16))) char smem[];
    const lds_cptr lds = (lds_cptr)smem;
    const unsigned ldsb = (unsigned)(uintptr_t)smem;
    const int w = rfl(threadIdx.x >> 6), hd = w >> 2, wq = w & 3, hb = (hd ^ 1) * HB;
    const int xcd = blockIdx.x & 7, idx = blockIdx.x >> 3;
    const int br = xcd >> 2, b = ((xcd >> 1) & 1) * 8 + (idx & 7), h0 = (xcd & 1) * 16 + (idx >> 3) * 2 + hd;
    QCtx q;
    DQ(q);
    q.lds = lds; q.ldx[0] = ldsb + X0 + w * 2048; q.ldx[1] = ldsb + X1 + (w < 4 ? HB : 0) + (w & 3) * 2048; q.ldxx = ldsb + X0 + 16384 + w * 256; q.ldw = ldsb + hb + wq * 2080; q.w = w;
    q.xp = args.x[br] + (size_t)b * (SQ * DM * 2);
    q.wp = args.wq[br] + h0 * (DH * 2);
    {
        const int c0 = KROT ? ((idx & 7) * 5 / 4 + (idx >> 3) * 5 / 2) % 10 : 0;
        q.wk = c0 * (64 * HD * 2); q.xk = c0 * 128;
    }
    const size_t hoff = (size_t)(b * NH + h0) * (SK * DH * 2);
    constexpr size_t HSTEP = (size_t)8 * SK * DH * 2;
    char* const ob0 = args.ows + ((size_t)((br * NB + b) * 9) * NH + h0) * 4096;
    AttnCtx ac;
    ac.lds = lds; ac.ldk = ldsb + hb + wq * 4096; ac.ldv = ldsb + hb + VE + wq * 4224;
    ac.rot = TROT;
    Seam sm; sm.ldsd = ldsb + STO + 512;
    Pend pd;
    bf16x4 af[10][2]; bf16x8 xb0[3];
    {
        x_setup(q); w_setup(q);
        const int l = opaque_lane(), p = l & 3, row8 = (l >> 2) & 7, hf = l >> 5;
#pragma unroll
        for (int j = 0; j < 2; ++j) ac.kvo[j] = row8 * 256 + hf * 64 + ((p ^ fsw((j << 1) | (row8 >> 2))) * 16);
        const unsigned po = (l & 1) * (unsigned)HSTEP + (l & 2) * 32768;
        touch<true>(args.k[br] + hoff, po + 16384, sm.ldsd); touch<true>(args.v[br] + hoff, po, sm.ldsd);
        const char* k0 = args.k[br] + hoff + wq * 4096 + toff(ac, 0);
        DMA_K(ac, KE, k0, 0); DMA_K(ac, KE, k0, 1); DMA_K(ac, KE, k0, 2); DMA_K(ac, KE, k0, 3);
        w_dma<0, 0>(q); w_dma<0, 1>(q); x_dma<0, 0>(q); x_dma<0, 1>(q); x_dma<0, 2>(q); w_dma<1, 0>(q); w_dma<1, 1>(q); w_dma<2, 0>(q); w_dma<2, 1>(q);
        w_dma<3, 0>(q); w_dma<3, 1>(q);
        x_dma<1, 0>(q); x_dma<1, 1>(q); x_dma<1, 2>(q);
#pragma unroll
        for (int i = 0; i < 9; ++i) pd.pk[i] = u32x4{0, 0, 0, 0};
        pd.ob[0] = ob0 + 8 * 4096 + (size_t)(2 * wq) * (NH * 4096); pd.ob[1] = pd.ob[0] + NH * 4096; pd.ob[2] = ob0 + 8 * 4096 + (size_t)8 * (NH * 4096) + wq * 1024; pd.lo = l * 16;
        q_addr(q, hb);
        SB();
        WAIT_BAR(9);
        q_first(q, af, xb0);
    }
    for (int it = 0; it < 2; ++it) {
        STAMP(4 * it + 0);
        const int l = opaque_lane(), r = l & 15, g = l >> 4, q4 = r >> 2, p = l & 3, row8 = (l >> 2) & 7, hf = l >> 5;
        const char* kh = args.k[br] + hoff + it * HSTEP;
        const char* vh = args.v[br] + hoff + it * HSTEP;
        ac.kp = kh + wq * 4096; ac.vp = vh + wq * 4096; ac.knext = ac.kp + (it ? toff(ac, 7) : HSTEP + toff(ac, 0));
#pragma unroll
        for (int j = 0; j < 2; ++j) ac.kvo[j] = row8 * 256 + hf * 64 + ((p ^ fsw((j << 1) | (row8 >> 2))) * 16);
        ac.vvo = q4 * 256 + g * 64 + p * 16;
        ac.pwk = ac.ldk + l * 16; ac.pwv = ac.ldv + l * 16;
        sm.kl = hb + (r >> 3) * 2048 + (r & 7) * 64 + ((g ^ fsw((r >> 2) & 3)) * 16);
        ac.ka[0] = sm.kl + wq * 4096;
        {
            sm.to[0] = (unsigned)(((idx * 8 + w) * 40 + (l < 10 ? l : 9)) * 128) & (0u - (unsigned)it) & TMASK;
            sm.tb = it ? args.wo[br] + (size_t)(xcd & 3) * (32 * 320 * 128) : q.xp;
        }
        bf16x8 qf[3][4], kf[4];
        {
            f32x4 qa[2][8], qx[2]; bf16x8 xb1[3]; u32x4 pf[12];
            const unsigned qxa = X0 + l * 16;
            SB();
#define QS2(S) q_step<S>(q, ac, pd, qa, qx, af, xb0, xb1, qf, kf, qxa, pf, sm); q_step<S + 1>(q, ac, pd, qa, qx, af, xb1, xb0, qf, kf, qxa, pf, sm)
            QS2(0); QS2(2);
            STAMP(12);
            QS2(4); QS2(6); QS2(8); QS2(10); QS2(12); QS2(14);
            STAMP(13);
            QS2(16); QS2(18);
            MFMA_PAD(); SB();
            qf[0][3] = qfrag(qa[0][6], qa[0][7]); qf[1][3] = qfrag(qa[1][6], qa[1][7]);
        }
        q.wp += (1 - it) * (8 * DH * 2);
        const unsigned vl = hb + VE + g * 1056 + q4 * 64 + p * 8;
#pragma unroll
        for (int i = 0; i < 4; ++i) { const unsigned kt = (unsigned)((i + wq) & 3); ac.ka[i] = sm.kl + kt * 4096; ac.va[i] = vl + kt * 4224; }
        ac.vax = vl + wq * 256; ac.pxr = PXO + hd * 2048 + l * 16; ac.pxw = ac.pxr + (wq >> 1) * 1024 + (wq & 1) * 8;
        f32x4 o[2][8], ox[2], negm[3]; float lsum[3] = {0.f, 0.f, 0.f};
        SB();
        if (PPONG && hd) asm volatile("s_barrier" ::: "memory");
        STAMP(4 * it + 1);
        fast_tile<0, true, false>(0, ac, qf, o, ox, negm, lsum, kf, q, sm);
        STAMP(14);
        for (int t = 1; t < 7; t += 2) {
            fast_tile<1, false, false>(t, ac, qf, o, ox, negm, lsum, kf, q, sm);
            fast_tile<0, false, false>(t + 1, ac, qf, o, ox, negm, lsum, kf, q, sm);
        }
        STAMP(15);
        x_setup(q); w_setup(q);
        SB();
        fast_tile<1, false, true>(7, ac, qf, o, ox, negm, lsum, kf, q, sm);
        STAMP(4 * it + 2);
        const int l2 = opaque_lane(), r2 = l2 & 15;
        q_addr(q, hb);
        if (!PPONG) q_first(q, af, xb0);
        MFMA_PAD(); SB();
        char* ob = ob0 + it * (8 * 4096);
        float lt[3];
        lt[0] = xsum(lsum[0]); lt[1] = xsum(lsum[1]); lt[2] = 0.f;
#pragma unroll
        for (int i = 0; i < 4; ++i) lt[2] += *LDSP(const float, lds + STO + hd * 256 + i * 64 + r2 * 4);
#pragma unroll
        for (int a = 0; a < 2; ++a) {
            const float il = __builtin_amdgcn_rcpf(lt[a]);
#pragma unroll
            for (int s = 0; s < 4; ++s) pd.pk[4 * a + s] = opack(o[a][2 * s], o[a][2 * s + 1], il);
        }
        pd.pk[8] = opack(ox[0], ox[1], __builtin_amdgcn_rcpf(lt[2]));
        const unsigned bad = (__any(!(lt[0] < 4096.f)) ? 1u : 0u) | (__any(!(lt[1] < 4096.f)) ? 2u : 0u) | (__any(!(lt[2] < 4096.f)) ? 4u : 0u);
        pd.ob[0] = ob + ((bad & 1) ? DISCARD : 0) + (size_t)(2 * wq) * (NH * 4096); pd.ob[1] = ob + ((bad & 2) ? DISCARD : 0) + (size_t)(2 * wq + 1) * (NH * 4096);
        pd.ob[2] = ob + ((bad & 4) ? DISCARD : 0) + (size_t)8 * (NH * 4096) + wq * 1024; pd.lo = l2 * 16;
        if (__builtin_expect(bad != 0, 0)) {
#pragma unroll 1
            for (int a = 0; a < 3; ++a) {
                if (!((bad >> a) & 1)) continue;
                const int qt = a < 2 ? 2 * wq + a : 8;
                bf16x8 tq[4] = {};
                slow_q(tq, q.xp + qt * (16 * DM * 2), args.wq[br] + (h0 + 8 * it) * (DH * 2));
#pragma unroll 1
                for (int s = a < 2 ? 0 : wq; s < (a < 2 ? 4 : wq + 1); ++s) slow_frag(tq, kh, vh + s * 64, ob + (size_t)qt * (NH * 4096) + s * 1024);
            }
            asm volatile("s_waitcnt vmcnt(0)" ::: "memory");
        }
        SB();
        if (PPONG) { if (!hd) asm volatile("s_barrier" ::: "memory"); q_first(q, af, xb0); }
        STAMP(4 * it + 3);
    }
    pstore<0>(pd); pstore<1>(pd); pstore<2>(pd); pstore<3>(pd); pstore<4>(pd); pstore<5>(pd); pstore<6>(pd); pstore<7>(pd); pstore<8>(pd);
    asm volatile("s_waitcnt vmcnt(0)" ::: "memory");
}

constexpr int K2_Q = 8 * 1056, K2_STG = 4 * K2_Q, K2_LDS = 147456 + 512;
struct K2Args { const char* ows; const char* wo[2]; char* out; };
#define TOUCH 0
#define K2_TSET 2
#define K2_TAHEAD 2
#define WOPOL ""
struct K2Ctx { lds_cptr lds; unsigned ldsw, ldsd, wv[4], av, ba[2][4]; const char* wp; const char* ap; const char* tb; int w, part; };
DI void k2_touch(const K2Ctx& c, const int G) {
    if ((c.w & 3) == 3) return;
    const int j = (c.w & 3) * 64 + opaque_lane();
    unsigned off;
    if (c.w < 4) { int u = 6 * j + c.part; u = u < 1024 ? u : 1023; off = (unsigned)(((u >> 6) & 3) * 1024 + (4 * G + (u >> 8)) * 32 + ((u >> 1) & 31)) * (DM * 2) + (u & 1) * 128; }
    else { int v = 5 * j + c.part; v = v < 768 ? v : 767; off = (unsigned)(v >> 7) * (NH * 4096) + ((v >> 5) & 3) * 32768 + G * 4096 + (v & 31) * 128; }
    asm volatile("s_mov_b32 m0, %2\n\ts_nop 0\n\tglobal_load_lds_dword %0, %1" :: "v"(off), "s"(c.tb), "s"(c.ldsd) : "memory");
}
template <int SET, int J> DI void k2_dma(const K2Ctx& c, const int st) { DMAP(WOPOL, c.ldsw, SET * K2_STG + J * 1056, c.wv[J], c.wp + (size_t)st * (32 * DM * 2), 0); }
template <int I> DI void k2_lda(const K2Ctx& c, const int st, u32x4& d) {
    asm volatile("global_load_dwordx4 %0, %1, %2" : "=v"(d) : "v"(c.av), "s"(c.ap + st * 1024 + I * (NH * 4096)) : "memory");
}
template <int SET> DI bf16x8 k2_bfrag(const K2Ctx& c, const int n) {
    const unsigned b = c.ba[SET >> 1][n >> 1]; const int im = (SET & 1) * K2_STG + (n & 1) * 32;
    return cat(ldtr(c.lds + b + im), ldtr(c.lds + b + im + 4 * 1056));
}
template <int SET, int MODE>
DI void k2_step(const K2Ctx& c, const int st, f32x4 (&acc)[3][8], u32x4 (&af)[4][3], bf16x8 (&bw)[8]) {
    constexpr int SN = (SET + 1) & 3, SA = (SET + 3) & 3;
#pragma unroll
    for (int n = 0; n < 8; ++n) {
        MFMA32T(acc[0][n], bw[n], af[SET][0]); MFMA32T(acc[1][n], bw[n], af[SET][1]); MFMA32T(acc[2][n], bw[n], af[SET][2]);
        if (MODE != 4) {
            if (n == 2) { bw[0] = k2_bfrag<SN>(c, 0); bw[1] = k2_bfrag<SN>(c, 1); bw[2] = k2_bfrag<SN>(c, 2); }
            if (n > 2) bw[n] = k2_bfrag<SN>(c, n);
        }
        if (MODE == 0 && SET == K2_TSET && n == 2) { if (TOUCH && (st >> 2) + K2_TAHEAD < 8) k2_touch(c, (st >> 2) + K2_TAHEAD); }
        if (MODE <= 1) {
            if (n == 2) k2_lda<0>(c, st + 3, af[SA][0]);
            if (n == 4) k2_lda<1>(c, st + 3, af[SA][1]);
            if (n == 6) k2_lda<2>(c, st + 3, af[SA][2]);
        }
        if (MODE == 0) {
            if (n == 3) k2_dma<SET, 0>(c, st + 4);
            if (n == 5) k2_dma<SET, 1>(c, st + 4);
            if (n == 7) { k2_dma<SET, 2>(c, st + 4); k2_dma<SET, 3>(c, st + 4); }
        }
        SB();
        if (n == 1 && MODE != 4) {
            if (MODE <= 1) asm volatile("s_waitcnt vmcnt(7) lgkmcnt(0)\n\ts_barrier" : "+v"(af[SN][0]), "+v"(af[SN][1]), "+v"(af[SN][2]) :: "memory");
            else if (MODE == 2) asm volatile("s_waitcnt vmcnt(3) lgkmcnt(0)\n\ts_barrier" : "+v"(af[SN][0]), "+v"(af[SN][1]), "+v"(af[SN][2]) :: "memory");
            else asm volatile("s_waitcnt vmcnt(0) lgkmcnt(0)\n\ts_barrier" : "+v"(af[SN][0]), "+v"(af[SN][1]), "+v"(af[SN][2]) :: "memory");
            SB();
        }
    }
}

__global__ __launch_bounds__(512, 2) void k2_oproj(const K2Args args) {
    extern __shared__ __attribute__((aligned(16))) char smem[];
    const lds_cptr lds = (lds_cptr)smem;
    const unsigned ldsb = (unsigned)(uintptr_t)smem;
    const int tid = threadIdx.x, l = tid & 63, r = l & 15, g = l >> 4, q4 = r >> 2, p = l & 3;
    const int w = rfl(tid >> 6), kq = w & 3, mh = w >> 2;
    const int xcd = blockIdx.x & 7, idx = blockIdx.x >> 3;
    const int br = xcd >> 2, mt = (xcd & 3) * 6 + idx / 5, nt = idx % 5;
    K2Ctx c;
    c.lds = lds; c.ldsw = ldsb + kq * K2_Q + mh * 4224;
    c.wp = args.wo[br] + (size_t)(kq * 1024 + mh * 16) * (DM * 2) + nt * 256;
    c.ap = args.ows + ((size_t)(br * 144 + mt * 6 + mh * 3) * NH + kq * 8) * 4096;
#pragma unroll
    for (int j = 0; j < 4; ++j) {
        c.wv[j] = (4 * j + q4) * (DM * 2) + g * 64 + p * 16;
        c.ba[0][j] = kq * K2_Q + g * 1056 + ((kq + j) & 3) * 256 + q4 * 64 + p * 8; c.ba[1][j] = c.ba[0][j] + 2 * K2_STG;
    }
    c.av = l * 16;
    c.ldsd = ldsb + 4 * K2_STG; c.w = w; c.part = w < 4 ? idx / 5 : nt;
    c.tb = w < 4 ? args.wo[br] + nt * 256 : args.ows + (size_t)(br * 144 + mt * 6) * (NH * 4096);
    f32x4 acc[3][8]; u32x4 af[4][3]; bf16x8 bw[8];
#pragma unroll
    for (int i = 0; i < 3; ++i)
#pragma unroll
        for (int n = 0; n < 8; ++n) { acc[i][n] = f32x4{0, 0, 0, 0}; PIN(acc[i][n]); }
    k2_dma<0, 0>(c, 0); k2_dma<0, 1>(c, 0); k2_dma<0, 2>(c, 0); k2_dma<0, 3>(c, 0);
    k2_lda<0>(c, 0, af[0][0]); k2_lda<1>(c, 0, af[0][1]); k2_lda<2>(c, 0, af[0][2]);
    k2_dma<1, 0>(c, 1); k2_dma<1, 1>(c, 1); k2_dma<1, 2>(c, 1); k2_dma<1, 3>(c, 1);
    k2_lda<0>(c, 1, af[1][0]); k2_lda<1>(c, 1, af[1][1]); k2_lda<2>(c, 1, af[1][2]);
    k2_dma<2, 0>(c, 2); k2_dma<2, 1>(c, 2); k2_dma<2, 2>(c, 2); k2_dma<2, 3>(c, 2);
    k2_lda<0>(c, 2, af[2][0]); k2_lda<1>(c, 2, af[2][1]); k2_lda<2>(c, 2, af[2][2]);
    k2_dma<3, 0>(c, 3); k2_dma<3, 1>(c, 3); k2_dma<3, 2>(c, 3); k2_dma<3, 3>(c, 3);
    if (TOUCH) k2_touch(c, 1);
    asm volatile("s_waitcnt vmcnt(18)\n\ts_barrier" : "+v"(af[0][0]), "+v"(af[0][1]), "+v"(af[0][2]) :: "memory");
#pragma unroll
    for (int n = 0; n < 8; ++n) bw[n] = k2_bfrag<0>(c, n);
    SB();
    for (int st = 0; st < 28; st += 4) {
        k2_step<0, 0>(c, st, acc, af, bw); k2_step<1, 0>(c, st + 1, acc, af, bw); k2_step<2, 0>(c, st + 2, acc, af, bw); k2_step<3, 0>(c, st + 3, acc, af, bw);
    }
    k2_step<0, 1>(c, 28, acc, af, bw); k2_step<1, 2>(c, 29, acc, af, bw); k2_step<2, 3>(c, 30, acc, af, bw); k2_step<3, 4>(c, 31, acc, af, bw);
    MFMA_PAD();
    asm volatile("s_waitcnt lgkmcnt(0)\n\ts_barrier" ::: "memory");
    SB();
#pragma unroll
    for (int d = 1; d < 4; ++d)
#pragma unroll
        for (int i = 0; i < 3; ++i)
#pragma unroll
            for (int a = 0; a < 2; ++a)
                *LDSP(f32x4, (lds_ptr)lds + (mh * 4 + ((kq + d) & 3)) * 18432 + ((d - 1) * 6144 + (i * 2 + a) * 1024) + l * 16) = acc[i][2 * d + a];
    asm volatile("s_waitcnt lgkmcnt(0)\n\ts_barrier" ::: "memory");
#pragma unroll
    for (int d = 0; d < 3; ++d)
#pragma unroll
        for (int i = 0; i < 3; ++i)
#pragma unroll
            for (int a = 0; a < 2; ++a) acc[i][a] += *LDSP(const f32x4, lds + w * 18432 + (d * 6144 + (i * 2 + a) * 1024) + l * 16);
#pragma unroll
    for (int i = 0; i < 3; ++i) {
        unsigned a0 = cvtpk(acc[i][0][0], acc[i][0][1]), a1 = cvtpk(acc[i][0][2], acc[i][0][3]);
        unsigned b0 = cvtpk(acc[i][1][0], acc[i][1][1]), b1 = cvtpk(acc[i][1][2], acc[i][1][3]);
        auto r0 = __builtin_amdgcn_permlane32_swap(a0, b0, false, false);
        auto r1 = __builtin_amdgcn_permlane32_swap(a1, b1, false, false);
        auto s0 = __builtin_amdgcn_permlane16_swap(r0[0], r0[1], false, false);
        auto s1 = __builtin_amdgcn_permlane16_swap(r1[0], r1[1], false, false);
        const u32x4 pk = {s0[0], s1[0], s0[1], s1[1]};
        const int row = (mt * 6 + mh * 3 + i) * 16 + r;
        *(u32x4*)(args.out + (size_t)row * (2 * DM * 2) + br * (DM * 2) + nt * 256 + kq * 64 + g * 16) = pk;
    }
}

extern "C" void kernel_launch(void* const* d_in, const int* in_sizes, int n_in, void* d_out, int out_size, void* d_ws, size_t ws_size,
                              hipStream_t stream) {
    (void)in_sizes; (void)n_in; (void)out_size; (void)ws_size;
    (void)hipFuncSetAttribute((const void*)k1_qattn, hipFuncAttributeMaxDynamicSharedMemorySize, K1_LDS);
    (void)hipFuncSetAttribute((const void*)k2_oproj, hipFuncAttributeMaxDynamicSharedMemorySize, K2_LDS);
    K1Args a1;
    a1.x[0] = (const char*)d_in[0]; a1.k[0] = (const char*)d_in[1]; a1.v[0] = (const char*)d_in[2]; a1.wq[0] = (const char*)d_in[3];
    a1.x[1] = (const char*)d_in[5]; a1.k[1] = (const char*)d_in[6]; a1.v[1] = (const char*)d_in[7]; a1.wq[1] = (const char*)d_in[8];
    a1.wo[0] = (const char*)d_in[4]; a1.wo[1] = (const char*)d_in[9]; a1.ows = (char*)d_ws;
    K2Args a2; a2.ows = (const char*)d_ws; a2.wo[0] = (const char*)d_in[4]; a2.wo[1] = (const char*)d_in[9]; a2.out = (char*)d_out;
    hipLaunchKernelGGL(k1_qattn, dim3(256), dim3(512), K1_LDS, stream, a1);
    hipLaunchKernelGGL(k2_oproj, dim3(240), dim3(512), K2_LDS, stream, a2);
}
```

```cpp
#include <hip/hip_runtime.h>
#include <hip/hip_bf16.h>

typedef short bf16x8 __attribute__((ext_vector_type(8)));
typedef short bf16x4 __attribute__((ext_vector_type(4)));
typedef float f32x4 __attribute__((ext_vector_type(4)));
typedef float f32x2 __attribute__((ext_vector_type(2)));
typedef __bf16 bf2_t __attribute__((ext_vector_type(2)));
typedef unsigned u32x4 __attribute__((ext_vector_type(4)));
typedef unsigned u32x2 __attribute__((ext_vector_type(2)));
typedef __attribute__((address_space(3))) char* lds_ptr;
typedef __attribute__((address_space(3))) const char* lds_cptr;
#define DI __device__ __forceinline__
#define LDSP(T, p) ((__attribute__((address_space(3))) T*)(p))

constexpr int NB = 16, SQ = 144, DM = 640, NH = 32, SK = 512, DH = 128, HD = NH * DH;
constexpr float QSCALE = 0.12751743082459868f;

#define MFMA32(a, b, c) __builtin_amdgcn_mfma_f32_16x16x32_bf16((a), (b), (c), 0, 0, 0)
#define MFMA16(a, b, c) __builtin_amdgcn_mfma_f32_16x16x16bf16_1k((a), (b), (c), 0, 0, 0)

#define MFMA32T(acc, a, b) asm volatile("v_mfma_f32_16x16x32_bf16 %0, %1, %2, %0" : "+v"(acc) : "v"(a), "v"(b))
#define MFMA32Z(acc, a, b) asm volatile("v_mfma_f32_16x16x32_bf16 %0, %1, %2, 0" : "=&v"(acc) : "v"(a), "v"(b))
#define MFMA16T(acc, a, b) asm volatile("v_mfma_f32_16x16x16_bf16 %0, %1, %2, %0" : "+v"(acc) : "v"(a), "v"(b))
#define MFMA16Z(acc, a, b) asm volatile("v_mfma_f32_16x16x16_bf16 %0, %1, %2, 0" : "=&v"(acc) : "v"(a), "v"(b))
#define MFMA_PAD() asm volatile("s_nop 7\n\ts_nop 3" ::: "memory")
DI unsigned cvtpk(float lo, float hi) { f32x2 v = {lo, hi}; bf2_t b = __builtin_convertvector(v, bf2_t); return __builtin_bit_cast(unsigned, b); }
DI bf16x8 ld128(lds_cptr p) { return *LDSP(const bf16x8, p); }
DI bf16x4 ldtr(lds_cptr p) { return __builtin_amdgcn_ds_read_tr16_b64_v4i16(LDSP(bf16x4, (lds_ptr)p)); }
DI bf16x8 cat(bf16x4 lo, bf16x4 hi) { return __builtin_shufflevector(lo, hi, 0, 1, 2, 3, 4, 5, 6, 7); }
DI int fsw(int u) { return (((u ^ (u >> 1)) & 1) << 1) | ((u >> 1) & 1); }
DI unsigned rfl(unsigned x) { return __builtin_amdgcn_readfirstlane(x); }

#define DIAG_LDS 160768
#define WAIT_BARL(N, L) asm volatile("s_waitcnt vmcnt(%1) lgkmcnt(%0)\n\ts_barrier" :: "i"(L), "i"(N) : "memory")
#define DQ(x)
#define WAIT_BAR(N) WAIT_BARL(N, 0)
#define STAMP(i)
constexpr int RG = 16896, HB = 4 * RG, KE = 0, KO = RG, VE = 2 * RG, VO = 3 * RG;
constexpr int X0 = 2 * HB;
constexpr int X1 = VO + 8448;
constexpr int X1X = X0 + 18432;
constexpr int PXO = X1X + 2048;
constexpr int STO = PXO + 4096;
constexpr int K1_LDS = STO + 1024;
#define SB() __builtin_amdgcn_sched_barrier(0)
#define PPONG 0
#define QPRIO 1
#define APRIO 1
#define PRIO(x) __builtin_amdgcn_s_setprio(x)
#define TS0 5
#define PFM 0
#define VPOS 0
#define KSLOT(m) ((m) == 0 ? 0 : (m) == 4 ? 1 : (m) == 8 ? 2 : (m) == 12 ? 3 : -1)
#define PIN(x) asm volatile("" : "+v"(x))
#define DMAP(POL, m0b, M0OFF, vo, sb, IMM) \
    asm volatile("s_add_u32 m0, %0, %4\n\ts_nop 0\n\tglobal_load_lds_dwordx4 %1, %2 offset:%3" POL :: "s"(m0b), "v"(vo), "s"(sb), "i"(IMM), "i"(M0OFF) : "memory", "scc")
#define QPOL ""

struct K1Args { const char* x[2]; const char* k[2]; const char* v[2]; const char* wq[2]; const char* wo[2]; char* ows; };

struct AttnCtx {
    lds_cptr lds; unsigned ldk, ldv;
    unsigned ka[4], va[4], vax, pxr, pxw, kvo[2], vvo, pwk, pwv;
    const char* kp; const char* vp; const char* knext;
    int pc0, pc1;
    int rot;
};
DI int toff(const AttnCtx& c, const int t) { return ((t + c.rot) & 7) * 16384; }
#define KVPOL " nt"
template <int SO, int J> DI void dma_k(const AttnCtx& c, const char* kt) { DMAP(KVPOL, c.ldk, SO + (J & 1) * 896, c.kvo[J >> 1], kt, (J >> 1) * 2048 + (J & 1) * 128); }
template <int SO, int J> DI void dma_v(const AttnCtx& c, const char* vt) { DMAP(KVPOL, c.ldv, SO + J * 32, c.vvo, vt, J * 1024); }
#define DMA_K(c, SO, t, j) do { switch (j) { case 0: dma_k<SO, 0>(c, t); break; case 1: dma_k<SO, 1>(c, t); break; case 2: dma_k<SO, 2>(c, t); break; default: dma_k<SO, 3>(c, t); break; } } while (0)
#define DMA_V(c, SO, t, j) do { switch (j) { case 0: dma_v<SO, 0>(c, t); break; case 1: dma_v<SO, 1>(c, t); break; case 2: dma_v<SO, 2>(c, t); break; default: dma_v<SO, 3>(c, t); break; } } while (0)
DI bf16x8 kfrag(const AttnCtx& c, int so, int n) { return ld128(c.lds + c.ka[n >> 2] + (so + (n & 3) * 512)); }
DI bf16x4 vtr(const AttnCtx& c, int so, int i, int dt) { return ldtr(c.lds + c.va[i] + (so + (dt >> 1) * 256 + (dt & 1) * 32)); }
DI bf16x4 vxtr(const AttnCtx& c, int so, int kt, int d) { return ldtr(c.lds + c.vax + (so + kt * 4224 + d * 32)); }
DI float xmax(float m) {
    auto a = __builtin_amdgcn_permlane32_swap(__float_as_uint(m), __float_as_uint(m), false, false);
    m = fmaxf(__uint_as_float(a[0]), __uint_as_float(a[1]));
    auto b = __builtin_amdgcn_permlane16_swap(__float_as_uint(m), __float_as_uint(m), false, false);
    return fmaxf(__uint_as_float(b[0]), __uint_as_float(b[1]));
}
DI float xsum(float m) {
    auto a = __builtin_amdgcn_permlane32_swap(__float_as_uint(m), __float_as_uint(m), false, false);
    m = __uint_as_float(a[0]) + __uint_as_float(a[1]);
    auto b = __builtin_amdgcn_permlane16_swap(__float_as_uint(m), __float_as_uint(m), false, false);
    return __uint_as_float(b[0]) + __uint_as_float(b[1]);
}
DI float max4(const f32x4 v) { return fmaxf(fmaxf(v[0], v[1]), fmaxf(v[2], v[3])); }
#define EXP4(v) do { v[0] = __builtin_amdgcn_exp2f(v[0]); v[1] = __builtin_amdgcn_exp2f(v[1]); v[2] = __builtin_amdgcn_exp2f(v[2]); v[3] = __builtin_amdgcn_exp2f(v[3]); PIN(v); } while (0)
#define EXP2(v, h) do { v[2 * (h)] = __builtin_amdgcn_exp2f(v[2 * (h)]); v[2 * (h) + 1] = __builtin_amdgcn_exp2f(v[2 * (h) + 1]); PIN(v); } while (0)
#define SUM4(v, ls) do { ls += v[0]; ls += v[1]; ls += v[2]; ls += v[3]; PIN(ls); } while (0)
#define PK4(v, d) do { d = u32x2{cvtpk(v[0], v[1]), cvtpk(v[2], v[3])}; PIN(d); } while (0)
#define SUMPK(v, ls, d) do { ls += v[0]; ls += v[1]; ls += v[2]; ls += v[3]; d = u32x2{cvtpk(v[0], v[1]), cvtpk(v[2], v[3])}; PIN(ls); PIN(d); } while (0)

struct QCtx {
    lds_cptr lds; unsigned ldx[2], ldxx, ldw; int w;
    unsigned xo[3], wv, xa_own[2], xa_x, wa, wax;
    unsigned wk, xk;
    const char* xp; const char* wp;
};
constexpr int wslot(int sl) { return sl < 2 ? VE + sl * 8320 : (sl < 4 ? KO + (sl - 2) * 8320 : VO); }
template <int SL, int J> DI void w_dma(QCtx& q) {
    DMAP(QPOL, q.ldw, wslot(SL) + J * 1040, q.wv, q.wp + q.wk + J * (4 * HD * 2), 0);
    if (J == 1) { q.wk += 32 * HD * 2; if (q.wk >= DM * HD * 2) q.wk = 0; }
}
template <int ST, int J> DI void x_dma(QCtx& q) {
    if (J < 2) DMAP(QPOL, q.ldx[ST], J * 1024, q.xo[J], q.xp + q.xk, 0);
    else {
        asm volatile("s_add_u32 m0, %0, %3\n\ts_nop 0\n\tglobal_load_lds_dword %1, %2" QPOL :: "s"(q.ldxx), "v"(q.xo[2]), "s"(q.xp + q.xk), "i"(ST * 2048) : "memory", "scc");
        q.xk += 128; if (q.xk >= DM * 2) q.xk = 0;
    }
}
DI int opaque_lane() { int l; asm volatile("v_mbcnt_lo_u32_b32 %0, -1, 0\n\tv_mbcnt_hi_u32_b32 %0, -1, %0" : "=v"(l)); return l; }
DI void x_setup(QCtx& q) {
    const int l = opaque_lane(), p = l & 3, row8 = (l >> 2) & 7, hf = l >> 5, w = q.w;
#pragma unroll
    for (int j = 0; j < 2; ++j) q.xo[j] = (16 * w + 8 * j + row8) * (DM * 2) + hf * 64 + ((p ^ fsw((j << 1) | (row8 >> 2))) * 16);
    q.xo[2] = (128 + 8 * (w >> 2) + 4 * (w & 1) + (l >> 4)) * (DM * 2) + ((w >> 1) & 1) * 64 + (((row8 & 3) ^ fsw(2 * (w >> 2) + (w & 1))) * 16) + p * 4;
}
DI void w_setup(QCtx& q) {
    const int l = opaque_lane();
    q.wv = (8 * (q.w & 3) + ((l & 15) >> 2)) * (HD * 2) + (l >> 4) * 64 + (l & 3) * 16;
}
template <bool NT> DI void touch(const char* p, const unsigned off, const unsigned ldsd) {
    if (NT) asm volatile("s_mov_b32 m0, %2\n\ts_nop 0\n\tglobal_load_lds_dword %0, %1 nt" :: "v"(off), "s"(p), "s"(ldsd) : "memory");
    else asm volatile("s_mov_b32 m0, %2\n\ts_nop 0\n\tglobal_load_lds_dword %0, %1" :: "v"(off), "s"(p), "s"(ldsd) : "memory");
}
struct Seam { unsigned ldsd, to[2], kl; const char* tb; };

#define PACE 6
DI void pace(int n) { asm volatile("s_cmp_eq_u32 %0, 0\n\ts_cbranch_scc1 2f\n1:\n\ts_sleep 1\n\ts_sub_u32 %0, %0, 1\n\ts_cmp_lg_u32 %0, 0\n\ts_cbranch_scc1 1b\n2:" : "+s"(n) :: "scc"); }
#define NOCLAIM 2
#define SEAM 2
#define KST 14
#define VST 17
#define WOQ1 0
constexpr int SEAMX = SEAM == 2 ? (WOQ1 ? 0 : 2) : (SEAM == 1 ? 4 : 0);
constexpr int TSN = SEAM == 2 && !WOQ1 ? 2 : 4;
template <int SLOT, bool FIRST, bool LAST, bool PRE = false>
DI void fast_tile(const int t, const AttnCtx& c, const bf16x8 (&qf)[3][4], f32x4 (&o)[2][8], f32x4 (&ox)[2], f32x4 (&negm)[3], float (&lsum)[3], bf16x8 (&kf)[4],
                  QCtx& nq, const Seam& sm) {
    constexpr int SO = SLOT * RG, SN = (SLOT ^ 1) * RG;
    DQ(nq);
    const f32x4 z4 = {0.f, 0.f, 0.f, 0.f};
    f32x4 s[2][4], sx, sr; u32x2 ph[2][4], px; bf16x4 vf[5][2]; bf16x8 pxf[2], kr[4]; bf16x4 vx[2][2];
    if (FIRST) {
#pragma unroll
        for (int ks = 0; ks < 4; ++ks) kr[ks] = ld128(c.lds + sm.kl + ks * 512);
    }
    const char* kn = (SLOT == 0 && !FIRST && t == 6) ? c.knext : c.kp + toff(c, t + 2);
    const char* vn = c.vp + toff(c, t + 1);
#pragma unroll
    for (int n = 0; n < 16; ++n) {
        const int i = n >> 2, ks = n & 3;
        s[0][i] = MFMA32(kf[ks], qf[0][ks], ks ? s[0][i] : ((FIRST && i == 0) ? z4 : negm[0]));
        if (n == 4) EXP2(sx, 0);
        if (n == 5) EXP2(s[0][0], 0);
        if (n == 6) EXP2(s[1][0], 0);
        if (n == 7) SUM4(sx, lsum[2]);
        if (n == 8) SUM4(s[0][0], lsum[0]);
        if (n == 9) EXP2(s[0][1], 0);
        if (n == 10) SUM4(s[1][0], lsum[1]);
        if (n == 11) EXP2(s[1][1], 0);
        if (n == 12) SUM4(s[0][1], lsum[0]);
        if (n == 13) SUM4(s[1][1], lsum[1]);
        if (n >= 14) { vf[n - 14][0] = vtr(c, SO, 0, n - 14); vf[n - 14][1] = vtr(c, SO, 1, n - 14); }
        SB();
        s[1][i] = MFMA32(kf[ks], qf[1][ks], ks ? s[1][i] : ((FIRST && i == 0) ? z4 : negm[1]));
        if (i == 0) { SB(); sx = MFMA32(kf[ks], qf[2][ks], ks ? sx : (FIRST ? z4 : negm[2])); if (FIRST) sr = MFMA32(kr[ks], qf[2][ks], ks ? sr : z4); }
        if (n < 12) kf[ks] = kfrag(c, SO, n + 4);
        if (n == 4) EXP2(sx, 1);
        if (n == 5) EXP2(s[0][0], 1);
        if (n == 6) EXP2(s[1][0], 1);
        if (n == 7) { PK4(sx, px); *LDSP(u32x2, (lds_ptr)c.lds + c.pxw) = px; }
        if (n == 8) PK4(s[0][0], ph[0][0]);
        if (n == 9) EXP2(s[0][1], 1);
        if (n == 10) PK4(s[1][0], ph[1][0]);
        if (n == 11) EXP2(s[1][1], 1);
        if (n == 12) PK4(s[0][1], ph[0][1]);
        if (n == 13) PK4(s[1][1], ph[1][1]);
        if (n == 14) { EXP2(s[0][2], 0); vf[2][0] = vtr(c, SO, 0, 2); }
        if (n == 15) { EXP2(s[0][2], 1); vf[2][1] = vtr(c, SO, 1, 2); }
        if (LAST && n == 12) *LDSP(float, (lds_ptr)c.lds + STO + nq.w * 64 + ((c.pxr >> 2) & 60)) = xsum(lsum[2]);
        if ((!FIRST || !PFM) && !LAST && (n & 3) == VPOS) DMA_V(c, SN, vn, n >> 2);
        if (LAST && !SEAM) {
            if (n == 0) w_dma<0, 0>(nq); if (n == 1) w_dma<0, 1>(nq); if (n == 2) x_dma<0, 0>(nq); if (n == 3) x_dma<0, 1>(nq);
            if (n == 7) x_dma<0, 2>(nq); if (n == 8) w_dma<1, 0>(nq); if (n == 10) w_dma<1, 1>(nq);
        }
        if (LAST && SEAM) { if (n == 0) w_dma<0, 0>(nq); if (n == 4) w_dma<0, 1>(nq); if (n == 8) w_dma<1, 0>(nq); if (n == 12) w_dma<1, 1>(nq); }
        if (APRIO) { if (n == 1) PRIO(2); if (n == 5) PRIO(1); if (n == 9) PRIO(0); }
        SB();
        if (FIRST && n == 3) {
            const float m0 = xmax(max4(s[0][0])), m1 = xmax(max4(s[1][0])), m2 = xmax(max4(sr));
            negm[0] = f32x4{-m0, -m0, -m0, -m0}; negm[1] = f32x4{-m1, -m1, -m1, -m1}; negm[2] = f32x4{-m2, -m2, -m2, -m2};
            s[0][0] -= m0; s[1][0] -= m1; sx -= m2;
            SB();
        }
        if (PACE && n == 12) pace(FIRST ? c.pc0 : (t == 1 ? c.pc1 : 0));
        if (n == 13) { if (FIRST && PFM) WAIT_BAR(0); else if (LAST) WAIT_BAR(SEAM ? 7 : 11); else WAIT_BAR(8); if (APRIO) PRIO(3); SB(); }
    }
#pragma unroll
    for (int m = 0; m < 16; ++m) {
        const int pr = m >> 3, dt = m & 7, mm = m + 4;
        const bf16x8 a = cat(vf[m % 5][0], vf[m % 5][1]);
        const u32x4 p0 = __builtin_shufflevector(ph[0][2 * pr], ph[0][2 * pr + 1], 0, 1, 2, 3), p1 = __builtin_shufflevector(ph[1][2 * pr], ph[1][2 * pr + 1], 0, 1, 2, 3);
        if (FIRST && pr == 0) MFMA32Z(o[0][dt], a, p0); else MFMA32T(o[0][dt], a, p0);
        if (m == 0) vf[3][0] = vtr(c, SO, 0, 3);
        if (m < 12) vf[mm % 5][0] = vtr(c, SO, 2 * (mm >> 3), mm & 7);
        if (m == 0) EXP2(s[1][2], 0);
        if (m == 1) EXP2(s[0][3], 0);
        if (m == 2) EXP2(s[1][3], 0);
        if (m == 3) SUM4(s[0][2], lsum[0]);
        if (m == 4) SUM4(s[1][2], lsum[1]);
        if (m == 5) PK4(s[0][3], ph[0][3]);
        if (m == 6) PK4(s[1][3], ph[1][3]);
        if (m == 7 || m == 8) vx[m - 7][0] = vxtr(c, SO, 0, m - 7);
        if (m == 10 || m == 11) vx[m - 10][0] = vxtr(c, SO, 2, m - 10);
        if (!LAST && m >= 14) kf[2 * (m - 14)] = kfrag(c, SN, 2 * (m - 14));
        SB();
        if (FIRST && pr == 0) MFMA32Z(o[1][dt], a, p1); else MFMA32T(o[1][dt], a, p1);
        if (m == 0) vf[3][1] = vtr(c, SO, 1, 3);
        if (m < 12) vf[mm % 5][1] = vtr(c, SO, 2 * (mm >> 3) + 1, mm & 7);
        if (m == 0) EXP2(s[1][2], 1);
        if (m == 1) EXP2(s[0][3], 1);
        if (m == 2) EXP2(s[1][3], 1);
        if (m == 3) PK4(s[0][2], ph[0][2]);
        if (m == 4) PK4(s[1][2], ph[1][2]);
        if (m == 5) SUM4(s[0][3], lsum[0]);
        if (m == 6) SUM4(s[1][3], lsum[1]);
        if (m == 5 || m == 6) pxf[m - 5] = ld128(c.lds + c.pxr + (m - 5) * 1024);
        if (m == 7 || m == 8) vx[m - 7][1] = vxtr(c, SO, 1, m - 7);
        if (m == 10 || m == 11) vx[m - 10][1] = vxtr(c, SO, 3, m - 10);
        if (!LAST && m >= 14) kf[2 * (m - 14) + 1] = kfrag(c, SN, 2 * (m - 14) + 1);
        if (m == 9 || m == 10) { SB(); if (FIRST) MFMA32Z(ox[m - 9], cat(vx[m - 9][0], vx[m - 9][1]), pxf[0]); else MFMA32T(ox[m - 9], cat(vx[m - 9][0], vx[m - 9][1]), pxf[0]); }
        if (m == 13 || m == 14) { SB(); MFMA32T(ox[m - 13], cat(vx[m - 13][0], vx[m - 13][1]), pxf[1]); }
        if (!LAST && !PRE && KSLOT(m) >= 0) DMA_K(c, SO, kn, KSLOT(m));
        if (PRE) { if (m == 0) x_dma<0, 0>(nq); if (m == 4) x_dma<0, 1>(nq); if (m == 8) x_dma<0, 2>(nq); }
        if (LAST) {
            if (SEAM) { if (m == 0) w_dma<2, 0>(nq); if (m == 4) w_dma<2, 1>(nq); if (m == 8) w_dma<3, 0>(nq); if (m == 12) w_dma<3, 1>(nq); }
            else { if (m == 4) w_dma<2, 0>(nq); if (m == 7) w_dma<2, 1>(nq); if (m == 8) w_dma<3, 0>(nq); if (m == 9) w_dma<3, 1>(nq); }
            if (m == 14) x_dma<1, 0>(nq); if (m == 15) { x_dma<1, 1>(nq); x_dma<1, 2>(nq); }
        }
        if (APRIO) { if (m == 1) PRIO(2); if (m == 5) PRIO(1); if (m == 9) PRIO(0); }
        SB();
        if (PACE && m == 12) pace(FIRST ? c.pc1 : (t == 1 ? c.pc1 : 0));
        if (m == 13) { if (FIRST && PFM) WAIT_BAR(4); else if (LAST) WAIT_BAR(6); else if (PRE) WAIT_BAR(7); else WAIT_BAR(8); if (APRIO) PRIO(3); SB(); }
    }
}

#define OPOL " sc1"
DI void ostore(char* p, const u32x4 v) { asm volatile("global_store_dwordx4 %0, %1, off" OPOL "\n\ts_nop 1" :: "v"(p), "v"(v) : "memory"); }
struct Pend { u32x4 pk[9]; char* ob[3]; unsigned lo; unsigned long long em; };
template <int I> DI void pstore(const Pend& p) { asm volatile("global_store_dwordx4 %0, %1, %2 offset:%3" OPOL "\n\ts_nop 1" :: "v"(p.lo), "v"(p.pk[I]), "s"(p.ob[I >> 2]), "i"((I & 3) * 1024) : "memory"); }
template <int I> DI void pstore_m(const Pend& p) {
    asm volatile("s_mov_b64 exec, %4\n\tglobal_store_dwordx4 %0, %1, %2 offset:%3" OPOL "\n\ts_mov_b64 exec, -1\n\ts_nop 0" :: "v"(p.lo), "v"(p.pk[I]), "s"(p.ob[I >> 2]), "i"((I & 3) * 1024), "s"(p.em) : "memory");
}
DI u32x4 opack(const f32x4 u, const f32x4 v, const float il) {
    return u32x4{cvtpk(u[0] * il, u[1] * il), cvtpk(u[2] * il, u[3] * il), cvtpk(v[0] * il, v[1] * il), cvtpk(v[2] * il, v[3] * il)};
}
DI bf16x8 qfrag(const f32x4 t0, const f32x4 t1) {
    unsigned a0 = cvtpk(t0[0] * QSCALE, t0[1] * QSCALE), a1 = cvtpk(t0[2] * QSCALE, t0[3] * QSCALE);
    unsigned b0 = cvtpk(t1[0] * QSCALE, t1[1] * QSCALE), b1 = cvtpk(t1[2] * QSCALE, t1[3] * QSCALE);
    auto r0 = __builtin_amdgcn_permlane32_swap(a0, b0, false, false);
    auto r1 = __builtin_amdgcn_permlane32_swap(a1, b1, false, false);
    auto s0 = __builtin_amdgcn_permlane16_swap(r0[0], r0[1], false, false);
    auto s1 = __builtin_amdgcn_permlane16_swap(r1[0], r1[1], false, false);
    u32x4 u = {s0[0], s1[0], s0[1], s1[1]};
    return __builtin_bit_cast(bf16x8, u);
}

DI void slow_frag(const bf16x8 (&tq)[4], const char* kp, const char* vp, char* dst) {
    const int l = opaque_lane(), r = l & 15, g = l >> 4;
    float m = -3.0e38f, ls = 0.f; f32x4 acc[2] = {{0, 0, 0, 0}, {0, 0, 0, 0}};
#pragma unroll 1
    for (int kt = 0; kt < 32; ++kt) {
        f32x4 s = {0, 0, 0, 0};
#pragma unroll
        for (int ks = 0; ks < 4; ++ks) s = MFMA32(*(const bf16x8*)(kp + (kt * 16 + r) * 256 + ks * 64 + g * 16), tq[ks], s);
        const float mn = fmaxf(m, xmax(max4(s))), f = __builtin_amdgcn_exp2f(m - mn);
        m = mn; s -= mn; ls *= f; u32x2 pk;
        EXP4(s); SUMPK(s, ls, pk);
#pragma unroll
        for (int d = 0; d < 2; ++d) {
            const short* vq = (const short*)(vp + (kt * 16 + 4 * g) * 256 + (d * 16 + r) * 2);
            const bf16x4 a = {vq[0], vq[128], vq[256], vq[384]};
            acc[d] = MFMA16(a, __builtin_bit_cast(bf16x4, pk), acc[d] * f);
        }
    }
    ostore(dst + l * 16, opack(acc[0], acc[1], __builtin_amdgcn_rcpf(xsum(ls))));
}

DI void slow_q(bf16x8 (&tq)[4], const char* xr, const char* wc) {
    const int l = opaque_lane(), r = l & 15, g = l >> 4;
#pragma unroll 1
    for (int ks = 0; ks < 4; ++ks) {
        f32x4 a0 = {0, 0, 0, 0}, a1 = {0, 0, 0, 0};
#pragma unroll 1
        for (int kk = 0; kk < 20; ++kk) {
            const bf16x8 xb = *(const bf16x8*)(xr + r * (DM * 2) + kk * 64 + g * 16);
            const short* wp = (const short*)(wc + (size_t)(32 * kk + 8 * g) * (HD * 2) + (32 * ks + r) * 2);
            bf16x8 w0, w1;
#pragma unroll
            for (int j = 0; j < 8; ++j) { w0[j] = wp[j * HD]; w1[j] = wp[j * HD + 16]; }
            a0 = MFMA32(w0, xb, a0); a1 = MFMA32(w1, xb, a1);
        }
        tq[0] = tq[1]; tq[1] = tq[2]; tq[2] = tq[3]; tq[3] = qfrag(a0, a1);
    }
}
DI bf16x4 wtr(const QCtx& q, const int sl, const int j, const int hi) {
    const int im = wslot(sl) + ((j == 0 || j == 9) ? (j & 1) * 32 : ((j - 1) >> 1) * 256 + ((j - 1) & 1) * 32) + hi * 1040;
    return ldtr(q.lds + ((j == 0 || j == 9) ? q.wax : q.wa) + im);
}
DI bf16x8 xfrag(const QCtx& q, const int s, const int i) {
    return i < 2 ? ld128(q.lds + q.xa_own[(s >> 1) & 1] + ((s & 1) * 512 + i * 2048)) : ld128(q.lds + q.xa_x + (((s >> 1) & 1) * 2048 + (s & 1) * 512));
}
DI void q_addr(QCtx& q, const int hb) {
    const int l = opaque_lane(), r = l & 15, wq = q.w & 3;
    const unsigned xa = (r >> 3) * 1024 + (r & 7) * 64 + (((l >> 4) ^ fsw((r >> 2) & 3)) * 16);
    q.xa_own[0] = X0 + xa + wq * 4096; q.xa_own[1] = X1 + (wq < 2 ? HB : 0) + xa + (wq & 1) * 4096; q.xa_x = X0 + 16384 + xa;
    q.wa = hb + (l >> 4) * 2080 + (r >> 2) * 64 + (l & 3) * 8; q.wax = q.wa + wq * 256;
}
DI void q_first(const QCtx& q, bf16x4 (&af)[10][2], bf16x8 (&xb)[3]) {
    xb[2] = xfrag(q, 0, 2);
#pragma unroll
    for (int j = 0; j < 10; ++j) { const int jj = j == 0 ? 0 : (j == 1 ? 9 : j - 1); if (jj != 8) { af[jj][0] = wtr(q, 0, jj, 0); af[jj][1] = wtr(q, 0, jj, 1); } if (j == 1) { xb[0] = xfrag(q, 0, 0); xb[1] = xfrag(q, 0, 1); } }
}
template <int I> DI void pf_load(const AttnCtx& c, u32x4& d) {
    if (I < 4 || I >= 8) asm volatile("global_load_dwordx4 %0, %1, %2 offset:%3 nt" : "=v"(d) : "v"(c.vvo), "s"(c.vp + toff(c, I >= 8)), "i"((I & 3) * 1024) : "memory");
    else asm volatile("global_load_dwordx4 %0, %1, %2 offset:%3 nt" : "=v"(d) : "v"(c.kvo[(I & 3) >> 1]), "s"(c.kp + toff(c, 1)), "i"(((I & 3) >> 1) * 2048 + (I & 1) * 128) : "memory");
}
template <int I> DI void pf_put(const unsigned pw, const u32x4& d) {
    asm volatile("ds_write_b128 %0, %1 offset:%2" :: "v"(pw), "v"(d), "i"(I < 4 ? I * 1056 : (I < 8 ? KO + (I & 3) * 1024 : RG + (I & 3) * 1056)) : "memory");
}
constexpr int q_w(int s) { return s >= 0 && s < 16 ? 2 : 0; }
constexpr int q_x(int s) { return s >= 0 && s < 17 && (s & 1) ? 3 : 0; }
#define KSPR 1
#define STODD 1
constexpr int q_st(int s) { return s < 0 || s > 16 ? 0 : (STODD ? ((s & 1) ? (s == 1 ? 2 : 1) : 0) : !(s & 1)); }
constexpr int q_post(int s) { return q_st(s) + (s > 0 && s < 16 && (s & 1) && (PFM || (s >= TS0 && s < TS0 + TSN)) ? 2 : 0) + (SEAM == 2 ? (KSPR ? (s <= KST && s > KST - 8 && !((KST - s) & 1)) : (s == KST ? 4 : 0)) : 0) + (SEAM == 2 && s == VST ? 4 : 0); }
constexpr int q_all(int s) { return q_w(s) + q_x(s) + q_post(s); }
constexpr int q_wait(int s) { return (s & 1) ? q_post(s - 2) + q_all(s - 1) : q_x(s - 3) + q_post(s - 3) + q_all(s - 2) + q_all(s - 1); }
template <int N, int L> DI void wait_bar(QCtx& dq) { WAIT_BARL(N, L); }
template <int S>
DI void q_step(QCtx& q, const AttnCtx& ac, const Pend& pd, f32x4 (&qa)[2][8], f32x4 (&qx)[2], bf16x4 (&af)[10][2], const bf16x8 (&xc)[3], bf16x8 (&xn)[3],
               bf16x8 (&qf)[3][4], bf16x8 (&kf)[4], const unsigned qxa, u32x4 (&pf)[12], const Seam& sm) {
    constexpr int SL = S % 5, SN = (S + 1) % 5, SP = (S + 4) % 5, BJ = S == 19 ? 3 : 1, PI = (S & 1) && S >= 5 && S <= 15 ? S - 5 : 0;
#pragma unroll
    for (int j = 0; j < 9; ++j) {
        const bf16x8 a0 = cat(af[j][0], af[j][1]);
        if (j > 0) { if (S == 0) MFMA32Z(qa[0][j - 1], a0, xc[0]); else MFMA32T(qa[0][j - 1], a0, xc[0]); }
        else { if (S == 0) MFMA32Z(qx[0], a0, xc[2]); else MFMA32T(qx[0], a0, xc[2]); }
        if (j == 0) af[8][0] = wtr(q, SL, 8, 0);
        if (S < 19) {
            if (j >= 2 && j < 7) { af[j - 2][0] = wtr(q, SN, j - 2, 0); af[j - 2][1] = wtr(q, SN, j - 2, 1); }
            if (j >= 7) af[j - 1][0] = wtr(q, SN, j - 1, 0);
        } else {
            if (j == 2) *LDSP(bf16x8, (lds_ptr)q.lds + qxa + q.w * 1024) = qfrag(qx[0], qx[1]);
            if (j == 4 || j == 6 || j == 8) qf[0][(j - 4) >> 1] = qfrag(qa[0][j - 4], qa[0][j - 3]);
            if (j >= 4 && j < 8) qf[2][j - 4] = ld128(q.lds + qxa + (q.w >> 2) * 4096 + (j - 4) * 1024);
        }
        SB();
        if (j > 0) { if (S == 0) MFMA32Z(qa[1][j - 1], a0, xc[1]); else MFMA32T(qa[1][j - 1], a0, xc[1]); }
        else { const bf16x8 a1 = cat(af[9][0], af[9][1]); if (S == 0) MFMA32Z(qx[1], a1, xc[2]); else MFMA32T(qx[1], a1, xc[2]); }
        if (j == 0) af[8][1] = wtr(q, SL, 8, 1);
        if (S < 19) {
            if (j == 2) { af[9][0] = wtr(q, SN, 9, 0); af[9][1] = wtr(q, SN, 9, 1); }
            if (j == 3) xn[2] = xfrag(q, S + 1, 2);
            if (j == 4 || j == 5) xn[j - 4] = xfrag(q, S + 1, j - 4);
            if (j == 6) { af[5][0] = wtr(q, SN, 5, 0); af[5][1] = wtr(q, SN, 5, 1); }
            if (j >= 7) af[j - 1][1] = wtr(q, SN, j - 1, 1);
        } else {
            if (j == 4 || j == 6 || j == 8) qf[1][(j - 4) >> 1] = qfrag(qa[1][j - 4], qa[1][j - 3]);
            if (j >= 5) kf[j - 5] = kfrag(ac, 0, j - 5);
        }
        if (S < 16) { if (j == 2) w_dma<SP, 0>(q); if (j == 3) w_dma<SP, 1>(q); }
        if (S < 17 && (S & 1)) {
            if (j == 4) x_dma<((S >> 1) & 1), 0>(q);
            if (j == 5) x_dma<((S >> 1) & 1), 1>(q);
            if (j == 6) x_dma<((S >> 1) & 1), 2>(q);
            if (S >= TS0 && S < TS0 + TSN) { if (j >= 7) touch<false>(sm.tb + (S - TS0 + j - 7) * 1280, sm.to[0], sm.ldsd); }
            else if (PFM) { if (j == 7) pf_load<PI>(ac, pf[PI]); if (j == 8) pf_load<PI + 1>(ac, pf[PI + 1]); }
        }
        if (!STODD && S < 17 && !(S & 1) && j == (S == KST && SEAM == 2 ? 4 : 6)) { if (NOCLAIM == 2) pstore_m<(S >> 1)>(pd); else pstore<(S >> 1)>(pd); }
        if (STODD && S < 17 && (S & 1) && j == 7) { if (NOCLAIM == 2) pstore_m<(S >> 1)>(pd); else pstore<(S >> 1)>(pd); }
        if (STODD && S == 1 && j == 8) { if (NOCLAIM == 2) pstore_m<8>(pd); else pstore<8>(pd); }
        if (SEAM == 2 && !KSPR && S == KST && j >= 5) DMA_K(ac, KE, ac.kp + toff(ac, 0), j - 5);
        if (SEAM == 2 && KSPR && S <= KST && S > KST - 8 && !((KST - S) & 1) && j == 5) DMA_K(ac, KE, ac.kp + toff(ac, 0), 3 - ((KST - S) >> 1));
        if (!PFM && S == (SEAM == 2 ? VST : 18) && j >= 2 && !(j & 1)) DMA_V(ac, 0, ac.vp + toff(ac, 0), (j - 2) >> 1);
        if (!PFM && S == 19 && j >= 5) DMA_K(ac, KO, ac.kp + toff(ac, 1), j - 5);
        if (PFM && S == 18) { if (j == 3) pf_put<0>(ac.pwv, pf[0]); if (j == 4) pf_put<1>(ac.pwv, pf[1]); if (j == 5) pf_put<2>(ac.pwv, pf[2]); if (j == 7) pf_put<3>(ac.pwv, pf[3]); }
        if (PFM && S == 19) {
            if (j == 4) pf_put<4>(ac.pwk, pf[4]); if (j == 5) { pf_put<5>(ac.pwk, pf[5]); pf_put<6>(ac.pwk, pf[6]); } if (j == 6) pf_put<7>(ac.pwk, pf[7]);
            if (j == 7) { pf_put<8>(ac.pwv, pf[8]); pf_put<9>(ac.pwv, pf[9]); } if (j == 8) { pf_put<10>(ac.pwv, pf[10]); pf_put<11>(ac.pwv, pf[11]); }
        }
        if (QPRIO) { if (j == 3) PRIO(2); if (j == 5) PRIO(1); if (j == 7) PRIO(0); }
        SB();
        if (j == BJ) {
            if (S == 0) wait_bar<7 + SEAMX, 15>(q);
            else if (S == 1) wait_bar<q_wait(1) + SEAMX, 8>(q);
            else if (S == 2) wait_bar<3 + SEAMX + q_all(0) + q_all(1), 15>(q);
            else if (S == 19) wait_bar<PFM ? 0 : (SEAM == 2 ? 4 : 9), 0>(q);
            else wait_bar<q_wait(S), (S & 1) ? 8 : 15>(q);
            if (QPRIO) PRIO(3);
            SB();
        }
    }
}

constexpr size_t DISCARD = (size_t)128 << 20;
#define TMASK ~0u
#define TSTR 1
#define TROT 0
#define PORD 2
#define KNX 1
#define STAG 0
#define SGRP ((idx >> 3) & 1)
#define KROT 1
__global__ __launch_bounds__(512, 2) void k1_qattn(const K1Args args) {
    extern __shared__ __attribute__((aligned(16))) char smem[];
    const lds_cptr lds = (lds_cptr)smem;
    const unsigned ldsb = (unsigned)(uintptr_t)smem;
    const unsigned long long tst_ = STAG ? __builtin_amdgcn_s_memrealtime() : 0;
    const int w = rfl(threadIdx.x >> 6), hd = w >> 2, wq = w & 3, hb = (hd ^ 1) * HB;
    const int xcd = blockIdx.x & 7, idx = blockIdx.x >> 3;
    const int br = xcd >> 2, b = ((xcd >> 1) & 1) * 8 + (idx & 7), h0 = (xcd & 1) * 16 + (idx >> 3) * 2 + hd;
    QCtx q;
    DQ(q);
    q.lds = lds; q.ldx[0] = ldsb + X0 + w * 2048; q.ldx[1] = ldsb + X1 + (w < 4 ? HB : 0) + (w & 3) * 2048; q.ldxx = ldsb + X0 + 16384 + w * 256; q.ldw = ldsb + hb + wq * 2080; q.w = w;
    q.xp = args.x[br] + (size_t)b * (SQ * DM * 2);
    q.wp = args.wq[br] + h0 * (DH * 2);
    {
        const int c0 = KROT ? ((idx & 7) * 5 / 4 + (idx >> 3) * 5 / 2) % 10 : 0;
        q.wk = c0 * (64 * HD * 2); q.xk = c0 * 128;
    }
    const size_t hoff = (size_t)(b * NH + h0) * (SK * DH * 2);
    constexpr size_t HSTEP = (size_t)8 * SK * DH * 2;
    char* const ob0 = args.ows + ((size_t)((br * NB + b) * 9) * NH + h0) * 4096;
    AttnCtx ac;
    ac.lds = lds; ac.ldk = ldsb + hb + wq * 4096; ac.ldv = ldsb + hb + VE + wq * 4224;
    ac.rot = TROT;
    Seam sm; sm.ldsd = ldsb + STO + 512;
    Pend pd;
    bf16x4 af[10][2]; bf16x8 xb0[3];
    {
        x_setup(q); w_setup(q);
        const int l = opaque_lane(), p = l & 3, row8 = (l >> 2) & 7, hf = l >> 5;
#pragma unroll
        for (int j = 0; j < 2; ++j) ac.kvo[j] = row8 * 256 + hf * 64 + ((p ^ fsw((j << 1) | (row8 >> 2))) * 16);
        const unsigned po = (l & 1) * (unsigned)HSTEP + (l & 2) * 32768;
        const char* k0 = args.k[br] + hoff + wq * 4096 + toff(ac, 0);
        x_dma<0, 0>(q); x_dma<0, 1>(q); x_dma<0, 2>(q); w_dma<0, 0>(q); w_dma<0, 1>(q); w_dma<1, 0>(q); w_dma<1, 1>(q); w_dma<2, 0>(q); w_dma<2, 1>(q);
        w_dma<3, 0>(q); w_dma<3, 1>(q);
        x_dma<1, 0>(q); x_dma<1, 1>(q); x_dma<1, 2>(q);
        if (SEAM == 1) { DMA_K(ac, KE, k0, 0); DMA_K(ac, KE, k0, 1); DMA_K(ac, KE, k0, 2); DMA_K(ac, KE, k0, 3); }
        if (SEAM == 2 && !WOQ1) { touch<true>(args.k[br] + hoff, po, sm.ldsd); touch<true>(args.v[br] + hoff, po, sm.ldsd); }
#pragma unroll
        for (int i = 0; i < 9; ++i) pd.pk[i] = u32x4{0, 0, 0, 0};
        pd.ob[0] = pd.ob[1] = pd.ob[2] = args.ows + DISCARD + blockIdx.x * 4096; pd.lo = l * 16; pd.em = 0;
        q_addr(q, hb);
        SB();
        if (STAG) { const unsigned long long t1_ = tst_ + (unsigned long long)(STAG) * (SGRP); while (__builtin_amdgcn_s_memrealtime() < t1_) __builtin_amdgcn_s_sleep(4); }
        WAIT_BAR(SEAM ? 9 + SEAMX : (PORD == 0 ? 9 : (PORD == 1 ? 15 : 13)));
        q_first(q, af, xb0);
    }
    for (int it = 0; it < 2; ++it) {
        STAMP(4 * it + 0);
        const int l = opaque_lane(), r = l & 15, g = l >> 4, q4 = r >> 2, p = l & 3, row8 = (l >> 2) & 7, hf = l >> 5;
        const char* kh = args.k[br] + hoff + it * HSTEP;
        const char* vh = args.v[br] + hoff + it * HSTEP;
        ac.kp = kh + wq * 4096; ac.vp = vh + wq * 4096; ac.knext = (KNX && it) ? q.xp + wq * 4096 : ac.kp + (it ? toff(ac, 7) : HSTEP + toff(ac, 0));
#pragma unroll
        for (int j = 0; j < 2; ++j) ac.kvo[j] = row8 * 256 + hf * 64 + ((p ^ fsw((j << 1) | (row8 >> 2))) * 16);
        ac.vvo = q4 * 256 + g * 64 + p * 16;
        ac.pc0 = 0; ac.pc1 = it ? 0 : PACE;
        ac.pwk = ac.ldk + l * 16; ac.pwv = ac.ldv + l * 16;
        sm.kl = hb + (r >> 3) * 2048 + (r & 7) * 64 + ((g ^ fsw((r >> 2) & 3)) * 16);
        ac.ka[0] = sm.kl + wq * 4096;
        {
            sm.to[0] = (unsigned)(((idx * 8 + w) * 40 + TSTR * (l < 10 / TSTR ? l : 10 / TSTR - 1)) * 128) & (0u - (unsigned)(it ^ WOQ1)) & TMASK;
            sm.tb = (it ^ WOQ1) ? args.wo[br] + (size_t)(xcd & 3) * (32 * 320 * 128) : q.xp;
        }
        bf16x8 qf[3][4], kf[4];
        {
            f32x4 qa[2][8], qx[2]; bf16x8 xb1[3]; u32x4 pf[12];
            const unsigned qxa = X0 + l * 16;
            SB();
#define QS2(S) q_step<S>(q, ac, pd, qa, qx, af, xb0, xb1, qf, kf, qxa, pf, sm); q_step<S + 1>(q, ac, pd, qa, qx, af, xb1, xb0, qf, kf, qxa, pf, sm)
            QS2(0); QS2(2);
            STAMP(12);
            QS2(4); QS2(6); QS2(8); QS2(10); QS2(12); QS2(14);
            STAMP(13);
            QS2(16); QS2(18);
            MFMA_PAD(); SB();
            qf[0][3] = qfrag(qa[0][6], qa[0][7]); qf[1][3] = qfrag(qa[1][6], qa[1][7]);
        }
        q.wp += (1 - it) * (8 * DH * 2);
        const unsigned vl = hb + VE + g * 1056 + q4 * 64 + p * 8;
#pragma unroll
        for (int i = 0; i < 4; ++i) { const unsigned kt = (unsigned)((i + wq) & 3); ac.ka[i] = sm.kl + kt * 4096; ac.va[i] = vl + kt * 4224; }
        ac.vax = vl + wq * 256; ac.pxr = PXO + hd * 2048 + l * 16; ac.pxw = ac.pxr + (wq >> 1) * 1024 + (wq & 1) * 8;
        f32x4 o[2][8], ox[2], negm[3]; float lsum[3] = {0.f, 0.f, 0.f};
        SB();
        if (PPONG && hd) asm volatile("s_barrier" ::: "memory");
        STAMP(4 * it + 1);
        fast_tile<0, true, false>(0, ac, qf, o, ox, negm, lsum, kf, q, sm);
        STAMP(14);
        for (int t = 1; t < (SEAM ? 5 : 7); t += 2) {
            fast_tile<1, false, false>(t, ac, qf, o, ox, negm, lsum, kf, q, sm);
            fast_tile<0, false, false>(t + 1, ac, qf, o, ox, negm, lsum, kf, q, sm);
        }
        if (SEAM) {
            fast_tile<1, false, false>(5, ac, qf, o, ox, negm, lsum, kf, q, sm);
            x_setup(q);
            const unsigned mk = (unsigned)it - 1u;
            q.xo[0] &= mk; q.xo[1] &= mk; q.xo[2] &= mk;
            SB();
            fast_tile<0, false, false, true>(6, ac, qf, o, ox, negm, lsum, kf, q, sm);
            STAMP(15);
            w_setup(q); q.wv &= mk;
        } else {
            STAMP(15);
            x_setup(q); w_setup(q);
        }
        SB();
        fast_tile<1, false, true>(7, ac, qf, o, ox, negm, lsum, kf, q, sm);
        STAMP(4 * it + 2);
        const int l2 = opaque_lane(), r2 = l2 & 15;
        if (SEAM == 2 && !WOQ1 && it == 0) {
            const char* tb = args.wo[br] + (size_t)(xcd & 3) * (32 * 320 * 128) + 2560; const unsigned to = (unsigned)(((idx * 8 + w) * 40 + (l2 < 10 ? l2 : 9)) * 128) & TMASK;
            touch<false>(tb, to, sm.ldsd); touch<false>(tb + 1280, to, sm.ldsd);
        }
        q_addr(q, hb);
        if (!PPONG) q_first(q, af, xb0);
        MFMA_PAD(); SB();
        char* ob = ob0 + it * (8 * 4096);
        float lt[3];
        lt[0] = xsum(lsum[0]); lt[1] = xsum(lsum[1]); lt[2] = 0.f;
#pragma unroll
        for (int i = 0; i < 4; ++i) lt[2] += *LDSP(const float, lds + STO + hd * 256 + i * 64 + r2 * 4);
#pragma unroll
        for (int a = 0; a < 2; ++a) {
            const float il = __builtin_amdgcn_rcpf(lt[a]);
#pragma unroll
            for (int s = 0; s < 4; ++s) {
                if (SEAM == 1 && !(s & 1)) { SB(); if (it == 0) DMA_K(ac, KE, ac.knext, 2 * a + (s >> 1)); SB(); }
                pd.pk[4 * a + s] = opack(o[a][2 * s], o[a][2 * s + 1], il);
            }
        }
        pd.pk[8] = opack(ox[0], ox[1], __builtin_amdgcn_rcpf(lt[2]));
        const unsigned bad = (__any(!(lt[0] < 4096.f)) ? 1u : 0u) | (__any(!(lt[1] < 4096.f)) ? 2u : 0u) | (__any(!(lt[2] < 4096.f)) ? 4u : 0u);
        pd.ob[0] = ob + ((bad & 1) ? DISCARD : 0) + (size_t)(2 * wq) * (NH * 4096); pd.ob[1] = ob + ((bad & 2) ? DISCARD : 0) + (size_t)(2 * wq + 1) * (NH * 4096);
        pd.ob[2] = ob + ((bad & 4) ? DISCARD : 0) + (size_t)8 * (NH * 4096) + wq * 1024; pd.lo = l2 * 16; pd.em = ~0ull;
        if (__builtin_expect(bad != 0, 0)) {
#pragma unroll 1
            for (int a = 0; a < 3; ++a) {
                if (!((bad >> a) & 1)) continue;
                const int qt = a < 2 ? 2 * wq + a : 8;
                bf16x8 tq[4] = {};
                slow_q(tq, q.xp + qt * (16 * DM * 2), args.wq[br] + (h0 + 8 * it) * (DH * 2));
#pragma unroll 1
                for (int s = a < 2 ? 0 : wq; s < (a < 2 ? 4 : wq + 1); ++s) slow_frag(tq, kh, vh + s * 64, ob + (size_t)qt * (NH * 4096) + s * 1024);
            }
            asm volatile("s_waitcnt vmcnt(0)" ::: "memory");
        }
        SB();
        if (PPONG) { if (!hd) asm volatile("s_barrier" ::: "memory"); q_first(q, af, xb0); }
        STAMP(4 * it + 3);
    }
    pstore<0>(pd); pstore<1>(pd); pstore<2>(pd); pstore<3>(pd); pstore<4>(pd); pstore<5>(pd); pstore<6>(pd); pstore<7>(pd); pstore<8>(pd);
    asm volatile("s_waitcnt vmcnt(0)" ::: "memory");
}

constexpr int K2_Q = 8 * 1056, K2_STG = 4 * K2_Q, K2_LDS = 147456 + 512;
struct K2Args { const char* ows; const char* wo[2]; char* out; };
#define TOUCH 0
#define K2_TSET 2
#define K2_TAHEAD 2
#define OUTPOL " nt"
#define WOPOL ""
struct K2Ctx { lds_cptr lds; unsigned ldsw, ldsd, wv[4], av, ba[2][4]; const char* wp; const char* ap; const char* tb; int w, part; };
DI void k2_touch(const K2Ctx& c, const int G) {
    if ((c.w & 3) == 3) return;
    const int j = (c.w & 3) * 64 + opaque_lane();
    unsigned off;
    if (c.w < 4) { int u = 6 * j + c.part; u = u < 1024 ? u : 1023; off = (unsigned)(((u >> 6) & 3) * 1024 + (4 * G + (u >> 8)) * 32 + ((u >> 1) & 31)) * (DM * 2) + (u & 1) * 128; }
    else { int v = 5 * j + c.part; v = v < 768 ? v : 767; off = (unsigned)(v >> 7) * (NH * 4096) + ((v >> 5) & 3) * 32768 + G * 4096 + (v & 31) * 128; }
    asm volatile("s_mov_b32 m0, %2\n\ts_nop 0\n\tglobal_load_lds_dword %0, %1" :: "v"(off), "s"(c.tb), "s"(c.ldsd) : "memory");
}
template <int SET, int J> DI void k2_dma(const K2Ctx& c, const int st) { DMAP(WOPOL, c.ldsw, SET * K2_STG + J * 1056, c.wv[J], c.wp + (size_t)st * (32 * DM * 2), 0); }
template <int I> DI void k2_lda(const K2Ctx& c, const int st, u32x4& d) {
    asm volatile("global_load_dwordx4 %0, %1, %2" : "=v"(d) : "v"(c.av), "s"(c.ap + st * 1024 + I * (NH * 4096)) : "memory");
}
template <int SET> DI bf16x8 k2_bfrag(const K2Ctx& c, const int n) {
    const unsigned b = c.ba[SET >> 1][n >> 1]; const int im = (SET & 1) * K2_STG + (n & 1) * 32;
    return cat(ldtr(c.lds + b + im), ldtr(c.lds + b + im + 4 * 1056));
}
template <int SET, int MODE>
DI void k2_step(const K2Ctx& c, const int st, f32x4 (&acc)[3][8], u32x4 (&af)[4][3], bf16x8 (&bw)[8]) {
    constexpr int SN = (SET + 1) & 3, SA = (SET + 3) & 3;
#pragma unroll
    for (int n = 0; n < 8; ++n) {
        MFMA32T(acc[0][n], bw[n], af[SET][0]); MFMA32T(acc[1][n], bw[n], af[SET][1]); MFMA32T(acc[2][n], bw[n], af[SET][2]);
        if (MODE != 4) {
            if (n == 2) { bw[0] = k2_bfrag<SN>(c, 0); bw[1] = k2_bfrag<SN>(c, 1); bw[2] = k2_bfrag<SN>(c, 2); }
            if (n > 2) bw[n] = k2_bfrag<SN>(c, n);
        }
        if (MODE == 0 && SET == K2_TSET && n == 2) { if (TOUCH && (st >> 2) + K2_TAHEAD < 8) k2_touch(c, (st >> 2) + K2_TAHEAD); }
        if (MODE <= 1) {
            if (n == 2) k2_lda<0>(c, st + 3, af[SA][0]);
            if (n == 4) k2_lda<1>(c, st + 3, af[SA][1]);
            if (n == 6) k2_lda<2>(c, st + 3, af[SA][2]);
        }
        if (MODE == 0) {
            if (n == 3) k2_dma<SET, 0>(c, st + 4);
            if (n == 5) k2_dma<SET, 1>(c, st + 4);
            if (n == 7) { k2_dma<SET, 2>(c, st + 4); k2_dma<SET, 3>(c, st + 4); }
        }
        SB();
        if (n == 1 && MODE != 4) {
            if (MODE <= 1) asm volatile("s_waitcnt vmcnt(7) lgkmcnt(0)\n\ts_barrier" : "+v"(af[SN][0]), "+v"(af[SN][1]), "+v"(af[SN][2]) :: "memory");
            else if (MODE == 2) asm volatile("s_waitcnt vmcnt(3) lgkmcnt(0)\n\ts_barrier" : "+v"(af[SN][0]), "+v"(af[SN][1]), "+v"(af[SN][2]) :: "memory");
            else asm volatile("s_waitcnt vmcnt(0) lgkmcnt(0)\n\ts_barrier" : "+v"(af[SN][0]), "+v"(af[SN][1]), "+v"(af[SN][2]) :: "memory");
            SB();
        }
    }
}

__global__ __launch_bounds__(512, 2) void k2_oproj(const K2Args args) {
    extern __shared__ __attribute__((aligned(16))) char smem[];
    const lds_cptr lds = (lds_cptr)smem;
    const unsigned ldsb = (unsigned)(uintptr_t)smem;
    const int tid = threadIdx.x, l = tid & 63, r = l & 15, g = l >> 4, q4 = r >> 2, p = l & 3;
    const int w = rfl(tid >> 6), kq = w & 3, mh = w >> 2;
    const int xcd = blockIdx.x & 7, idx = blockIdx.x >> 3;
    const int br = xcd >> 2, mt = (xcd & 3) * 6 + idx / 5, nt = idx % 5;
    K2Ctx c;
    c.lds = lds; c.ldsw = ldsb + kq * K2_Q + mh * 4224;
    c.wp = args.wo[br] + (size_t)(kq * 1024 + mh * 16) * (DM * 2) + nt * 256;
    c.ap = args.ows + ((size_t)(br * 144 + mt * 6 + mh * 3) * NH + kq * 8) * 4096;
#pragma unroll
    for (int j = 0; j < 4; ++j) {
        c.wv[j] = (4 * j + q4) * (DM * 2) + g * 64 + p * 16;
        c.ba[0][j] = kq * K2_Q + g * 1056 + ((kq + j) & 3) * 256 + q4 * 64 + p * 8; c.ba[1][j] = c.ba[0][j] + 2 * K2_STG;
    }
    c.av = l * 16;
    c.ldsd = ldsb + 4 * K2_STG; c.w = w; c.part = w < 4 ? idx / 5 : nt;
    c.tb = w < 4 ? args.wo[br] + nt * 256 : args.ows + (size_t)(br * 144 + mt * 6) * (NH * 4096);
    f32x4 acc[3][8]; u32x4 af[4][3]; bf16x8 bw[8];
#pragma unroll
    for (int i = 0; i < 3; ++i)
#pragma unroll
        for (int n = 0; n < 8; ++n) { acc[i][n] = f32x4{0, 0, 0, 0}; PIN(acc[i][n]); }
    k2_dma<0, 0>(c, 0); k2_dma<0, 1>(c, 0); k2_dma<0, 2>(c, 0); k2_dma<0, 3>(c, 0);
    k2_lda<0>(c, 0, af[0][0]); k2_lda<1>(c, 0, af[0][1]); k2_lda<2>(c, 0, af[0][2]);
    k2_dma<1, 0>(c, 1); k2_dma<1, 1>(c, 1); k2_dma<1, 2>(c, 1); k2_dma<1, 3>(c, 1);
    k2_lda<0>(c, 1, af[1][0]); k2_lda<1>(c, 1, af[1][1]); k2_lda<2>(c, 1, af[1][2]);
    k2_dma<2, 0>(c, 2); k2_dma<2, 1>(c, 2); k2_dma<2, 2>(c, 2); k2_dma<2, 3>(c, 2);
    k2_lda<0>(c, 2, af[2][0]); k2_lda<1>(c, 2, af[2][1]); k2_lda<2>(c, 2, af[2][2]);
    k2_dma<3, 0>(c, 3); k2_dma<3, 1>(c, 3); k2_dma<3, 2>(c, 3); k2_dma<3, 3>(c, 3);
    if (TOUCH) k2_touch(c, 1);
    asm volatile("s_waitcnt vmcnt(18)\n\ts_barrier" : "+v"(af[0][0]), "+v"(af[0][1]), "+v"(af[0][2]) :: "memory");
#pragma unroll
    for (int n = 0; n < 8; ++n) bw[n] = k2_bfrag<0>(c, n);
    SB();
    for (int st = 0; st < 28; st += 4) {
        k2_step<0, 0>(c, st, acc, af, bw); k2_step<1, 0>(c, st + 1, acc, af, bw); k2_step<2, 0>(c, st + 2, acc, af, bw); k2_step<3, 0>(c, st + 3, acc, af, bw);
    }
    k2_step<0, 1>(c, 28, acc, af, bw); k2_step<1, 2>(c, 29, acc, af, bw); k2_step<2, 3>(c, 30, acc, af, bw); k2_step<3, 4>(c, 31, acc, af, bw);
    MFMA_PAD();
    asm volatile("s_waitcnt lgkmcnt(0)\n\ts_barrier" ::: "memory");
    SB();
#pragma unroll
    for (int d = 1; d < 4; ++d)
#pragma unroll
        for (int i = 0; i < 3; ++i)
#pragma unroll
            for (int a = 0; a < 2; ++a)
                *LDSP(f32x4, (lds_ptr)lds + (mh * 4 + ((kq + d) & 3)) * 18432 + ((d - 1) * 6144 + (i * 2 + a) * 1024) + l * 16) = acc[i][2 * d + a];
    asm volatile("s_waitcnt lgkmcnt(0)\n\ts_barrier" ::: "memory");
#pragma unroll
    for (int d = 0; d < 3; ++d)
#pragma unroll
        for (int i = 0; i < 3; ++i)
#pragma unroll
            for (int a = 0; a < 2; ++a) acc[i][a] += *LDSP(const f32x4, lds + w * 18432 + (d * 6144 + (i * 2 + a) * 1024) + l * 16);
#pragma unroll
    for (int i = 0; i < 3; ++i) {
        unsigned a0 = cvtpk(acc[i][0][0], acc[i][0][1]), a1 = cvtpk(acc[i][0][2], acc[i][0][3]);
        unsigned b0 = cvtpk(acc[i][1][0], acc[i][1][1]), b1 = cvtpk(acc[i][1][2], acc[i][1][3]);
        auto r0 = __builtin_amdgcn_permlane32_swap(a0, b0, false, false);
        auto r1 = __builtin_amdgcn_permlane32_swap(a1, b1, false, false);
        auto s0 = __builtin_amdgcn_permlane16_swap(r0[0], r0[1], false, false);
        auto s1 = __builtin_amdgcn_permlane16_swap(r1[0], r1[1], false, false);
        const u32x4 pk = {s0[0], s1[0], s0[1], s1[1]};
        const int row = (mt * 6 + mh * 3 + i) * 16 + r;
        asm volatile("global_store_dwordx4 %0, %1, off" OUTPOL "\n\ts_nop 1" :: "v"(args.out + (size_t)row * (2 * DM * 2) + br * (DM * 2) + nt * 256 + kq * 64 + g * 16), "v"(pk) : "memory");
    }
}

extern "C" void kernel_launch(void* const* d_in, const int* in_sizes, int n_in, void* d_out, int out_size, void* d_ws, size_t ws_size,
                              hipStream_t stream) {
    (void)in_sizes; (void)n_in; (void)out_size; (void)ws_size;
    (void)hipFuncSetAttribute((const void*)k1_qattn, hipFuncAttributeMaxDynamicSharedMemorySize, K1_LDS);
    (void)hipFuncSetAttribute((const void*)k2_oproj, hipFuncAttributeMaxDynamicSharedMemorySize, K2_LDS);
    K1Args a1;
    a1.x[0] = (const char*)d_in[0]; a1.k[0] = (const char*)d_in[1]; a1.v[0] = (const char*)d_in[2]; a1.wq[0] = (const char*)d_in[3];
    a1.x[1] = (const char*)d_in[5]; a1.k[1] = (const char*)d_in[6]; a1.v[1] = (const char*)d_in[7]; a1.wq[1] = (const char*)d_in[8];
    a1.wo[0] = (const char*)d_in[4]; a1.wo[1] = (const char*)d_in[9]; a1.ows = (char*)d_ws;
    K2Args a2; a2.ows = (const char*)d_ws; a2.wo[0] = (const char*)d_in[4]; a2.wo[1] = (const char*)d_in[9]; a2.out = (char*)d_out;
    hipLaunchKernelGGL(k1_qattn, dim3(256), dim3(512), K1_LDS, stream, a1);
    hipLaunchKernelGGL(k2_oproj, dim3(240), dim3(512), K2_LDS, stream, a2);
}
```

```cpp
#include <hip/hip_runtime.h>
#include <hip/hip_bf16.h>

typedef short bf16x8 __attribute__((ext_vector_type(8)));
typedef short bf16x4 __attribute__((ext_vector_type(4)));
typedef float f32x4 __attribute__((ext_vector_type(4)));
typedef float f32x2 __attribute__((ext_vector_type(2)));
typedef __bf16 bf2_t __attribute__((ext_vector_type(2)));
typedef unsigned u32x4 __attribute__((ext_vector_type(4)));
typedef unsigned u32x2 __attribute__((ext_vector_type(2)));
typedef __attribute__((address_space(3))) char* lds_ptr;
typedef __attribute__((address_space(3))) const char* lds_cptr;
#define DI __device__ __forceinline__
#define LDSP(T, p) ((__attribute__((address_space(3))) T*)(p))

constexpr int NB = 16, SQ = 144, DM = 640, NH = 32, SK = 512, DH = 128, HD = NH * DH;
constexpr float QSCALE = 0.12751743082459868f;

#define MFMA32(a, b, c) __builtin_amdgcn_mfma_f32_16x16x32_bf16((a), (b), (c), 0, 0, 0)
#define MFMA16(a, b, c) __builtin_amdgcn_mfma_f32_16x16x16bf16_1k((a), (b), (c), 0, 0, 0)

#define MFMA32T(acc, a, b) asm volatile("v_mfma_f32_16x16x32_bf16 %0, %1, %2, %0" : "+v"(acc) : "v"(a), "v"(b))
#define MFMA32Z(acc, a, b) asm volatile("v_mfma_f32_16x16x32_bf16 %0, %1, %2, 0" : "=&v"(acc) : "v"(a), "v"(b))
#define MFMA16T(acc, a, b) asm volatile("v_mfma_f32_16x16x16_bf16 %0, %1, %2, %0" : "+v"(acc) : "v"(a), "v"(b))
#define MFMA16Z(acc, a, b) asm volatile("v_mfma_f32_16x16x16_bf16 %0, %1, %2, 0" : "=&v"(acc) : "v"(a), "v"(b))
#define MFMA_PAD() asm volatile("s_nop 7\n\ts_nop 3" ::: "memory")
DI unsigned cvtpk(float lo, float hi) { f32x2 v = {lo, hi}; bf2_t b = __builtin_convertvector(v, bf2_t); return __builtin_bit_cast(unsigned, b); }
DI bf16x8 ld128(lds_cptr p) { return *LDSP(const bf16x8, p); }
DI bf16x4 ldtr(lds_cptr p) { return __builtin_amdgcn_ds_read_tr16_b64_v4i16(LDSP(bf16x4, (lds_ptr)p)); }
DI bf16x8 cat(bf16x4 lo, bf16x4 hi) { return __builtin_shufflevector(lo, hi, 0, 1, 2, 3, 4, 5, 6, 7); }
DI int fsw(int u) { return (((u ^ (u >> 1)) & 1) << 1) | ((u >> 1) & 1); }
DI unsigned rfl(unsigned x) { return __builtin_amdgcn_readfirstlane(x); }

#define DIAG_LDS 160768
#define WAIT_BARL(N, L) asm volatile("s_waitcnt vmcnt(%1) lgkmcnt(%0)\n\ts_barrier" :: "i"(L), "i"(N) : "memory")
#define DQ(x)
#define WAIT_BAR(N) WAIT_BARL(N, 0)
#define STAMP(i)
constexpr int RG = 16896, HB = 4 * RG, KE = 0, KO = RG, VE = 2 * RG, VO = 3 * RG;
constexpr int X0 = 2 * HB;
constexpr int X1 = VO + 8448;
constexpr int X1X = X0 + 18432;
constexpr int PXO = X1X + 2048;
constexpr int STO = PXO + 4096;
constexpr int K1_LDS = STO + 1024;
#define SB() __builtin_amdgcn_sched_barrier(0)
#define PRIO(x) __builtin_amdgcn_s_setprio(x)
#define TS0 5
#define KST 14
#define PIN(x) asm volatile("" : "+v"(x))
#define DMAP(POL, m0b, M0OFF, vo, sb, IMM) \
    asm volatile("s_add_u32 m0, %0, %4\n\ts_nop 0\n\tglobal_load_lds_dwordx4 %1, %2 offset:%3" POL :: "s"(m0b), "v"(vo), "s"(sb), "i"(IMM), "i"(M0OFF) : "memory", "scc")

struct K1Args { const char* x[2]; const char* k[2]; const char* v[2]; const char* wq[2]; const char* wo[2]; char* ows; char* out; };

struct AttnCtx {
    lds_cptr lds; unsigned ldk, ldv;
    unsigned ka[4], va[4], vax, pxr, pxw, kvo[2], vvo;
    const char* kp; const char* vp;
    int pc0, pc1;
};
#define KVPOL " nt"
template <int SO, int J> DI void dma_k(const AttnCtx& c, const char* kt) { DMAP(KVPOL, c.ldk, SO + (J & 1) * 896, c.kvo[J >> 1], kt, (J >> 1) * 2048 + (J & 1) * 128); }
template <int SO, int J> DI void dma_v(const AttnCtx& c, const char* vt) { DMAP(KVPOL, c.ldv, SO + J * 32, c.vvo, vt, J * 1024); }
#define DMA_K(c, SO, t, j) do { switch (j) { case 0: dma_k<SO, 0>(c, t); break; case 1: dma_k<SO, 1>(c, t); break; case 2: dma_k<SO, 2>(c, t); break; default: dma_k<SO, 3>(c, t); break; } } while (0)
#define DMA_V(c, SO, t, j) do { switch (j) { case 0: dma_v<SO, 0>(c, t); break; case 1: dma_v<SO, 1>(c, t); break; case 2: dma_v<SO, 2>(c, t); break; default: dma_v<SO, 3>(c, t); break; } } while (0)
DI bf16x8 kfrag(const AttnCtx& c, int so, int n) { return ld128(c.lds + c.ka[n >> 2] + (so + (n & 3) * 512)); }
DI bf16x4 vtr(const AttnCtx& c, int so, int i, int dt) { return ldtr(c.lds + c.va[i] + (so + (dt >> 1) * 256 + (dt & 1) * 32)); }
DI bf16x4 vxtr(const AttnCtx& c, int so, int kt, int d) { return ldtr(c.lds + c.vax + (so + kt * 4224 + d * 32)); }
DI float xmax(float m) {
    auto a = __builtin_amdgcn_permlane32_swap(__float_as_uint(m), __float_as_uint(m), false, false);
    m = fmaxf(__uint_as_float(a[0]), __uint_as_float(a[1]));
    auto b = __builtin_amdgcn_permlane16_swap(__float_as_uint(m), __float_as_uint(m), false, false);
    return fmaxf(__uint_as_float(b[0]), __uint_as_float(b[1]));
}
DI float xsum(float m) {
    auto a = __builtin_amdgcn_permlane32_swap(__float_as_uint(m), __float_as_uint(m), false, false);
    m = __uint_as_float(a[0]) + __uint_as_float(a[1]);
    auto b = __builtin_amdgcn_permlane16_swap(__float_as_uint(m), __float_as_uint(m), false, false);
    return __uint_as_float(b[0]) + __uint_as_float(b[1]);
}
DI float max4(const f32x4 v) { return fmaxf(fmaxf(v[0], v[1]), fmaxf(v[2], v[3])); }
#define EXP4(v) do { v[0] = __builtin_amdgcn_exp2f(v[0]); v[1] = __builtin_amdgcn_exp2f(v[1]); v[2] = __builtin_amdgcn_exp2f(v[2]); v[3] = __builtin_amdgcn_exp2f(v[3]); PIN(v); } while (0)
#define EXP2(v, h) do { v[2 * (h)] = __builtin_amdgcn_exp2f(v[2 * (h)]); v[2 * (h) + 1] = __builtin_amdgcn_exp2f(v[2 * (h) + 1]); PIN(v); } while (0)
#define SUM4(v, ls) do { ls += v[0]; ls += v[1]; ls += v[2]; ls += v[3]; PIN(ls); } while (0)
#define PK4(v, d) do { d = u32x2{cvtpk(v[0], v[1]), cvtpk(v[2], v[3])}; PIN(d); } while (0)
#define SUMPK(v, ls, d) do { ls += v[0]; ls += v[1]; ls += v[2]; ls += v[3]; d = u32x2{cvtpk(v[0], v[1]), cvtpk(v[2], v[3])}; PIN(ls); PIN(d); } while (0)

struct QCtx {
    lds_cptr lds; unsigned ldx[2], ldxx, ldw; int w;
    unsigned xo[3], wv, xa_own[2], xa_x, wa, wax;
    unsigned w7[4], js;
    unsigned wk, xk;
    const char* xp; const char* wp;
};
constexpr int wslot(int sl) { return sl < 2 ? VE + sl * 8320 : (sl < 4 ? KO + (sl - 2) * 8320 : VO); }
template <int SL, int J> DI void w_dma(QCtx& q) {
    DMAP("", q.ldw, wslot(SL) + J * 1040, q.wv, q.wp + q.wk + J * (4 * HD * 2), 0);
    if (J == 1) { q.wk += 32 * HD * 2; if (q.wk >= DM * HD * 2) q.wk = 0; }
}
template <int SL, int J> DI void w_dma7(const QCtx& q) { DMAP("", q.ldw, wslot(SL) + J * 1040, q.wv, q.wp + q.w7[SL] + J * q.js, 0); }
template <int ST, int J, bool DEV = false> DI void x_dma(QCtx& q) {
    if (J < 2) DMAP("", q.ldx[ST], J * 1024, q.xo[J], q.xp + q.xk, 0);
    else {
        if (DEV) asm volatile("s_add_u32 m0, %0, %3\n\ts_nop 0\n\tglobal_load_lds_dword %1, %2 sc1" :: "s"(q.ldxx), "v"(q.xo[2]), "s"(q.xp + q.xk), "i"(ST * 2048) : "memory", "scc");
        else asm volatile("s_add_u32 m0, %0, %3\n\ts_nop 0\n\tglobal_load_lds_dword %1, %2" :: "s"(q.ldxx), "v"(q.xo[2]), "s"(q.xp + q.xk), "i"(ST * 2048) : "memory", "scc");
        q.xk += 128; if (q.xk >= DM * 2) q.xk = 0;
    }
}
DI int opaque_lane() { int l; asm volatile("v_mbcnt_lo_u32_b32 %0, -1, 0\n\tv_mbcnt_hi_u32_b32 %0, -1, %0" : "=v"(l)); return l; }
DI void x_setup(QCtx& q) {
    const int l = opaque_lane(), p = l & 3, row8 = (l >> 2) & 7, hf = l >> 5, w = q.w;
#pragma unroll
    for (int j = 0; j < 2; ++j) q.xo[j] = (16 * w + 8 * j + row8) * (DM * 2) + hf * 64 + ((p ^ fsw((j << 1) | (row8 >> 2))) * 16);
    q.xo[2] = (128 + 8 * (w >> 2) + 4 * (w & 1) + (l >> 4)) * (DM * 2) + ((w >> 1) & 1) * 64 + (((row8 & 3) ^ fsw(2 * (w >> 2) + (w & 1))) * 16) + p * 4;
}
DI void w_setup(QCtx& q) {
    const int l = opaque_lane();
    q.wv = (8 * (q.w & 3) + ((l & 15) >> 2)) * (HD * 2) + (l >> 4) * 64 + (l & 3) * 16;
}
template <bool NT> DI void touch(const char* p, const unsigned off, const unsigned ldsd) {
    if (NT) asm volatile("s_mov_b32 m0, %2\n\ts_nop 0\n\tglobal_load_lds_dword %0, %1 nt" :: "v"(off), "s"(p), "s"(ldsd) : "memory");
    else asm volatile("s_mov_b32 m0, %2\n\ts_nop 0\n\tglobal_load_lds_dword %0, %1" :: "v"(off), "s"(p), "s"(ldsd) : "memory");
}
struct Seam { unsigned ldsd, to, kl; const char* tb; };
#define PACE 6
#define THR 7
#define THR2 8
DI void pace(int n) { asm volatile("s_cmp_eq_u32 %0, 0\n\ts_cbranch_scc1 2f\n1:\n\ts_sleep 1\n\ts_sub_u32 %0, %0, 1\n\ts_cmp_lg_u32 %0, 0\n\ts_cbranch_scc1 1b\n2:" : "+s"(n) :: "scc"); }

#define OPOL " sc1"
DI void ostore(char* p, const u32x4 v) { asm volatile("global_store_dwordx4 %0, %1, off" OPOL "\n\ts_nop 1" :: "v"(p), "v"(v) : "memory"); }
struct Pend { u32x4 pk[9]; char* ob[3]; unsigned lo; unsigned long long em; };
template <int I> DI void pstore(const Pend& p) { asm volatile("global_store_dwordx4 %0, %1, %2 offset:%3" OPOL "\n\ts_nop 1" :: "v"(p.lo), "v"(p.pk[I]), "s"(p.ob[I >> 2]), "i"((I & 3) * 1024) : "memory"); }
template <int I> DI void pstore_m(const Pend& p) {
    asm volatile("s_mov_b64 exec, %4\n\tglobal_store_dwordx4 %0, %1, %2 offset:%3" OPOL "\n\ts_mov_b64 exec, -1\n\ts_nop 0" :: "v"(p.lo), "v"(p.pk[I]), "s"(p.ob[I >> 2]), "i"((I & 3) * 1024), "s"(p.em) : "memory");
}
DI u32x4 opack(const f32x4 u, const f32x4 v, const float il) {
    return u32x4{cvtpk(u[0] * il, u[1] * il), cvtpk(u[2] * il, u[3] * il), cvtpk(v[0] * il, v[1] * il), cvtpk(v[2] * il, v[3] * il)};
}
DI bf16x4 wtr(const QCtx& q, const int sl, const int j, const int hi) {
    const int im = wslot(sl) + ((j == 0 || j == 9) ? (j & 1) * 32 : ((j - 1) >> 1) * 256 + ((j - 1) & 1) * 32) + hi * 1040;
    return ldtr(q.lds + ((j == 0 || j == 9) ? q.wax : q.wa) + im);
}
DI bf16x8 xfrag(const QCtx& q, const int s, const int i) {
    return i < 2 ? ld128(q.lds + q.xa_own[(s >> 1) & 1] + ((s & 1) * 512 + i * 2048)) : ld128(q.lds + q.xa_x + (((s >> 1) & 1) * 2048 + (s & 1) * 512));
}
DI void q_addr(QCtx& q, const int hb) {
    const int l = opaque_lane(), r = l & 15, wq = q.w & 3;
    const unsigned xa = (r >> 3) * 1024 + (r & 7) * 64 + (((l >> 4) ^ fsw((r >> 2) & 3)) * 16);
    q.xa_own[0] = X0 + xa + wq * 4096; q.xa_own[1] = X1 + (wq < 2 ? HB : 0) + xa + (wq & 1) * 4096; q.xa_x = X0 + 16384 + xa;
    q.wa = hb + (l >> 4) * 2080 + (r >> 2) * 64 + (l & 3) * 8; q.wax = q.wa + wq * 256;
}
DI void q_first(const QCtx& q, bf16x4 (&af)[10][2], bf16x8 (&xb)[3]) {
    xb[2] = xfrag(q, 0, 2);
#pragma unroll
    for (int j = 0; j < 10; ++j) { const int jj = j == 0 ? 0 : (j == 1 ? 9 : j - 1); if (jj != 8) { af[jj][0] = wtr(q, 0, jj, 0); af[jj][1] = wtr(q, 0, jj, 1); } if (j == 1) { xb[0] = xfrag(q, 0, 0); xb[1] = xfrag(q, 0, 1); } }
}
#define EARLY 1
struct Fin { Pend* pd; float lt[2], il[2]; int hb; char* cnt; unsigned pm; };
DI unsigned wave0(const int w) { unsigned m; asm("s_sub_u32 %0, %1, 1\n\ts_ashr_i32 %0, %0, 31" : "=s"(m) : "s"(w) : "scc"); return m; }
DI void publish(char* cnt, const unsigned lanes) {
    asm volatile("s_mov_b32 exec_lo, %3\n\ts_mov_b32 exec_hi, 0\n\tglobal_atomic_add %0, %1, %2 sc1\n\ts_mov_b64 exec, -1" :: "v"(0), "v"(1), "s"(cnt), "s"(lanes) : "memory");
}
template <int SLOT, bool FIRST, bool LAST, bool PRE = false>
DI void fast_tile(const int t, AttnCtx& c, const bf16x8 (&qf)[3][4], f32x4 (&o)[2][8], f32x4 (&ox)[2], f32x4 (&negm)[3], float (&lsum)[3], bf16x8 (&kf)[4],
                  QCtx& nq, const Seam& sm, Fin& f) {
    constexpr int SO = SLOT * RG, SN = (SLOT ^ 1) * RG;
    DQ(nq);
    const f32x4 z4 = {0.f, 0.f, 0.f, 0.f};
    f32x4 s[2][4], sx, sr; u32x2 ph[2][4], px; bf16x4 vf[5][2]; bf16x8 pxf[2], kr[4]; bf16x4 vx[2][2];
    if (FIRST) {
#pragma unroll
        for (int ks = 0; ks < 4; ++ks) kr[ks] = ld128(c.lds + sm.kl + ks * 512);
    }
    const char* kn = c.kp + (t + 2) * 16384;
    const char* vn = c.vp + (t + 1) * 16384;
#pragma unroll
    for (int n = 0; n < 16; ++n) {
        const int i = n >> 2, ks = n & 3;
        s[0][i] = MFMA32(kf[ks], qf[0][ks], ks ? s[0][i] : ((FIRST && i == 0) ? z4 : negm[0]));
        if (n == 4) EXP2(sx, 0);
        if (n == 5) EXP2(s[0][0], 0);
        if (n == 6) EXP2(s[1][0], 0);
        if (n == 7) SUM4(sx, lsum[2]);
        if (n == 8) SUM4(s[0][0], lsum[0]);
        if (n == 9) EXP2(s[0][1], 0);
        if (n == 10) SUM4(s[1][0], lsum[1]);
        if (n == 11) EXP2(s[1][1], 0);
        if (n == 12) SUM4(s[0][1], lsum[0]);
        if (n == 13) SUM4(s[1][1], lsum[1]);
        if (n >= 14) { vf[n - 14][0] = vtr(c, SO, 0, n - 14); vf[n - 14][1] = vtr(c, SO, 1, n - 14); }
        SB();
        s[1][i] = MFMA32(kf[ks], qf[1][ks], ks ? s[1][i] : ((FIRST && i == 0) ? z4 : negm[1]));
        if (i == 0) { SB(); sx = MFMA32(kf[ks], qf[2][ks], ks ? sx : (FIRST ? z4 : negm[2])); if (FIRST) sr = MFMA32(kr[ks], qf[2][ks], ks ? sr : z4); }
        if (n < 12) kf[ks] = kfrag(c, SO, n + 4);
        if (n == 4) EXP2(sx, 1);
        if (n == 5) EXP2(s[0][0], 1);
        if (n == 6) EXP2(s[1][0], 1);
        if (n == 7) { PK4(sx, px); *LDSP(u32x2, (lds_ptr)c.lds + c.pxw) = px; }
        if (n == 8) PK4(s[0][0], ph[0][0]);
        if (n == 9) EXP2(s[0][1], 1);
        if (n == 10) PK4(s[1][0], ph[1][0]);
        if (n == 11) EXP2(s[1][1], 1);
        if (n == 12) PK4(s[0][1], ph[0][1]);
        if (n == 13) PK4(s[1][1], ph[1][1]);
        if (n == 14) { EXP2(s[0][2], 0); vf[2][0] = vtr(c, SO, 0, 2); }
        if (n == 15) { EXP2(s[0][2], 1); vf[2][1] = vtr(c, SO, 1, 2); }
        if (LAST && n == 12) *LDSP(float, (lds_ptr)c.lds + STO + nq.w * 64 + ((c.pxr >> 2) & 60)) = xsum(lsum[2]);
        if (FIRST && n == 0) publish(f.cnt, f.pm);
        if (!LAST && (n & 3) == 0) DMA_V(c, SN, vn, n >> 2);
        if (LAST) { if (n == 0) w_dma7<0, 0>(nq); if (n == 4) w_dma7<0, 1>(nq); if (n == 8) w_dma7<1, 0>(nq); if (n == 12) w_dma7<1, 1>(nq); }
        if (n == 1) PRIO(2); if (n == 5) PRIO(1); if (n == 9) PRIO(0);
        SB();
        if (FIRST && n == 3) {
            const float m0 = xmax(max4(s[0][0])), m1 = xmax(max4(s[1][0])), m2 = xmax(max4(sr));
            negm[0] = f32x4{-m0, -m0, -m0, -m0}; negm[1] = f32x4{-m1, -m1, -m1, -m1}; negm[2] = f32x4{-m2, -m2, -m2, -m2};
            s[0][0] -= m0; s[1][0] -= m1; sx -= m2;
            SB();
        }
        if (n == 12 && !FIRST) pace(t == 1 ? max(c.pc0, c.pc1) : c.pc0);
        if (n == 13) {
            WAIT_BAR(LAST ? 7 : (FIRST ? 9 : 8));
            PRIO(3); SB();
        }
    }
#pragma unroll
    for (int m = 0; m < 16; ++m) {
        const int pr = m >> 3, dt = m & 7, mm = m + 4;
        const bf16x8 a = cat(vf[m % 5][0], vf[m % 5][1]);
        const u32x4 p0 = __builtin_shufflevector(ph[0][2 * pr], ph[0][2 * pr + 1], 0, 1, 2, 3), p1 = __builtin_shufflevector(ph[1][2 * pr], ph[1][2 * pr + 1], 0, 1, 2, 3);
        if (FIRST && pr == 0) MFMA32Z(o[0][dt], a, p0); else MFMA32T(o[0][dt], a, p0);
        if (m == 0) vf[3][0] = vtr(c, SO, 0, 3);
        if (m < 12) vf[mm % 5][0] = vtr(c, SO, 2 * (mm >> 3), mm & 7);
        if (m == 0) EXP2(s[1][2], 0);
        if (m == 1) EXP2(s[0][3], 0);
        if (m == 2) EXP2(s[1][3], 0);
        if (m == 3) SUM4(s[0][2], lsum[0]);
        if (m == 4) SUM4(s[1][2], lsum[1]);
        if (m == 5) PK4(s[0][3], ph[0][3]);
        if (m == 6) PK4(s[1][3], ph[1][3]);
        if (m == 7 || m == 8) vx[m - 7][0] = vxtr(c, SO, 0, m - 7);
        if (m == 10 || m == 11) vx[m - 10][0] = vxtr(c, SO, 2, m - 10);
        if (!LAST && m >= 14) kf[2 * (m - 14)] = kfrag(c, SN, 2 * (m - 14));
        if (LAST && EARLY) {
            if (m == 7) { f.lt[0] = xsum(lsum[0]); f.il[0] = __builtin_amdgcn_rcpf(f.lt[0]); PIN(f.il[0]); }
            if (m == 9) q_addr(nq, f.hb);
            if (m >= 11 && (m & 1)) { f.pd->pk[(m - 11) >> 1] = opack(o[0][m - 11], o[0][m - 10], f.il[0]); PIN(f.pd->pk[(m - 11) >> 1]); }
        }
        SB();
        if (FIRST && pr == 0) MFMA32Z(o[1][dt], a, p1); else MFMA32T(o[1][dt], a, p1);
        if (m == 0) vf[3][1] = vtr(c, SO, 1, 3);
        if (m < 12) vf[mm % 5][1] = vtr(c, SO, 2 * (mm >> 3) + 1, mm & 7);
        if (m == 0) EXP2(s[1][2], 1);
        if (m == 1) EXP2(s[0][3], 1);
        if (m == 2) EXP2(s[1][3], 1);
        if (m == 3) PK4(s[0][2], ph[0][2]);
        if (m == 4) PK4(s[1][2], ph[1][2]);
        if (m == 5) SUM4(s[0][3], lsum[0]);
        if (m == 6) SUM4(s[1][3], lsum[1]);
        if (m == 5 || m == 6) pxf[m - 5] = ld128(c.lds + c.pxr + (m - 5) * 1024);
        if (m == 7 || m == 8) vx[m - 7][1] = vxtr(c, SO, 1, m - 7);
        if (m == 10 || m == 11) vx[m - 10][1] = vxtr(c, SO, 3, m - 10);
        if (!LAST && m >= 14) kf[2 * (m - 14) + 1] = kfrag(c, SN, 2 * (m - 14) + 1);
        if (LAST && EARLY) {
            if (m == 8) { f.lt[1] = xsum(lsum[1]); f.il[1] = __builtin_amdgcn_rcpf(f.lt[1]); PIN(f.il[1]); }
            if (m >= 11 && (m & 1)) { f.pd->pk[4 + ((m - 11) >> 1)] = opack(o[1][m - 11], o[1][m - 10], f.il[1]); PIN(f.pd->pk[4 + ((m - 11) >> 1)]); }
        }
        if (m == 9 || m == 10) { SB(); if (FIRST) MFMA32Z(ox[m - 9], cat(vx[m - 9][0], vx[m - 9][1]), pxf[0]); else MFMA32T(ox[m - 9], cat(vx[m - 9][0], vx[m - 9][1]), pxf[0]); }
        if (m == 13 || m == 14) { SB(); MFMA32T(ox[m - 13], cat(vx[m - 13][0], vx[m - 13][1]), pxf[1]); }
        if (!LAST && !PRE && (m & 3) == 0) DMA_K(c, SO, kn, m >> 2);
        if (PRE) { if (m == 0) x_dma<0, 0>(nq); if (m == 4) x_dma<0, 1>(nq); if (m == 8) x_dma<0, 2, true>(nq); }
        if (LAST) {
            if (m == 0) w_dma7<2, 0>(nq); if (m == 4) w_dma7<2, 1>(nq); if (m == 8) w_dma7<3, 0>(nq); if (m == 12) w_dma7<3, 1>(nq);
            if (m == 14) x_dma<1, 0>(nq); if (m == 15) { x_dma<1, 1>(nq); x_dma<1, 2>(nq); }
        }
        if (m == 1) PRIO(2); if (m == 5) PRIO(1); if (m == 9) PRIO(0);
        SB();
        if (m == 12 && !LAST && !PRE) pace((FIRST || t == 1) ? max(c.pc0, c.pc1) : c.pc0);
        if (m == 13) { WAIT_BAR(LAST ? 6 : (PRE ? 7 : (FIRST ? 9 : 8))); PRIO(3); SB(); }
    }
}

DI bf16x8 qfrag(const f32x4 t0, const f32x4 t1) {
    unsigned a0 = cvtpk(t0[0] * QSCALE, t0[1] * QSCALE), a1 = cvtpk(t0[2] * QSCALE, t0[3] * QSCALE);
    unsigned b0 = cvtpk(t1[0] * QSCALE, t1[1] * QSCALE), b1 = cvtpk(t1[2] * QSCALE, t1[3] * QSCALE);
    auto r0 = __builtin_amdgcn_permlane32_swap(a0, b0, false, false);
    auto r1 = __builtin_amdgcn_permlane32_swap(a1, b1, false, false);
    auto s0 = __builtin_amdgcn_permlane16_swap(r0[0], r0[1], false, false);
    auto s1 = __builtin_amdgcn_permlane16_swap(r1[0], r1[1], false, false);
    u32x4 u = {s0[0], s1[0], s0[1], s1[1]};
    return __builtin_bit_cast(bf16x8, u);
}

DI void slow_frag(const bf16x8 (&tq)[4], const char* kp, const char* vp, char* dst) {
    const int l = opaque_lane(), r = l & 15, g = l >> 4;
    float m = -3.0e38f, ls = 0.f; f32x4 acc[2] = {{0, 0, 0, 0}, {0, 0, 0, 0}};
#pragma unroll 1
    for (int kt = 0; kt < 32; ++kt) {
        f32x4 s = {0, 0, 0, 0};
#pragma unroll
        for (int ks = 0; ks < 4; ++ks) s = MFMA32(*(const bf16x8*)(kp + (kt * 16 + r) * 256 + ks * 64 + g * 16), tq[ks], s);
        const float mn = fmaxf(m, xmax(max4(s))), f = __builtin_amdgcn_exp2f(m - mn);
        m = mn; s -= mn; ls *= f; u32x2 pk;
        EXP4(s); SUMPK(s, ls, pk);
#pragma unroll
        for (int d = 0; d < 2; ++d) {
            const short* vq = (const short*)(vp + (kt * 16 + 4 * g) * 256 + (d * 16 + r) * 2);
            const bf16x4 a = {vq[0], vq[128], vq[256], vq[384]};
            acc[d] = MFMA16(a, __builtin_bit_cast(bf16x4, pk), acc[d] * f);
        }
    }
    ostore(dst + l * 16, opack(acc[0], acc[1], __builtin_amdgcn_rcpf(xsum(ls))));
}

DI void slow_q(bf16x8 (&tq)[4], const char* xr, const char* wc) {
    const int l = opaque_lane(), r = l & 15, g = l >> 4;
#pragma unroll 1
    for (int ks = 0; ks < 4; ++ks) {
        f32x4 a0 = {0, 0, 0, 0}, a1 = {0, 0, 0, 0};
#pragma unroll 1
        for (int kk = 0; kk < 20; ++kk) {
            const bf16x8 xb = *(const bf16x8*)(xr + r * (DM * 2) + kk * 64 + g * 16);
            const short* wp = (const short*)(wc + (size_t)(32 * kk + 8 * g) * (HD * 2) + (32 * ks + r) * 2);
            bf16x8 w0, w1;
#pragma unroll
            for (int j = 0; j < 8; ++j) { w0[j] = wp[j * HD]; w1[j] = wp[j * HD + 16]; }
            a0 = MFMA32(w0, xb, a0); a1 = MFMA32(w1, xb, a1);
        }
        tq[0] = tq[1]; tq[1] = tq[2]; tq[2] = tq[3]; tq[3] = qfrag(a0, a1);
    }
}
constexpr int q_w(int s) { return s >= 0 && s < 16 ? 2 : 0; }
constexpr int q_x(int s) { return s >= 0 && s < 17 && (s & 1) ? 3 : 0; }
constexpr bool q_k(int s) { return s <= KST && s > KST - 8 && !((KST - s) & 1); }
constexpr int q_post(int s) { return (s >= 0 && s <= 16 && !(s & 1) ? 1 : 0) + (s == TS0 ? 2 : 0) + (q_k(s) ? 1 : 0) + (s == 17 ? 4 : 0); }
constexpr int q_all(int s) { return q_w(s) + q_x(s) + q_post(s); }
constexpr int q_wait(int s) { return (s & 1) ? q_post(s - 2) + q_all(s - 1) : q_x(s - 3) + q_post(s - 3) + q_all(s - 2) + q_all(s - 1); }
template <int N, int L> DI void wait_bar(QCtx& dq) { WAIT_BARL(N, L); }
template <int S>
DI void q_step(QCtx& q, const AttnCtx& ac, const Pend& pd, f32x4 (&qa)[2][8], f32x4 (&qx)[2], bf16x4 (&af)[10][2], const bf16x8 (&xc)[3], bf16x8 (&xn)[3],
               bf16x8 (&qf)[3][4], bf16x8 (&kf)[4], const unsigned qxa, const Seam& sm) {
    constexpr int SL = S % 5, SN = (S + 1) % 5, SP = (S + 4) % 5, BJ = S == 19 ? 3 : 1;
#pragma unroll
    for (int j = 0; j < 9; ++j) {
        const bf16x8 a0 = cat(af[j][0], af[j][1]);
        if (j > 0) { if (S == 0) MFMA32Z(qa[0][j - 1], a0, xc[0]); else MFMA32T(qa[0][j - 1], a0, xc[0]); }
        else { if (S == 0) MFMA32Z(qx[0], a0, xc[2]); else MFMA32T(qx[0], a0, xc[2]); }
        if (j == 0) af[8][0] = wtr(q, SL, 8, 0);
        if (S < 19) {
            if (j >= 2 && j < 7) { af[j - 2][0] = wtr(q, SN, j - 2, 0); af[j - 2][1] = wtr(q, SN, j - 2, 1); }
            if (j >= 7) af[j - 1][0] = wtr(q, SN, j - 1, 0);
        } else {
            if (j == 2) *LDSP(bf16x8, (lds_ptr)q.lds + qxa + q.w * 1024) = qfrag(qx[0], qx[1]);
            if (j == 4 || j == 6 || j == 8) qf[0][(j - 4) >> 1] = qfrag(qa[0][j - 4], qa[0][j - 3]);
            if (j >= 4 && j < 8) qf[2][j - 4] = ld128(q.lds + qxa + (q.w >> 2) * 4096 + (j - 4) * 1024);
        }
        SB();
        if (j > 0) { if (S == 0) MFMA32Z(qa[1][j - 1], a0, xc[1]); else MFMA32T(qa[1][j - 1], a0, xc[1]); }
        else { const bf16x8 a1 = cat(af[9][0], af[9][1]); if (S == 0) MFMA32Z(qx[1], a1, xc[2]); else MFMA32T(qx[1], a1, xc[2]); }
        if (j == 0) af[8][1] = wtr(q, SL, 8, 1);
        if (S < 19) {
            if (j == 2) { af[9][0] = wtr(q, SN, 9, 0); af[9][1] = wtr(q, SN, 9, 1); }
            if (j == 3) xn[2] = xfrag(q, S + 1, 2);
            if (j == 4 || j == 5) xn[j - 4] = xfrag(q, S + 1, j - 4);
            if (j == 6) { af[5][0] = wtr(q, SN, 5, 0); af[5][1] = wtr(q, SN, 5, 1); }
            if (j >= 7) af[j - 1][1] = wtr(q, SN, j - 1, 1);
        } else {
            if (j == 4 || j == 6 || j == 8) qf[1][(j - 4) >> 1] = qfrag(qa[1][j - 4], qa[1][j - 3]);
            if (j >= 5) kf[j - 5] = kfrag(ac, 0, j - 5);
        }
        if (S < 16) { if (j == 2) w_dma<SP, 0>(q); if (j == 3) w_dma<SP, 1>(q); }
        if (S < 17 && (S & 1)) {
            if (j == 4) x_dma<((S >> 1) & 1), 0>(q);
            if (j == 5) x_dma<((S >> 1) & 1), 1>(q);
            if (j == 6) x_dma<((S >> 1) & 1), 2>(q);
            if (S == TS0 && j >= 7) touch<false>(sm.tb + (j - 7) * 1280, sm.to, sm.ldsd);
        }
        if (q_k(S) && j == ((S & 1) ? 7 : 5)) DMA_K(ac, KE, ac.kp, 3 - ((KST - S) >> 1));
        if (S < 17 && !(S & 1) && j == 6) pstore_m<(S >> 1)>(pd);
        if (S == 17 && j >= 2 && !(j & 1)) DMA_V(ac, 0, ac.vp, (j - 2) >> 1);
        if (S == 19 && j >= 5) DMA_K(ac, KO, ac.kp + 16384, j - 5);
        if (j == 3) PRIO(2); if (j == 5) PRIO(1); if (j == 7) PRIO(0);
        SB();
        if (j == BJ) {
            if (S == 0) wait_bar<7 + 2, 15>(q);
            else if (S == 1) wait_bar<q_wait(1) + 2, 8>(q);
            else if (S == 2) wait_bar<3 + 2 + q_all(0) + q_all(1), 15>(q);
            else if (S == 19) wait_bar<4, 0>(q);
            else wait_bar<q_wait(S), (S & 1) ? 8 : 15>(q);
            PRIO(3);
            SB();
        }
    }
}

constexpr size_t DISCARD = (size_t)128 << 20, CNT = (size_t)192 << 20;
constexpr int SNAP = STO + 768, CUR = X0 + 16384;
constexpr int k2_reg(int s) { return s == 0 ? VE : (s == 1 ? KO : (s == 2 ? KE : VO)); }
struct K2Ctx { lds_cptr lds; unsigned ldsw, wv, av, ba[4]; const char* wp; const char* ap; int kd, kq; };
DI unsigned k2_pos(const int st, const int kq) { return (unsigned)(((st + kq) & 15) + (st & 16) * 2); }
DI unsigned k2_woff(const int kd, const int st, const int half) { return (k2_pos(st, kd + half) * 32u + half * 512u) * (DM * 2); }
template <int SET, int I> DI void k2_dma(const K2Ctx& c, const int st) { DMAP("", c.ldsw, k2_reg(SET) + (I >> 1) * 8320 + (I & 1) * 1040, c.wv, c.wp + k2_woff(c.kd, st, I >> 1) + (I & 1) * (16 * DM * 2), 0); }
template <int I> DI void k2_lda(const K2Ctx& c, const int st, u32x4& d) {
    asm volatile("global_load_dwordx4 %0, %1, %2 sc1" : "=v"(d) : "v"(c.av), "s"(c.ap + k2_pos(st, c.kq) * 1024u + I * (NH * 4096)) : "memory");
}
template <int SET> DI bf16x8 k2_bfrag(const K2Ctx& c, const int n) {
    const unsigned b = c.ba[n >> 1]; const int im = k2_reg(SET) + (n & 1) * 32;
    return cat(ldtr(c.lds + b + im), ldtr(c.lds + b + im + 1040));
}
template <int SET, int MODE, int W, bool Z = false>
DI void k2_step(const K2Ctx& c, const int st, f32x4 (&acc)[3][8], u32x4 (&af)[4][3], bf16x8 (&bw)[8]) {
    constexpr int SN = (SET + 1) & 3, SA = (SET + 3) & 3;
#pragma unroll
    for (int n = 0; n < 8; ++n) {
        if (Z) MFMA32Z(acc[0][n], bw[n], af[SET][0]); else MFMA32T(acc[0][n], bw[n], af[SET][0]);
        if (n >= 2) { if (n == 2) bw[0] = k2_bfrag<SN>(c, 0); else bw[n - 1] = k2_bfrag<SN>(c, n - 1); }
        if (n == 0) bw[7] = k2_bfrag<SET>(c, 7);
        SB();
        if (Z) MFMA32Z(acc[1][n], bw[n], af[SET][1]); else MFMA32T(acc[1][n], bw[n], af[SET][1]);
        if (n == 2) bw[1] = k2_bfrag<SN>(c, 1);
        if (MODE <= 1) {
            if (n == 2) k2_lda<0>(c, st + 3, af[SA][0]);
            if (n == 4) k2_lda<1>(c, st + 3, af[SA][1]);
            if (n == 6) k2_lda<2>(c, st + 3, af[SA][2]);
        }
        if (MODE == 0) {
            if (n == 3) k2_dma<SET, 0>(c, st + 4);
            if (n == 5) k2_dma<SET, 1>(c, st + 4);
            if (n == 7) k2_dma<SET, 2>(c, st + 4);
        }
        SB();
        if (Z) MFMA32Z(acc[2][n], bw[n], af[SET][2]); else MFMA32T(acc[2][n], bw[n], af[SET][2]);
        if (MODE == 0 && n == 7) k2_dma<SET, 3>(c, st + 4);
        SB();
        if (n == 1) {
            asm volatile("s_waitcnt vmcnt(%3) lgkmcnt(0)\n\ts_barrier" : "+v"(af[SN][0]), "+v"(af[SN][1]), "+v"(af[SN][2]) : "i"(W) : "memory");
            SB();
        }
    }
}
#define PATIENCE 2000000
DI bool k2_ready(const lds_cptr lds, const char* cnt, const unsigned co, const int rd, const int w) {
    const int l = opaque_lane();
    const unsigned snap = *LDSP(const unsigned, lds + SNAP + (l & 3) * 4);
    const bool mine = ((l >> 1) & 1) == rd;
    if (__builtin_expect(!__any(mine && *LDSP(const unsigned, lds + CUR + (l & 3) * 4) - snap < 8u), 1)) return true;
    if (w == 0) {
        const unsigned long long t0 = __builtin_amdgcn_s_memrealtime();
        unsigned ok = 0;
        do {
            __builtin_amdgcn_s_sleep(32);
            unsigned v; asm volatile("global_load_dword %0, %1, %2 sc1\n\ts_waitcnt vmcnt(0)" : "=v"(v) : "v"(co), "s"(cnt) : "memory");
            ok = !__any(mine && v - snap < 8u);
        } while (!ok && __builtin_amdgcn_s_memrealtime() - t0 < PATIENCE);
        *LDSP(unsigned, (lds_ptr)lds + CUR + 128) = ok;
    }
    asm volatile("s_waitcnt lgkmcnt(0)\n\ts_barrier" ::: "memory");
    return rfl(*LDSP(const unsigned, lds + CUR + 128)) != 0;
}
template <bool RETRY>
DI bool k2_run(const K1Args& args, const lds_cptr lds, const unsigned ldsb, char* ows, const int w, const int br, const int mt, const int nt, const Pend& pd,
               char* cnt, const unsigned co, char* mycnt) {
    const int l = opaque_lane(), r = l & 15, g = l >> 4, q4 = r >> 2, p = l & 3, kq = w & 3, mh = w >> 2;
    K2Ctx c;
    c.lds = lds; c.ldsw = ldsb + (mh ^ 1) * HB + kq * 2080; c.kd = (mh ^ 1) * 2; c.kq = kq;
    c.wp = args.wo[br] + (size_t)((mh ^ 1) * 2048) * (DM * 2) + nt * 256;
    c.ap = ows + (size_t)((br * 144 + mt * 6 + mh * 3) * NH + (kq >> 1) * 16 + (kq & 1) * 4) * 4096;
    c.wv = (4 * kq + q4) * (DM * 2) + g * 64 + p * 16; c.av = l * 16;
#pragma unroll
    for (int j = 0; j < 4; ++j) c.ba[j] = (kq >> 1) * HB + (kq & 1) * 8320 + g * 2080 + ((kq + j) & 3) * 256 + q4 * 64 + p * 8;
    f32x4 acc[3][8]; u32x4 af[4][3]; bf16x8 bw[8];
    if (RETRY) { k2_dma<0, 0>(c, 0); k2_dma<0, 1>(c, 0); k2_dma<0, 2>(c, 0); k2_dma<0, 3>(c, 0); k2_dma<1, 0>(c, 1); k2_dma<1, 1>(c, 1); k2_dma<1, 2>(c, 1); k2_dma<1, 3>(c, 1); }
    k2_lda<0>(c, 0, af[0][0]); k2_lda<1>(c, 0, af[0][1]); k2_lda<2>(c, 0, af[0][2]);
    k2_lda<0>(c, 1, af[1][0]); k2_lda<1>(c, 1, af[1][1]); k2_lda<2>(c, 1, af[1][2]);
    k2_dma<2, 0>(c, 2); k2_dma<2, 1>(c, 2); k2_dma<2, 2>(c, 2); k2_dma<2, 3>(c, 2);
    k2_lda<0>(c, 2, af[2][0]); k2_lda<1>(c, 2, af[2][1]); k2_lda<2>(c, 2, af[2][2]);
    k2_dma<3, 0>(c, 3); k2_dma<3, 1>(c, 3); k2_dma<3, 2>(c, 3); k2_dma<3, 3>(c, 3);
    constexpr int NS = RETRY ? 0 : 9;
    if (!RETRY) { pstore<0>(pd); pstore<1>(pd); pstore<2>(pd); pstore<3>(pd); pstore<4>(pd); pstore<5>(pd); pstore<6>(pd); pstore<7>(pd); pstore<8>(pd); }
    asm volatile("s_waitcnt vmcnt(%3)\n\ts_barrier" : "+v"(af[0][0]), "+v"(af[0][1]), "+v"(af[0][2]) : "i"(14 + NS) : "memory");
    STAMP(8);
#pragma unroll
    for (int n = 0; n < 7; ++n) bw[n] = k2_bfrag<0>(c, n);
    SB();
    const unsigned w0 = wave0(w);
    k2_step<0, 0, 11 + NS, true>(c, 0, acc, af, bw); k2_step<1, 0, 11 + NS>(c, 1, acc, af, bw); k2_step<2, 0, 7>(c, 2, acc, af, bw);
    if (!RETRY) publish(mycnt, w0 & 1u);
    k2_step<3, 0, 7>(c, 3, acc, af, bw);
    for (int st = 4; st < 28; st += 4) {
        if (!RETRY) {
            if (st == 8) asm volatile("s_mov_b32 m0, %2\n\ts_mov_b32 exec_lo, %3\n\ts_mov_b32 exec_hi, 0\n\tglobal_load_lds_dword %0, %1 sc1\n\ts_mov_b64 exec, -1" :: "v"(co), "s"(cnt), "s"(ldsb + CUR), "s"(w0 & 15u) : "memory");
            if (st == 12 && !k2_ready(lds, cnt, co, 1, w)) { asm volatile("s_waitcnt vmcnt(0)" ::: "memory"); return false; }
            SB();
        }
        k2_step<0, 0, 7>(c, st, acc, af, bw); k2_step<1, 0, 7>(c, st + 1, acc, af, bw); k2_step<2, 0, 7>(c, st + 2, acc, af, bw); k2_step<3, 0, 7>(c, st + 3, acc, af, bw);
    }
    k2_step<0, 1, 7>(c, 28, acc, af, bw); k2_step<1, 2, 3>(c, 29, acc, af, bw); k2_step<2, 2, 0>(c, 30, acc, af, bw);
    bw[7] = k2_bfrag<3>(c, 7);
    asm volatile("s_waitcnt lgkmcnt(0)\n\ts_barrier" : "+v"(bw[7]) :: "memory");
    SB();
#pragma unroll
    for (int u = 0; u < 8; ++u) {
        const int n = (u + 2) & 7, d = u >> 1;
        MFMA32T(acc[0][n], bw[n], af[3][0]); MFMA32T(acc[1][n], bw[n], af[3][1]); MFMA32T(acc[2][n], bw[n], af[3][2]);
        if (u >= 2) {
#pragma unroll
            for (int i = 0; i < 3; ++i)
                *LDSP(f32x4, (lds_ptr)lds + (mh * 4 + ((kq + d) & 3)) * 18432 + ((d - 1) * 6144 + (i * 2 + (u & 1)) * 1024) + c.av) = acc[i][u];
        }
        SB();
    }
    MFMA_PAD();
    STAMP(9);
    asm volatile("s_waitcnt lgkmcnt(0)\n\ts_barrier" ::: "memory");
#pragma unroll
    for (int d = 0; d < 3; ++d)
#pragma unroll
        for (int i = 0; i < 3; ++i)
#pragma unroll
            for (int a = 0; a < 2; ++a) acc[i][a] += *LDSP(const f32x4, lds + w * 18432 + (d * 6144 + (i * 2 + a) * 1024) + c.av);
#pragma unroll
    for (int i = 0; i < 3; ++i) {
        unsigned a0 = cvtpk(acc[i][0][0], acc[i][0][1]), a1 = cvtpk(acc[i][0][2], acc[i][0][3]);
        unsigned b0 = cvtpk(acc[i][1][0], acc[i][1][1]), b1 = cvtpk(acc[i][1][2], acc[i][1][3]);
        auto r0 = __builtin_amdgcn_permlane32_swap(a0, b0, false, false);
        auto r1 = __builtin_amdgcn_permlane32_swap(a1, b1, false, false);
        auto s0 = __builtin_amdgcn_permlane16_swap(r0[0], r0[1], false, false);
        auto s1 = __builtin_amdgcn_permlane16_swap(r1[0], r1[1], false, false);
        const u32x4 pk = {s0[0], s1[0], s0[1], s1[1]};
        const int row = (mt * 6 + mh * 3 + i) * 16 + r;
        asm volatile("global_store_dwordx4 %0, %1, off nt\n\ts_nop 1" :: "v"(args.out + (size_t)row * (2 * DM * 2) + br * (DM * 2) + nt * 256 + kq * 64 + g * 16), "v"(pk) : "memory");
    }
    return true;
}

DI unsigned cnt_off(const int l, const int br, const int mt) { return (unsigned)(((br * NB + (mt * 6 + 5 * (l & 1)) / 9) * 2 + ((l >> 1) & 1)) * 128); }
__global__ __launch_bounds__(512, 2) void k1_qattn(const K1Args args) {
    extern __shared__ __attribute__((aligned(16))) char smem[];
    const lds_cptr lds = (lds_cptr)smem;
    const unsigned ldsb = (unsigned)(uintptr_t)smem;
    const char* xb_; const char* wb_; char* ows;
    asm volatile("s_load_dwordx2 %0, %3, %4 offset:0\n\ts_load_dwordx2 %1, %3, %4 offset:48\n\ts_load_dwordx2 %2, %3, 80" : "=&s"(xb_), "=&s"(wb_), "=&s"(ows)
                 : "s"(__builtin_amdgcn_kernarg_segment_ptr()), "s"((blockIdx.x & 4) * 2) : "memory");
    SB();
    const int w = rfl(threadIdx.x >> 6), hd = w >> 2, wq = w & 3, hb = (hd ^ 1) * HB;
    const int xcd = blockIdx.x & 7, idx = blockIdx.x >> 3;
    const int br = xcd >> 2, b = ((xcd >> 1) & 1) * 8 + (idx & 7), h0 = (xcd & 1) * 16 + (idx >> 3) * 2 + hd;
    const int mt = (xcd & 3) * 6 + idx / 5, nt = idx % 5;
    QCtx q;
    DQ(q);
    q.lds = lds; q.ldx[0] = ldsb + X0 + w * 2048; q.ldx[1] = ldsb + X1 + (w < 4 ? HB : 0) + (w & 3) * 2048; q.ldxx = ldsb + X0 + 16384 + w * 256; q.ldw = ldsb + hb + wq * 2080; q.w = w;
    {
        const int c0 = ((idx & 7) * 5 / 4 + (idx >> 3) * 5 / 2) % 10;
        q.wk = c0 * (64 * HD * 2); q.xk = c0 * 128;
    }
    const size_t hoff = (size_t)(b * NH + h0) * (SK * DH * 2);
    constexpr size_t HSTEP = (size_t)8 * SK * DH * 2;
    char* ob0;
    AttnCtx ac;
    ac.lds = lds; ac.ldk = ldsb + hb + wq * 4096; ac.ldv = ldsb + hb + VE + wq * 4224;
    Seam sm; sm.ldsd = ldsb + STO + 512;
    Pend pd;
    bf16x4 af[10][2]; bf16x8 xb0[3];
    {
        x_setup(q); w_setup(q);
        asm volatile("" : "+v"(q.xo[0]), "+v"(q.xo[1]), "+v"(q.xo[2]), "+v"(q.wv), "+s"(q.ldx[0]), "+s"(q.ldxx), "+s"(q.ldw), "+s"(q.wk), "+s"(q.xk));
        SB();
        asm volatile("s_waitcnt lgkmcnt(0)" : "+s"(xb_), "+s"(wb_), "+s"(ows) :: "memory");
        q.xp = xb_ + (size_t)b * (SQ * DM * 2); q.wp = wb_ + h0 * (DH * 2);
        const int l = opaque_lane();
        x_dma<0, 0>(q); x_dma<0, 1>(q); x_dma<0, 2>(q); w_dma<0, 0>(q); w_dma<0, 1>(q); w_dma<1, 0>(q); w_dma<1, 1>(q); w_dma<2, 0>(q); w_dma<2, 1>(q);
        w_dma<3, 0>(q); w_dma<3, 1>(q);
        x_dma<1, 0>(q); x_dma<1, 1>(q); x_dma<1, 2>(q);
        const unsigned po = (l & 1) * (unsigned)HSTEP + (l & 2) * 32768;
        touch<true>(args.v[br] + hoff, po, sm.ldsd);
        asm volatile("s_mov_b32 m0, %2\n\ts_mov_b32 exec_lo, %3\n\ts_mov_b32 exec_hi, 0\n\tglobal_load_lds_dword %0, %1 sc1\n\ts_mov_b64 exec, -1"
                     :: "v"(cnt_off(l, br, mt)), "s"(ows + CNT), "s"(ldsb + SNAP), "s"(wave0(w) & 15u) : "memory");
#pragma unroll
        for (int i = 0; i < 9; ++i) pd.pk[i] = u32x4{0, 0, 0, 0};
        ob0 = ows + ((size_t)((br * NB + b) * 9) * NH + h0) * 4096;
        pd.ob[0] = pd.ob[1] = pd.ob[2] = ob0; pd.lo = l * 16; pd.em = 0;
        q_addr(q, hb);
        SB();
        WAIT_BAR(11);
        q_first(q, af, xb0);
    }
    for (int it = 0; it < 2; ++it) {
        STAMP(4 * it + 0);
        const int l = opaque_lane(), r = l & 15, g = l >> 4, q4 = r >> 2, p = l & 3, row8 = (l >> 2) & 7, hf = l >> 5;
        const char* kh = args.k[br] + hoff + it * HSTEP;
        const char* vh = args.v[br] + hoff + it * HSTEP;
        ac.kp = kh + wq * 4096; ac.vp = vh + wq * 4096;
#pragma unroll
        for (int j = 0; j < 2; ++j) ac.kvo[j] = row8 * 256 + hf * 64 + ((p ^ fsw((j << 1) | (row8 >> 2))) * 16);
        ac.vvo = q4 * 256 + g * 64 + p * 16;
        ac.pc1 = it ? 0 : PACE;
        ac.pc0 = (xcd & 1) ? (it ? THR2 : THR) : 0;
        sm.kl = hb + (r >> 3) * 2048 + (r & 7) * 64 + ((g ^ fsw((r >> 2) & 3)) * 16);
        ac.ka[0] = sm.kl + wq * 4096;
        sm.to = (unsigned)(((idx * 8 + w) * 40 + (l < 10 ? l : 9)) * 128) & (0u - (unsigned)it);
        sm.tb = args.wo[br] + (size_t)(xcd & 3) * (32 * 320 * 128);
        bf16x8 qf[3][4], kf[4];
        {
            f32x4 qa[2][8], qx[2]; bf16x8 xb1[3];
            const unsigned qxa = X0 + l * 16;
            SB();
#define QS2(S) q_step<S>(q, ac, pd, qa, qx, af, xb0, xb1, qf, kf, qxa, sm); q_step<S + 1>(q, ac, pd, qa, qx, af, xb1, xb0, qf, kf, qxa, sm)
            QS2(0); QS2(2);
            STAMP(12);
            QS2(4); QS2(6); QS2(8); QS2(10); QS2(12); QS2(14);
            STAMP(13);
            QS2(16); QS2(18);
            MFMA_PAD(); SB();
            qf[0][3] = qfrag(qa[0][6], qa[0][7]); qf[1][3] = qfrag(qa[1][6], qa[1][7]);
        }
        q.wp += (1 - it) * (8 * DH * 2);
        const unsigned vl = hb + VE + g * 1056 + q4 * 64 + p * 8;
#pragma unroll
        for (int i = 0; i < 4; ++i) { const unsigned kt = (unsigned)((i + wq) & 3); ac.ka[i] = sm.kl + kt * 4096; ac.va[i] = vl + kt * 4224; }
        ac.vax = vl + wq * 256; ac.pxr = PXO + hd * 2048 + l * 16; ac.pxw = ac.pxr + (wq >> 1) * 1024 + (wq & 1) * 8;
        f32x4 o[2][8], ox[2], negm[3]; float lsum[3] = {0.f, 0.f, 0.f};
        Fin fin; fin.pd = &pd; fin.hb = hb; fin.cnt = ows + CNT + (br * NB + b) * 256; fin.pm = wave0(w) & (unsigned)it;
        SB();
        STAMP(4 * it + 1);
        fast_tile<0, true, false>(0, ac, qf, o, ox, negm, lsum, kf, q, sm, fin);
        STAMP(14);
        for (int t = 1; t < 5; t += 2) {
            fast_tile<1, false, false>(t, ac, qf, o, ox, negm, lsum, kf, q, sm, fin);
            fast_tile<0, false, false>(t + 1, ac, qf, o, ox, negm, lsum, kf, q, sm, fin);
        }
        fast_tile<1, false, false>(5, ac, qf, o, ox, negm, lsum, kf, q, sm, fin);
        const unsigned mk = (unsigned)it - 1u;
        x_setup(q);
        q.xo[0] &= mk; q.xo[1] &= mk; q.xo[2] = (q.xo[2] & mk) | (w == 0 ? cnt_off(opaque_lane(), br, mt) & ~mk : 0u);
        if (it) { q.xp = ows + CNT; q.xk = 0; q.ldx[1] = q.ldx[0]; }
        SB();
        fast_tile<0, false, false, true>(6, ac, qf, o, ox, negm, lsum, kf, q, sm, fin);
        STAMP(15);
        w_setup(q);
        {
            const int l3 = opaque_lane();
            q.wv = (q.wv & mk) | (((4 * wq + ((l3 & 15) >> 2)) * (DM * 2) + (l3 >> 4) * 64 + (l3 & 3) * 16) & ~mk);
#pragma unroll
            for (int i = 0; i < 4; ++i) {
                q.w7[i] = it ? k2_woff((hd ^ 1) * 2, i >> 1, i & 1) : q.wk;
                q.wk += 32 * HD * 2; if (q.wk >= DM * HD * 2) q.wk = 0;
            }
            q.js = it ? 16 * DM * 2 : 4 * HD * 2;
            if (it) q.wp = args.wo[br] + (size_t)((hd ^ 1) * 2048) * (DM * 2) + (idx < 30 ? nt : 0) * 256;
        }
        SB();
        fast_tile<1, false, true>(7, ac, qf, o, ox, negm, lsum, kf, q, sm, fin);
        STAMP(4 * it + 2);
        const int l2 = opaque_lane(), r2 = l2 & 15;
        if (it == 0) {
            const unsigned to = (unsigned)(((idx * 8 + w) * 40 + (l2 < 10 ? l2 : 9)) * 128);
            touch<false>(sm.tb + 2560, to, sm.ldsd); touch<false>(sm.tb + 3840, to, sm.ldsd);
        }
        if (!EARLY) q_addr(q, hb);
        q_first(q, af, xb0);
        MFMA_PAD(); SB();
        char* ob = ob0 + it * (8 * 4096);
        float lt[3];
        lt[0] = EARLY ? fin.lt[0] : xsum(lsum[0]); lt[1] = EARLY ? fin.lt[1] : xsum(lsum[1]); lt[2] = 0.f;
#pragma unroll
        for (int i = 0; i < 4; ++i) lt[2] += *LDSP(const float, lds + STO + hd * 256 + i * 64 + r2 * 4);
#pragma unroll
        for (int a = 0; a < 2; ++a) {
            const float il = EARLY ? fin.il[a] : __builtin_amdgcn_rcpf(lt[a]);
#pragma unroll
            for (int s = EARLY ? 3 : 0; s < 4; ++s) pd.pk[4 * a + s] = opack(o[a][2 * s], o[a][2 * s + 1], il);
        }
        pd.pk[8] = opack(ox[0], ox[1], __builtin_amdgcn_rcpf(lt[2]));
        const unsigned bad = (__any(!(lt[0] < 4096.f)) ? 1u : 0u) | (__any(!(lt[1] < 4096.f)) ? 2u : 0u) | (__any(!(lt[2] < 4096.f)) ? 4u : 0u);
        pd.ob[0] = ob + ((bad & 1) ? DISCARD : 0) + (size_t)(2 * wq) * (NH * 4096); pd.ob[1] = ob + ((bad & 2) ? DISCARD : 0) + (size_t)(2 * wq + 1) * (NH * 4096);
        pd.ob[2] = ob + ((bad & 4) ? DISCARD : 0) + (size_t)8 * (NH * 4096) + wq * 1024; pd.lo = l2 * 16; pd.em = ~0ull;
        if (__builtin_expect(bad != 0, 0)) {
#pragma unroll 1
            for (int a = 0; a < 3; ++a) {
                if (!((bad >> a) & 1)) continue;
                const int qt = a < 2 ? 2 * wq + a : 8;
                bf16x8 tq[4] = {};
                slow_q(tq, args.x[br] + (size_t)(b * SQ + qt * 16) * (DM * 2), args.wq[br] + (h0 + 8 * it) * (DH * 2));
#pragma unroll 1
                for (int s = a < 2 ? 0 : wq; s < (a < 2 ? 4 : wq + 1); ++s) slow_frag(tq, kh, vh + s * 64, ob + (size_t)qt * (NH * 4096) + s * 1024);
            }
            asm volatile("s_waitcnt vmcnt(0)" ::: "memory");
        }
        SB();
        STAMP(4 * it + 3);
    }
    char* const cnt = ows + CNT; char* const mycnt = cnt + (br * NB + b) * 256 + 128;
    const unsigned co = cnt_off(opaque_lane(), br, mt);
    bool ok = idx < 30 && k2_ready(lds, cnt, co, 0, w);
    if (ok) ok = k2_run<false>(args, lds, ldsb, ows, w, br, mt, nt, pd, cnt, co, mycnt);
    else {
        pstore<0>(pd); pstore<1>(pd); pstore<2>(pd); pstore<3>(pd); pstore<4>(pd); pstore<5>(pd); pstore<6>(pd); pstore<7>(pd); pstore<8>(pd);
        asm volatile("s_waitcnt vmcnt(0)\n\ts_barrier" ::: "memory");
        publish(mycnt, wave0(w) & 1u);
    }
    if (__builtin_expect(!ok && idx < 30, 0)) {
#pragma unroll 1
        for (int u = w; u < 6 * NH; u += 8) {
            const int rt = mt * 6 + (u >> 5), h = u & 31, bb = rt / 9;
            const size_t ho = (size_t)(bb * NH + h) * (SK * DH * 2);
            bf16x8 tq[4] = {};
            slow_q(tq, args.x[br] + (size_t)rt * (16 * DM * 2), args.wq[br] + h * (DH * 2));
#pragma unroll 1
            for (int s = 0; s < 4; ++s) slow_frag(tq, args.k[br] + ho, args.v[br] + ho + s * 64, ows + ((size_t)(br * 144 + rt) * NH + h) * 4096 + s * 1024);
        }
        asm volatile("s_waitcnt vmcnt(0) lgkmcnt(0)\n\ts_barrier" ::: "memory");
        k2_run<true>(args, lds, ldsb, ows, w, br, mt, nt, pd, cnt, co, mycnt);
    }
}

extern "C" void kernel_launch(void* const* d_in, const int* in_sizes, int n_in, void* d_out, int out_size, void* d_ws, size_t ws_size,
                              hipStream_t stream) {
    (void)in_sizes; (void)n_in; (void)out_size; (void)ws_size;
    (void)hipFuncSetAttribute((const void*)k1_qattn, hipFuncAttributeMaxDynamicSharedMemorySize, K1_LDS);
    K1Args a1;
    a1.x[0] = (const char*)d_in[0]; a1.k[0] = (const char*)d_in[1]; a1.v[0] = (const char*)d_in[2]; a1.wq[0] = (const char*)d_in[3];
    a1.x[1] = (const char*)d_in[5]; a1.k[1] = (const char*)d_in[6]; a1.v[1] = (const char*)d_in[7]; a1.wq[1] = (const char*)d_in[8];
    a1.wo[0] = (const char*)d_in[4]; a1.wo[1] = (const char*)d_in[9]; a1.ows = (char*)d_ws; a1.out = (char*)d_out;
    hipLaunchKernelGGL(k1_qattn, dim3(256), dim3(512), K1_LDS, stream, a1);
}
```

```cpp
#include <hip/hip_runtime.h>
#include <hip/hip_bf16.h>

typedef short bf16x8 __attribute__((ext_vector_type(8)));
typedef short bf16x4 __attribute__((ext_vector_type(4)));
typedef float f32x4 __attribute__((ext_vector_type(4)));
typedef float f32x2 __attribute__((ext_vector_type(2)));
typedef __bf16 bf2_t __attribute__((ext_vector_type(2)));
typedef unsigned u32x4 __attribute__((ext_vector_type(4)));
typedef unsigned u32x2 __attribute__((ext_vector_type(2)));
typedef __attribute__((address_space(3))) char* lds_ptr;
typedef __attribute__((address_space(3))) const char* lds_cptr;
#define DI __device__ __forceinline__
#define LDSP(T, p) ((__attribute__((address_space(3))) T*)(p))

constexpr int NB = 16, SQ = 144, DM = 640, NH = 32, SK = 512, DH = 128, HD = NH * DH;
constexpr float QSCALE = 0.12751743082459868f;

#define MFMA32(a, b, c) __builtin_amdgcn_mfma_f32_16x16x32_bf16((a), (b), (c), 0, 0, 0)
#define MFMA16(a, b, c) __builtin_amdgcn_mfma_f32_16x16x16bf16_1k((a), (b), (c), 0, 0, 0)

#define MFMA32T(acc, a, b) asm volatile("v_mfma_f32_16x16x32_bf16 %0, %1, %2, %0" : "+v"(acc) : "v"(a), "v"(b))
#define MFMA32Z(acc, a, b) asm volatile("v_mfma_f32_16x16x32_bf16 %0, %1, %2, 0" : "=&v"(acc) : "v"(a), "v"(b))
#define MFMA16T(acc, a, b) asm volatile("v_mfma_f32_16x16x16_bf16 %0, %1, %2, %0" : "+v"(acc) : "v"(a), "v"(b))
#define MFMA16Z(acc, a, b) asm volatile("v_mfma_f32_16x16x16_bf16 %0, %1, %2, 0" : "=&v"(acc) : "v"(a), "v"(b))
#define MFMA_PAD() asm volatile("s_nop 7\n\ts_nop 3" ::: "memory")
DI unsigned cvtpk(float lo, float hi) { f32x2 v = {lo, hi}; bf2_t b = __builtin_convertvector(v, bf2_t); return __builtin_bit_cast(unsigned, b); }
DI bf16x8 ld128(lds_cptr p) { return *LDSP(const bf16x8, p); }
DI bf16x4 ldtr(lds_cptr p) { return __builtin_amdgcn_ds_read_tr16_b64_v4i16(LDSP(bf16x4, (lds_ptr)p)); }
DI bf16x8 cat(bf16x4 lo, bf16x4 hi) { return __builtin_shufflevector(lo, hi, 0, 1, 2, 3, 4, 5, 6, 7); }
DI int fsw(int u) { return (((u ^ (u >> 1)) & 1) << 1) | ((u >> 1) & 1); }
DI unsigned rfl(unsigned x) { return __builtin_amdgcn_readfirstlane(x); }

#define DIAG_LDS 160768
#define WAIT_BARL(N, L) asm volatile("s_waitcnt vmcnt(%1) lgkmcnt(%0)\n\ts_barrier" :: "i"(L), "i"(N) : "memory")
#define DQ(x)
#define WAIT_BAR(N) WAIT_BARL(N, 0)
#define STAMP(i)
constexpr int RG = 16896, HB = 4 * RG, KE = 0, KO = RG, VE = 2 * RG, VO = 3 * RG;
constexpr int X0 = 2 * HB;
constexpr int X1 = VO + 8448;
constexpr int X1X = X0 + 18432;
constexpr int PXO = X1X + 2048;
constexpr int STO = PXO + 4096;
constexpr int K1_LDS = STO + 1024;
#define SB() __builtin_amdgcn_sched_barrier(0)
#define PRIO(x) __builtin_amdgcn_s_setprio(x)
#define TS0 5
#define KST 14
#define PIN(x) asm volatile("" : "+v"(x))
#define DMAP(POL, m0b, M0OFF, vo, sb, IMM) \
    asm volatile("s_add_u32 m0, %0, %4\n\ts_nop 0\n\tglobal_load_lds_dwordx4 %1, %2 offset:%3" POL :: "s"(m0b), "v"(vo), "s"(sb), "i"(IMM), "i"(M0OFF) : "memory", "scc")

struct K1Args { const char* x[2]; const char* k[2]; const char* v[2]; const char* wq[2]; const char* wo[2]; char* ows; char* out; };

struct AttnCtx {
    lds_cptr lds; unsigned ldk, ldv;
    unsigned ka[4], va[4], vax, pxr, pxw, kvo[2], vvo;
    const char* kp; const char* vp;
    int pc0, pc1;
};
#define KVPOL " nt"
template <int SO, int J> DI void dma_k(const AttnCtx& c, const char* kt) { DMAP(KVPOL, c.ldk, SO + (J & 1) * 896, c.kvo[J >> 1], kt, (J >> 1) * 2048 + (J & 1) * 128); }
template <int SO, int J> DI void dma_v(const AttnCtx& c, const char* vt) { DMAP(KVPOL, c.ldv, SO + J * 32, c.vvo, vt, J * 1024); }
#define DMA_K(c, SO, t, j) do { switch (j) { case 0: dma_k<SO, 0>(c, t); break; case 1: dma_k<SO, 1>(c, t); break; case 2: dma_k<SO, 2>(c, t); break; default: dma_k<SO, 3>(c, t); break; } } while (0)
#define DMA_V(c, SO, t, j) do { switch (j) { case 0: dma_v<SO, 0>(c, t); break; case 1: dma_v<SO, 1>(c, t); break; case 2: dma_v<SO, 2>(c, t); break; default: dma_v<SO, 3>(c, t); break; } } while (0)
DI bf16x8 kfrag(const AttnCtx& c, int so, int n) { return ld128(c.lds + c.ka[n >> 2] + (so + (n & 3) * 512)); }
DI bf16x4 vtr(const AttnCtx& c, int so, int i, int dt) { return ldtr(c.lds + c.va[i] + (so + (dt >> 1) * 256 + (dt & 1) * 32)); }
DI bf16x4 vxtr(const AttnCtx& c, int so, int kt, int d) { return ldtr(c.lds + c.vax + (so + kt * 4224 + d * 32)); }
DI float xmax(float m) {
    auto a = __builtin_amdgcn_permlane32_swap(__float_as_uint(m), __float_as_uint(m), false, false);
    m = fmaxf(__uint_as_float(a[0]), __uint_as_float(a[1]));
    auto b = __builtin_amdgcn_permlane16_swap(__float_as_uint(m), __float_as_uint(m), false, false);
    return fmaxf(__uint_as_float(b[0]), __uint_as_float(b[1]));
}
DI float xsum(float m) {
    auto a = __builtin_amdgcn_permlane32_swap(__float_as_uint(m), __float_as_uint(m), false, false);
    m = __uint_as_float(a[0]) + __uint_as_float(a[1]);
    auto b = __builtin_amdgcn_permlane16_swap(__float_as_uint(m), __float_as_uint(m), false, false);
    return __uint_as_float(b[0]) + __uint_as_float(b[1]);
}
DI float max4(const f32x4 v) { return fmaxf(fmaxf(v[0], v[1]), fmaxf(v[2], v[3])); }
#define EXP4(v) do { v[0] = __builtin_amdgcn_exp2f(v[0]); v[1] = __builtin_amdgcn_exp2f(v[1]); v[2] = __builtin_amdgcn_exp2f(v[2]); v[3] = __builtin_amdgcn_exp2f(v[3]); PIN(v); } while (0)
#define EXP2(v, h) do { v[2 * (h)] = __builtin_amdgcn_exp2f(v[2 * (h)]); v[2 * (h) + 1] = __builtin_amdgcn_exp2f(v[2 * (h) + 1]); PIN(v); } while (0)
#define SUM4(v, ls) do { ls += v[0]; ls += v[1]; ls += v[2]; ls += v[3]; PIN(ls); } while (0)
#define PK4(v, d) do { d = u32x2{cvtpk(v[0], v[1]), cvtpk(v[2], v[3])}; PIN(d); } while (0)
#define SUMPK(v, ls, d) do { ls += v[0]; ls += v[1]; ls += v[2]; ls += v[3]; d = u32x2{cvtpk(v[0], v[1]), cvtpk(v[2], v[3])}; PIN(ls); PIN(d); } while (0)

struct QCtx {
    lds_cptr lds; unsigned ldx[2], ldxx, ldw; int w;
    unsigned xo[3], wv, xa_own[2], xa_x, wa, wax;
    unsigned wk, xk;
    const char* xp; const char* wp;
};
constexpr int wslot(int sl) { return sl < 2 ? VE + sl * 8320 : (sl < 4 ? KO + (sl - 2) * 8320 : VO); }
template <int SL, int J> DI void w_dma(QCtx& q) {
    DMAP("", q.ldw, wslot(SL) + J * 1040, q.wv, q.wp + q.wk + J * (4 * HD * 2), 0);
    if (J == 1) { q.wk += 32 * HD * 2; if (q.wk >= DM * HD * 2) q.wk = 0; }
}
template <int ST, int J> DI void x_dma(QCtx& q) {
    if (J < 2) DMAP("", q.ldx[ST], J * 1024, q.xo[J], q.xp + q.xk, 0);
    else {
        asm volatile("s_add_u32 m0, %0, %3\n\ts_nop 0\n\tglobal_load_lds_dword %1, %2" :: "s"(q.ldxx), "v"(q.xo[2]), "s"(q.xp + q.xk), "i"(ST * 2048) : "memory", "scc");
        q.xk += 128; if (q.xk >= DM * 2) q.xk = 0;
    }
}
DI int opaque_lane() { int l; asm volatile("v_mbcnt_lo_u32_b32 %0, -1, 0\n\tv_mbcnt_hi_u32_b32 %0, -1, %0" : "=v"(l)); return l; }
DI void x_setup(QCtx& q) {
    const int l = opaque_lane(), p = l & 3, row8 = (l >> 2) & 7, hf = l >> 5, w = q.w;
#pragma unroll
    for (int j = 0; j < 2; ++j) q.xo[j] = (16 * w + 8 * j + row8) * (DM * 2) + hf * 64 + ((p ^ fsw((j << 1) | (row8 >> 2))) * 16);
    q.xo[2] = (128 + 8 * (w >> 2) + 4 * (w & 1) + (l >> 4)) * (DM * 2) + ((w >> 1) & 1) * 64 + (((row8 & 3) ^ fsw(2 * (w >> 2) + (w & 1))) * 16) + p * 4;
}
DI void w_setup(QCtx& q) {
    const int l = opaque_lane();
    q.wv = (8 * (q.w & 3) + ((l & 15) >> 2)) * (HD * 2) + (l >> 4) * 64 + (l & 3) * 16;
}
template <bool NT> DI void touch(const char* p, const unsigned off, const unsigned ldsd) {
    if (NT) asm volatile("s_mov_b32 m0, %2\n\ts_nop 0\n\tglobal_load_lds_dword %0, %1 nt" :: "v"(off), "s"(p), "s"(ldsd) : "memory");
    else asm volatile("s_mov_b32 m0, %2\n\ts_nop 0\n\tglobal_load_lds_dword %0, %1" :: "v"(off), "s"(p), "s"(ldsd) : "memory");
}
struct Seam { unsigned ldsd, to, kl; const char* tb; };
#define PACE 9
#define THR 9
#define THR2 10
DI void pace(int n) { asm volatile("s_cmp_eq_u32 %0, 0\n\ts_cbranch_scc1 2f\n1:\n\ts_sleep 1\n\ts_sub_u32 %0, %0, 1\n\ts_cmp_lg_u32 %0, 0\n\ts_cbranch_scc1 1b\n2:" : "+s"(n) :: "scc"); }

#define OPOL " sc1"
DI void ostore(char* p, const u32x4 v) { asm volatile("global_store_dwordx4 %0, %1, off" OPOL "\n\ts_nop 1" :: "v"(p), "v"(v) : "memory"); }
struct Pend { u32x4 pk[9]; char* ob[3]; unsigned lo; unsigned long long em; };
template <int I> DI void pstore(const Pend& p) { asm volatile("global_store_dwordx4 %0, %1, %2 offset:%3" OPOL "\n\ts_nop 1" :: "v"(p.lo), "v"(p.pk[I]), "s"(p.ob[I >> 2]), "i"((I & 3) * 1024) : "memory"); }
template <int I> DI void pstore_m(const Pend& p) {
    asm volatile("s_mov_b64 exec, %4\n\tglobal_store_dwordx4 %0, %1, %2 offset:%3" OPOL "\n\ts_mov_b64 exec, -1\n\ts_nop 0" :: "v"(p.lo), "v"(p.pk[I]), "s"(p.ob[I >> 2]), "i"((I & 3) * 1024), "s"(p.em) : "memory");
}
DI u32x4 opack(const f32x4 u, const f32x4 v, const float il) {
    return u32x4{cvtpk(u[0] * il, u[1] * il), cvtpk(u[2] * il, u[3] * il), cvtpk(v[0] * il, v[1] * il), cvtpk(v[2] * il, v[3] * il)};
}
DI bf16x4 wtr(const QCtx& q, const int sl, const int j, const int hi) {
    const int im = wslot(sl) + ((j == 0 || j == 9) ? (j & 1) * 32 : ((j - 1) >> 1) * 256 + ((j - 1) & 1) * 32) + hi * 1040;
    return ldtr(q.lds + ((j == 0 || j == 9) ? q.wax : q.wa) + im);
}
DI bf16x8 xfrag(const QCtx& q, const int s, const int i) {
    return i < 2 ? ld128(q.lds + q.xa_own[(s >> 1) & 1] + ((s & 1) * 512 + i * 2048)) : ld128(q.lds + q.xa_x + (((s >> 1) & 1) * 2048 + (s & 1) * 512));
}
DI void q_addr(QCtx& q, const int hb) {
    const int l = opaque_lane(), r = l & 15, wq = q.w & 3;
    const unsigned xa = (r >> 3) * 1024 + (r & 7) * 64 + (((l >> 4) ^ fsw((r >> 2) & 3)) * 16);
    q.xa_own[0] = X0 + xa + wq * 4096; q.xa_own[1] = X1 + (wq < 2 ? HB : 0) + xa + (wq & 1) * 4096; q.xa_x = X0 + 16384 + xa;
    q.wa = hb + (l >> 4) * 2080 + (r >> 2) * 64 + (l & 3) * 8; q.wax = q.wa + wq * 256;
}
DI void q_first(const QCtx& q, bf16x4 (&af)[10][2], bf16x8 (&xb)[3]) {
    xb[2] = xfrag(q, 0, 2);
#pragma unroll
    for (int j = 0; j < 10; ++j) { const int jj = j == 0 ? 0 : (j == 1 ? 9 : j - 1); if (jj != 8) { af[jj][0] = wtr(q, 0, jj, 0); af[jj][1] = wtr(q, 0, jj, 1); } if (j == 1) { xb[0] = xfrag(q, 0, 0); xb[1] = xfrag(q, 0, 1); } }
}
constexpr size_t DISCARD = (size_t)128 << 20, CNT = (size_t)192 << 20;
constexpr int SNAP = STO + 768, CUR = X0 + 16384;
constexpr int k2_reg(int s) { return s == 0 ? VE : (s == 1 ? KO : (s == 2 ? KE : VO)); }
struct K2Ctx { lds_cptr lds; unsigned ldsw, wv, av, ba[4]; const char* wp; const char* ap; int kd, kq; };
DI unsigned k2_pos(const int st, const int kq) { return (unsigned)(((st + kq) & 15) + (st & 16) * 2); }
DI unsigned k2_woff(const int kd, const int st, const int half) { return (k2_pos(st, kd + half) * 32u + half * 512u) * (DM * 2); }
template <int SET, int I> DI void k2_dma(const K2Ctx& c, const int st) { DMAP("", c.ldsw, k2_reg(SET) + (I >> 1) * 8320 + (I & 1) * 1040, c.wv, c.wp + k2_woff(c.kd, st, I >> 1) + (I & 1) * (16 * DM * 2), 0); }
template <int I> DI void k2_lda(const K2Ctx& c, const int st, u32x4& d) {
    asm volatile("global_load_dwordx4 %0, %1, %2 sc1" : "=v"(d) : "v"(c.av), "s"(c.ap + k2_pos(st, c.kq) * 1024u + I * (NH * 4096)) : "memory");
}
struct Fin { Pend* pd; float lt[2], il[2]; int hb; char* cnt; unsigned pm, my, co, cur, am; K2Ctx c2; u32x4 (*a2)[3]; };
template <int I> DI void k2_lda_m(const K2Ctx& c, const int st, u32x4& d, const unsigned m) {
    asm volatile("s_mov_b32 exec_lo, %3\n\ts_mov_b32 exec_hi, %3\n\tglobal_load_dwordx4 %0, %1, %2 sc1\n\ts_mov_b64 exec, -1" : "=v"(d) : "v"(c.av), "s"(c.ap + k2_pos(st, c.kq) * 1024u + I * (NH * 4096)), "s"(m) : "memory");
}
DI void cnt_fetch(const char* cnt, const unsigned co, const unsigned ldsd, const unsigned lanes) {
    asm volatile("s_mov_b32 m0, %2\n\ts_mov_b32 exec_lo, %3\n\ts_mov_b32 exec_hi, 0\n\tglobal_load_lds_dword %0, %1 sc1\n\ts_mov_b64 exec, -1" :: "v"(co), "s"(cnt), "s"(ldsd), "s"(lanes) : "memory");
}
DI unsigned wave0(const int w) { unsigned m; asm("s_sub_u32 %0, %1, 1\n\ts_ashr_i32 %0, %0, 31" : "=s"(m) : "s"(w) : "scc"); return m; }
DI void publish(char* cnt, const unsigned lanes) {
    asm volatile("s_mov_b32 exec_lo, %3\n\ts_mov_b32 exec_hi, 0\n\tglobal_atomic_add %0, %1, %2 sc1\n\ts_mov_b64 exec, -1" :: "v"(0), "v"(1), "s"(cnt), "s"(lanes) : "memory");
}
template <int SLOT, bool FIRST, bool LAST, bool PRE = false, bool T5 = false, bool R2 = false>
DI void fast_tile(const int t, AttnCtx& c, const bf16x8 (&qf)[3][4], f32x4 (&o)[2][8], f32x4 (&ox)[2], f32x4 (&negm)[3], float (&lsum)[3], bf16x8 (&kf)[4],
                  QCtx& nq, const Seam& sm, Fin& f) {
    constexpr int SO = SLOT * RG, SN = (SLOT ^ 1) * RG;
    DQ(nq);
    const f32x4 z4 = {0.f, 0.f, 0.f, 0.f};
    f32x4 s[2][4], sx, sr; u32x2 ph[2][4], px; bf16x4 vf[5][2]; bf16x8 pxf[2], kr[4]; bf16x4 vx[2][2];
    if (FIRST) {
#pragma unroll
        for (int ks = 0; ks < 4; ++ks) kr[ks] = ld128(c.lds + sm.kl + ks * 512);
    }
    const char* kn = c.kp + (t + 2) * 16384;
    const char* vn = c.vp + (t + 1) * 16384;
#pragma unroll
    for (int n = 0; n < 16; ++n) {
        const int i = n >> 2, ks = n & 3;
        s[0][i] = MFMA32(kf[ks], qf[0][ks], ks ? s[0][i] : ((FIRST && i == 0) ? z4 : negm[0]));
        if (n == 4) EXP2(sx, 0);
        if (n == 5) EXP2(s[0][0], 0);
        if (n == 6) EXP2(s[1][0], 0);
        if (n == 7) SUM4(sx, lsum[2]);
        if (n == 8) SUM4(s[0][0], lsum[0]);
        if (n == 9) EXP2(s[0][1], 0);
        if (n == 10) SUM4(s[1][0], lsum[1]);
        if (n == 11) EXP2(s[1][1], 0);
        if (n == 12) SUM4(s[0][1], lsum[0]);
        if (n == 13) SUM4(s[1][1], lsum[1]);
        if (n >= 14) { vf[n - 14][0] = vtr(c, SO, 0, n - 14); vf[n - 14][1] = vtr(c, SO, 1, n - 14); }
        SB();
        s[1][i] = MFMA32(kf[ks], qf[1][ks], ks ? s[1][i] : ((FIRST && i == 0) ? z4 : negm[1]));
        if (i == 0) { SB(); sx = MFMA32(kf[ks], qf[2][ks], ks ? sx : (FIRST ? z4 : negm[2])); if (FIRST) sr = MFMA32(kr[ks], qf[2][ks], ks ? sr : z4); }
        if (n < 12) kf[ks] = kfrag(c, SO, n + 4);
        if (n == 4) EXP2(sx, 1);
        if (n == 5) EXP2(s[0][0], 1);
        if (n == 6) EXP2(s[1][0], 1);
        if (n == 7) { PK4(sx, px); *LDSP(u32x2, (lds_ptr)c.lds + c.pxw) = px; }
        if (n == 8) PK4(s[0][0], ph[0][0]);
        if (n == 9) EXP2(s[0][1], 1);
        if (n == 10) PK4(s[1][0], ph[1][0]);
        if (n == 11) EXP2(s[1][1], 1);
        if (n == 12) PK4(s[0][1], ph[0][1]);
        if (n == 13) PK4(s[1][1], ph[1][1]);
        if (n == 14) { EXP2(s[0][2], 0); vf[2][0] = vtr(c, SO, 0, 2); }
        if (n == 15) { EXP2(s[0][2], 1); vf[2][1] = vtr(c, SO, 1, 2); }
        if (LAST && n == 12) *LDSP(float, (lds_ptr)c.lds + STO + nq.w * 64 + ((c.pxr >> 2) & 60)) = xsum(lsum[2]);
        if (FIRST && n == 0) publish(f.cnt + f.my, f.pm);
        if (T5 && n == 0) cnt_fetch(f.cnt, f.co, f.cur, f.pm * 15u);
        if (!LAST && (n & 3) == 0) DMA_V(c, SN, vn, n >> 2);
        if (LAST && !R2) { if (n == 0) w_dma<0, 0>(nq); if (n == 4) w_dma<0, 1>(nq); if (n == 8) w_dma<1, 0>(nq); if (n == 12) w_dma<1, 1>(nq); }
        if (LAST && R2) { if (n == 0) k2_dma<0, 0>(f.c2, 0); if (n == 4) k2_dma<0, 1>(f.c2, 0); if (n == 8) k2_dma<0, 2>(f.c2, 0); if (n == 12) k2_dma<0, 3>(f.c2, 0); }
        if (n == 1) PRIO(2); if (n == 5) PRIO(1); if (n == 9) PRIO(0);
        SB();
        if (FIRST && n == 3) {
            const float m0 = xmax(max4(s[0][0])), m1 = xmax(max4(s[1][0])), m2 = xmax(max4(sr));
            negm[0] = f32x4{-m0, -m0, -m0, -m0}; negm[1] = f32x4{-m1, -m1, -m1, -m1}; negm[2] = f32x4{-m2, -m2, -m2, -m2};
            s[0][0] -= m0; s[1][0] -= m1; sx -= m2;
            SB();
        }
        if (n == 12 && !FIRST) pace(t == 1 ? max(c.pc0, c.pc1) : c.pc0);
        if (n == 13) {
            WAIT_BAR((LAST && !R2) ? 7 : ((FIRST || T5) ? 9 : 8));
            PRIO(3); SB();
        }
    }
#pragma unroll
    for (int m = 0; m < 16; ++m) {
        const int pr = m >> 3, dt = m & 7, mm = m + 4;
        const bf16x8 a = cat(vf[m % 5][0], vf[m % 5][1]);
        const u32x4 p0 = __builtin_shufflevector(ph[0][2 * pr], ph[0][2 * pr + 1], 0, 1, 2, 3), p1 = __builtin_shufflevector(ph[1][2 * pr], ph[1][2 * pr + 1], 0, 1, 2, 3);
        if (FIRST && pr == 0) MFMA32Z(o[0][dt], a, p0); else MFMA32T(o[0][dt], a, p0);
        if (m == 0) vf[3][0] = vtr(c, SO, 0, 3);
        if (m < 12) vf[mm % 5][0] = vtr(c, SO, 2 * (mm >> 3), mm & 7);
        if (m == 0) EXP2(s[1][2], 0);
        if (m == 1) EXP2(s[0][3], 0);
        if (m == 2) EXP2(s[1][3], 0);
        if (m == 3) SUM4(s[0][2], lsum[0]);
        if (m == 4) SUM4(s[1][2], lsum[1]);
        if (m == 5) PK4(s[0][3], ph[0][3]);
        if (m == 6) PK4(s[1][3], ph[1][3]);
        if (m == 7 || m == 8) vx[m - 7][0] = vxtr(c, SO, 0, m - 7);
        if (m == 10 || m == 11) vx[m - 10][0] = vxtr(c, SO, 2, m - 10);
        if (!LAST && m >= 14) kf[2 * (m - 14)] = kfrag(c, SN, 2 * (m - 14));
        if (LAST) {
            if (m == 7) { f.lt[0] = xsum(lsum[0]); f.il[0] = __builtin_amdgcn_rcpf(f.lt[0]); PIN(f.il[0]); }
            if (m == 9 && !R2) q_addr(nq, f.hb);
            if (m >= 11 && (m & 1)) { f.pd->pk[(m - 11) >> 1] = opack(o[0][m - 11], o[0][m - 10], f.il[0]); PIN(f.pd->pk[(m - 11) >> 1]); }
        }
        SB();
        if (FIRST && pr == 0) MFMA32Z(o[1][dt], a, p1); else MFMA32T(o[1][dt], a, p1);
        if (m == 0) vf[3][1] = vtr(c, SO, 1, 3);
        if (m < 12) vf[mm % 5][1] = vtr(c, SO, 2 * (mm >> 3) + 1, mm & 7);
        if (m == 0) EXP2(s[1][2], 1);
        if (m == 1) EXP2(s[0][3], 1);
        if (m == 2) EXP2(s[1][3], 1);
        if (m == 3) PK4(s[0][2], ph[0][2]);
        if (m == 4) PK4(s[1][2], ph[1][2]);
        if (m == 5) SUM4(s[0][3], lsum[0]);
        if (m == 6) SUM4(s[1][3], lsum[1]);
        if (m == 5 || m == 6) pxf[m - 5] = ld128(c.lds + c.pxr + (m - 5) * 1024);
        if (m == 7 || m == 8) vx[m - 7][1] = vxtr(c, SO, 1, m - 7);
        if (m == 10 || m == 11) vx[m - 10][1] = vxtr(c, SO, 3, m - 10);
        if (!LAST && m >= 14) kf[2 * (m - 14) + 1] = kfrag(c, SN, 2 * (m - 14) + 1);
        if (LAST) {
            if (m == 8) { f.lt[1] = xsum(lsum[1]); f.il[1] = __builtin_amdgcn_rcpf(f.lt[1]); PIN(f.il[1]); }
            if (m >= 11 && (m & 1)) { f.pd->pk[4 + ((m - 11) >> 1)] = opack(o[1][m - 11], o[1][m - 10], f.il[1]); PIN(f.pd->pk[4 + ((m - 11) >> 1)]); }
        }
        if (m == 9 || m == 10) { SB(); if (FIRST) MFMA32Z(ox[m - 9], cat(vx[m - 9][0], vx[m - 9][1]), pxf[0]); else MFMA32T(ox[m - 9], cat(vx[m - 9][0], vx[m - 9][1]), pxf[0]); }
        if (m == 13 || m == 14) { SB(); MFMA32T(ox[m - 13], cat(vx[m - 13][0], vx[m - 13][1]), pxf[1]); }
        if (!LAST && !PRE && (m & 3) == 0) DMA_K(c, SO, kn, m >> 2);
        if (PRE && !R2) { if (m == 0) x_dma<0, 0>(nq); if (m == 4) x_dma<0, 1>(nq); if (m == 8) x_dma<0, 2>(nq); }
        if (PRE && R2) { if (m == 0) k2_dma<2, 0>(f.c2, 2); if (m == 4) k2_dma<2, 1>(f.c2, 2); if (m == 8) k2_dma<2, 2>(f.c2, 2); if (m == 12) k2_dma<2, 3>(f.c2, 2); }
        if (LAST && !R2) {
            if (m == 0) w_dma<2, 0>(nq); if (m == 4) w_dma<2, 1>(nq); if (m == 8) w_dma<3, 0>(nq); if (m == 12) w_dma<3, 1>(nq);
            if (m == 14) x_dma<1, 0>(nq); if (m == 15) { x_dma<1, 1>(nq); x_dma<1, 2>(nq); }
        }
        if (LAST && R2) {
            const int u = m < 3 ? m : m - 4;
            if (m < 3 || (m >= 7 && m < 13)) {
                if (u % 3 == 0) k2_lda_m<0>(f.c2, u / 3, f.a2[u / 3][0], f.am);
                if (u % 3 == 1) k2_lda_m<1>(f.c2, u / 3, f.a2[u / 3][1], f.am);
                if (u % 3 == 2) k2_lda_m<2>(f.c2, u / 3, f.a2[u / 3][2], f.am);
            }
            if (m == 3) k2_dma<1, 0>(f.c2, 1); if (m == 4) k2_dma<1, 1>(f.c2, 1); if (m == 5) k2_dma<1, 2>(f.c2, 1); if (m == 6) k2_dma<1, 3>(f.c2, 1);
            if (m == 14) { k2_dma<3, 0>(f.c2, 3); k2_dma<3, 1>(f.c2, 3); } if (m == 15) { k2_dma<3, 2>(f.c2, 3); k2_dma<3, 3>(f.c2, 3); }
        }
        if (m == 1) PRIO(2); if (m == 5) PRIO(1); if (m == 9) PRIO(0);
        SB();
        if (m == 12 && !LAST && !PRE) pace((FIRST || t == 1) ? max(c.pc0, c.pc1) : c.pc0);
        if (m == 13) { WAIT_BAR(LAST ? (R2 ? 63 : 6) : ((PRE && !R2) ? 7 : ((FIRST || T5) ? 9 : 8))); PRIO(3); SB(); }
    }
}

DI bf16x8 qfrag(const f32x4 t0, const f32x4 t1) {
    unsigned a0 = cvtpk(t0[0] * QSCALE, t0[1] * QSCALE), a1 = cvtpk(t0[2] * QSCALE, t0[3] * QSCALE);
    unsigned b0 = cvtpk(t1[0] * QSCALE, t1[1] * QSCALE), b1 = cvtpk(t1[2] * QSCALE, t1[3] * QSCALE);
    auto r0 = __builtin_amdgcn_permlane32_swap(a0, b0, false, false);
    auto r1 = __builtin_amdgcn_permlane32_swap(a1, b1, false, false);
    auto s0 = __builtin_amdgcn_permlane16_swap(r0[0], r0[1], false, false);
    auto s1 = __builtin_amdgcn_permlane16_swap(r1[0], r1[1], false, false);
    u32x4 u = {s0[0], s1[0], s0[1], s1[1]};
    return __builtin_bit_cast(bf16x8, u);
}

DI void slow_frag(const bf16x8 (&tq)[4], const char* kp, const char* vp, char* dst) {
    const int l = opaque_lane(), r = l & 15, g = l >> 4;
    float m = -3.0e38f, ls = 0.f; f32x4 acc[2] = {{0, 0, 0, 0}, {0, 0, 0, 0}};
#pragma unroll 1
    for (int kt = 0; kt < 32; ++kt) {
        f32x4 s = {0, 0, 0, 0};
#pragma unroll
        for (int ks = 0; ks < 4; ++ks) s = MFMA32(*(const bf16x8*)(kp + (kt * 16 + r) * 256 + ks * 64 + g * 16), tq[ks], s);
        const float mn = fmaxf(m, xmax(max4(s))), f = __builtin_amdgcn_exp2f(m - mn);
        m = mn; s -= mn; ls *= f; u32x2 pk;
        EXP4(s); SUMPK(s, ls, pk);
#pragma unroll
        for (int d = 0; d < 2; ++d) {
            const short* vq = (const short*)(vp + (kt * 16 + 4 * g) * 256 + (d * 16 + r) * 2);
            const bf16x4 a = {vq[0], vq[128], vq[256], vq[384]};
            acc[d] = MFMA16(a, __builtin_bit_cast(bf16x4, pk), acc[d] * f);
        }
    }
    ostore(dst + l * 16, opack(acc[0], acc[1], __builtin_amdgcn_rcpf(xsum(ls))));
}

DI void slow_q(bf16x8 (&tq)[4], const char* xr, const char* wc) {
    const int l = opaque_lane(), r = l & 15, g = l >> 4;
#pragma unroll 1
    for (int ks = 0; ks < 4; ++ks) {
        f32x4 a0 = {0, 0, 0, 0}, a1 = {0, 0, 0, 0};
#pragma unroll 1
        for (int kk = 0; kk < 20; ++kk) {
            const bf16x8 xb = *(const bf16x8*)(xr + r * (DM * 2) + kk * 64 + g * 16);
            const short* wp = (const short*)(wc + (size_t)(32 * kk + 8 * g) * (HD * 2) + (32 * ks + r) * 2);
            bf16x8 w0, w1;
#pragma unroll
            for (int j = 0; j < 8; ++j) { w0[j] = wp[j * HD]; w1[j] = wp[j * HD + 16]; }
            a0 = MFMA32(w0, xb, a0); a1 = MFMA32(w1, xb, a1);
        }
        tq[0] = tq[1]; tq[1] = tq[2]; tq[2] = tq[3]; tq[3] = qfrag(a0, a1);
    }
}
constexpr int q_w(int s) { return s >= 0 && s < 16 ? 2 : 0; }
constexpr int q_x(int s) { return s >= 0 && s < 17 && (s & 1) ? 3 : 0; }
constexpr bool q_k(int s) { return s <= KST && s > KST - 8 && !((KST - s) & 1); }
constexpr int q_post(int s) { return (s >= 0 && s <= 16 && !(s & 1) ? 1 : 0) + (s == TS0 ? 2 : 0) + (q_k(s) ? 1 : 0) + (s == 17 ? 4 : 0); }
constexpr int q_all(int s) { return q_w(s) + q_x(s) + q_post(s); }
constexpr int q_wait(int s) { return (s & 1) ? q_post(s - 2) + q_all(s - 1) : q_x(s - 3) + q_post(s - 3) + q_all(s - 2) + q_all(s - 1); }
template <int N, int L> DI void wait_bar(QCtx& dq) { WAIT_BARL(N, L); }
template <int S>
DI void q_step(QCtx& q, const AttnCtx& ac, const Pend& pd, f32x4 (&qa)[2][8], f32x4 (&qx)[2], bf16x4 (&af)[10][2], const bf16x8 (&xc)[3], bf16x8 (&xn)[3],
               bf16x8 (&qf)[3][4], bf16x8 (&kf)[4], const unsigned qxa, const Seam& sm) {
    constexpr int SL = S % 5, SN = (S + 1) % 5, SP = (S + 4) % 5, BJ = S == 19 ? 3 : 1;
#pragma unroll
    for (int j = 0; j < 9; ++j) {
        const bf16x8 a0 = cat(af[j][0], af[j][1]);
        if (j > 0) { if (S == 0) MFMA32Z(qa[0][j - 1], a0, xc[0]); else MFMA32T(qa[0][j - 1], a0, xc[0]); }
        else { if (S == 0) MFMA32Z(qx[0], a0, xc[2]); else MFMA32T(qx[0], a0, xc[2]); }
        if (j == 0) af[8][0] = wtr(q, SL, 8, 0);
        if (S < 19) {
            if (j >= 2 && j < 7) { af[j - 2][0] = wtr(q, SN, j - 2, 0); af[j - 2][1] = wtr(q, SN, j - 2, 1); }
            if (j >= 7) af[j - 1][0] = wtr(q, SN, j - 1, 0);
        } else {
            if (j == 2) *LDSP(bf16x8, (lds_ptr)q.lds + qxa + q.w * 1024) = qfrag(qx[0], qx[1]);
            if (j == 4 || j == 6 || j == 8) qf[0][(j - 4) >> 1] = qfrag(qa[0][j - 4], qa[0][j - 3]);
            if (j >= 4 && j < 8) qf[2][j - 4] = ld128(q.lds + qxa + (q.w >> 2) * 4096 + (j - 4) * 1024);
        }
        SB();
        if (j > 0) { if (S == 0) MFMA32Z(qa[1][j - 1], a0, xc[1]); else MFMA32T(qa[1][j - 1], a0, xc[1]); }
        else { const bf16x8 a1 = cat(af[9][0], af[9][1]); if (S == 0) MFMA32Z(qx[1], a1, xc[2]); else MFMA32T(qx[1], a1, xc[2]); }
        if (j == 0) af[8][1] = wtr(q, SL, 8, 1);
        if (S < 19) {
            if (j == 2) { af[9][0] = wtr(q, SN, 9, 0); af[9][1] = wtr(q, SN, 9, 1); }
            if (j == 3) xn[2] = xfrag(q, S + 1, 2);
            if (j == 4 || j == 5) xn[j - 4] = xfrag(q, S + 1, j - 4);
            if (j == 6) { af[5][0] = wtr(q, SN, 5, 0); af[5][1] = wtr(q, SN, 5, 1); }
            if (j >= 7) af[j - 1][1] = wtr(q, SN, j - 1, 1);
        } else {
            if (j == 4 || j == 6 || j == 8) qf[1][(j - 4) >> 1] = qfrag(qa[1][j - 4], qa[1][j - 3]);
            if (j >= 5) kf[j - 5] = kfrag(ac, 0, j - 5);
        }
        if (S < 16) { if (j == 2) w_dma<SP, 0>(q); if (j == 3) w_dma<SP, 1>(q); }
        if (S < 17 && (S & 1)) {
            if (j == 4) x_dma<((S >> 1) & 1), 0>(q);
            if (j == 5) x_dma<((S >> 1) & 1), 1>(q);
            if (j == 6) x_dma<((S >> 1) & 1), 2>(q);
            if (S == TS0 && j >= 7) touch<false>(sm.tb + (j - 7) * 1280, sm.to, sm.ldsd);
        }
        if (q_k(S) && j == ((S & 1) ? 7 : 5)) DMA_K(ac, KE, ac.kp, 3 - ((KST - S) >> 1));
        if (S < 17 && !(S & 1) && j == 6) pstore_m<(S >> 1)>(pd);
        if (S == 17 && j >= 2 && !(j & 1)) DMA_V(ac, 0, ac.vp, (j - 2) >> 1);
        if (S == 19 && j >= 5) DMA_K(ac, KO, ac.kp + 16384, j - 5);
        if (j == 3) PRIO(2); if (j == 5) PRIO(1); if (j == 7) PRIO(0);
        SB();
        if (j == BJ) {
            if (S == 0) wait_bar<7 + 2, 15>(q);
            else if (S == 1) wait_bar<q_wait(1) + 2, 8>(q);
            else if (S == 2) wait_bar<3 + 2 + q_all(0) + q_all(1), 15>(q);
            else if (S == 19) wait_bar<4, 0>(q);
            else wait_bar<q_wait(S), (S & 1) ? 8 : 15>(q);
            PRIO(3);
            SB();
        }
    }
}

template <int SET> DI bf16x8 k2_bfrag(const K2Ctx& c, const int n) {
    const unsigned b = c.ba[n >> 1]; const int im = k2_reg(SET) + (n & 1) * 32;
    return cat(ldtr(c.lds + b + im), ldtr(c.lds + b + im + 1040));
}
template <int SET, int MODE, int W, bool Z = false>
DI void k2_step(const K2Ctx& c, const int st, f32x4 (&acc)[3][8], u32x4 (&af)[4][3], bf16x8 (&bw)[8]) {
    constexpr int SN = (SET + 1) & 3, SA = (SET + 3) & 3;
#pragma unroll
    for (int n = 0; n < 8; ++n) {
        if (Z) MFMA32Z(acc[0][n], bw[n], af[SET][0]); else MFMA32T(acc[0][n], bw[n], af[SET][0]);
        if (n >= 2) { if (n == 2) bw[0] = k2_bfrag<SN>(c, 0); else bw[n - 1] = k2_bfrag<SN>(c, n - 1); }
        if (n == 0) bw[7] = k2_bfrag<SET>(c, 7);
        SB();
        if (Z) MFMA32Z(acc[1][n], bw[n], af[SET][1]); else MFMA32T(acc[1][n], bw[n], af[SET][1]);
        if (n == 2) bw[1] = k2_bfrag<SN>(c, 1);
        if (MODE <= 1) {
            if (n == 2) k2_lda<0>(c, st + 3, af[SA][0]);
            if (n == 4) k2_lda<1>(c, st + 3, af[SA][1]);
            if (n == 6) k2_lda<2>(c, st + 3, af[SA][2]);
        }
        if (MODE == 0) {
            if (n == 3) k2_dma<SET, 0>(c, st + 4);
            if (n == 5) k2_dma<SET, 1>(c, st + 4);
            if (n == 7) k2_dma<SET, 2>(c, st + 4);
        }
        SB();
        if (Z) MFMA32Z(acc[2][n], bw[n], af[SET][2]); else MFMA32T(acc[2][n], bw[n], af[SET][2]);
        if (MODE == 0 && n == 7) k2_dma<SET, 3>(c, st + 4);
        SB();
        if (n == 1) {
            asm volatile("s_waitcnt vmcnt(%3) lgkmcnt(0)\n\ts_barrier" : "+v"(af[SN][0]), "+v"(af[SN][1]), "+v"(af[SN][2]) : "i"(W) : "memory");
            SB();
        }
    }
}
#define PATIENCE 2000000
DI unsigned k2_peek(const lds_cptr lds, const int rd) {
    const int l = opaque_lane();
    const unsigned long long no = __ballot(((l >> 1) & 1) == rd && *LDSP(const unsigned, lds + CUR + (l & 3) * 4) - *LDSP(const unsigned, lds + SNAP + (l & 3) * 4) < 8u);
    unsigned m; asm("s_cmp_eq_u64 %1, 0\n\ts_cselect_b32 %0, -1, 0" : "=s"(m) : "s"(no) : "scc");
    return m;
}
DI bool k2_wait(const lds_cptr lds, const char* cnt, const unsigned co, const int w) {
    if (w == 0) {
        const unsigned snap = *LDSP(const unsigned, lds + SNAP + (opaque_lane() & 3) * 4);
        const unsigned long long t0 = __builtin_amdgcn_s_memrealtime();
        unsigned ok = 0;
        do {
            __builtin_amdgcn_s_sleep(32);
            unsigned v; asm volatile("global_load_dword %0, %1, %2 sc1\n\ts_waitcnt vmcnt(0)" : "=v"(v) : "v"(co), "s"(cnt) : "memory");
            ok = !__any(v - snap < 8u);
        } while (!ok && __builtin_amdgcn_s_memrealtime() - t0 < PATIENCE);
        *LDSP(unsigned, (lds_ptr)lds + CUR + 128) = ok;
    }
    asm volatile("s_waitcnt lgkmcnt(0)\n\ts_barrier" ::: "memory");
    return rfl(*LDSP(const unsigned, lds + CUR + 128)) != 0;
}
DI void k2_setup(K2Ctx& c, const K1Args& args, const lds_cptr lds, const unsigned ldsb, const char* ows, const int w, const int br, const int mt, const int nt) {
    const int l = opaque_lane(), kq = w & 3, mh = w >> 2;
    c.lds = lds; c.ldsw = ldsb + (mh ^ 1) * HB + kq * 2080; c.kd = (mh ^ 1) * 2; c.kq = kq;
    c.wp = args.wo[br] + (size_t)((mh ^ 1) * 2048) * (DM * 2) + nt * 256;
    c.ap = ows + (size_t)((br * 144 + mt * 6 + mh * 3) * NH + (kq >> 1) * 16 + (kq & 1) * 4) * 4096;
    c.wv = (4 * kq + ((l & 15) >> 2)) * (DM * 2) + (l >> 4) * 64 + (l & 3) * 16; c.av = l * 16;
}
template <bool RETRY>
DI bool k2_run(const K1Args& args, const lds_cptr lds, const unsigned ldsb, char* ows, const int w, const int br, const int mt, const int nt, const Pend& pd,
               char* cnt, const unsigned co, char* mycnt, u32x4 (&af)[4][3]) {
    const int l = opaque_lane(), r = l & 15, g = l >> 4, q4 = r >> 2, p = l & 3, kq = w & 3, mh = w >> 2;
    K2Ctx c;
    k2_setup(c, args, lds, ldsb, ows, w, br, mt, nt);
#pragma unroll
    for (int j = 0; j < 4; ++j) c.ba[j] = (kq >> 1) * HB + (kq & 1) * 8320 + g * 2080 + ((kq + j) & 3) * 256 + q4 * 64 + p * 8;
    f32x4 acc[3][8]; bf16x8 bw[8];
    if (RETRY) {
        k2_dma<0, 0>(c, 0); k2_dma<0, 1>(c, 0); k2_dma<0, 2>(c, 0); k2_dma<0, 3>(c, 0); k2_dma<1, 0>(c, 1); k2_dma<1, 1>(c, 1); k2_dma<1, 2>(c, 1); k2_dma<1, 3>(c, 1);
        k2_dma<2, 0>(c, 2); k2_dma<2, 1>(c, 2); k2_dma<2, 2>(c, 2); k2_dma<2, 3>(c, 2);
#pragma unroll
        for (int i = 0; i < 3; ++i) { k2_lda<0>(c, i, af[i][0]); k2_lda<1>(c, i, af[i][1]); k2_lda<2>(c, i, af[i][2]); }
    }
    if (RETRY) { k2_dma<3, 0>(c, 3); k2_dma<3, 1>(c, 3); k2_dma<3, 2>(c, 3); k2_dma<3, 3>(c, 3); }
    else { pstore<0>(pd); pstore<1>(pd); pstore<2>(pd); pstore<3>(pd); pstore<4>(pd); pstore<5>(pd); pstore<6>(pd); pstore<7>(pd); pstore<8>(pd); }
    asm volatile("s_waitcnt vmcnt(%3)\n\ts_barrier" : "+v"(af[0][0]), "+v"(af[0][1]), "+v"(af[0][2]) : "i"(RETRY ? 10 : 23) : "memory");
    STAMP(8);
#pragma unroll
    for (int n = 0; n < 7; ++n) bw[n] = k2_bfrag<0>(c, n);
    SB();
    const unsigned w0 = wave0(w);
    k2_step<0, 0, RETRY ? 7 : 16, true>(c, 0, acc, af, bw); k2_step<1, 0, RETRY ? 11 : 20>(c, 1, acc, af, bw); k2_step<2, 0, 7>(c, 2, acc, af, bw);
    if (!RETRY) publish(mycnt, w0 & 1u);
    k2_step<3, 0, 7>(c, 3, acc, af, bw);
    for (int st = 4; st < 28; st += 4) {
        if (!RETRY) {
            if (st == 8) cnt_fetch(cnt, co, ldsb + CUR, w0 & 15u);
            if (st == 12 && __builtin_expect(!k2_peek(lds, 1), 0) && !k2_wait(lds, cnt, co, w)) { asm volatile("s_waitcnt vmcnt(0)" ::: "memory"); return false; }
            SB();
        }
        k2_step<0, 0, 7>(c, st, acc, af, bw); k2_step<1, 0, 7>(c, st + 1, acc, af, bw); k2_step<2, 0, 7>(c, st + 2, acc, af, bw); k2_step<3, 0, 7>(c, st + 3, acc, af, bw);
    }
    k2_step<0, 1, 7>(c, 28, acc, af, bw); k2_step<1, 2, 3>(c, 29, acc, af, bw); k2_step<2, 2, 0>(c, 30, acc, af, bw);
    bw[7] = k2_bfrag<3>(c, 7);
    asm volatile("s_waitcnt lgkmcnt(0)\n\ts_barrier" : "+v"(bw[7]) :: "memory");
    SB();
#pragma unroll
    for (int u = 0; u < 8; ++u) {
        const int n = (u + 2) & 7, d = u >> 1;
        MFMA32T(acc[0][n], bw[n], af[3][0]); MFMA32T(acc[1][n], bw[n], af[3][1]); MFMA32T(acc[2][n], bw[n], af[3][2]);
        if (u >= 2) {
#pragma unroll
            for (int i = 0; i < 3; ++i)
                *LDSP(f32x4, (lds_ptr)lds + (mh * 4 + ((kq + d) & 3)) * 18432 + ((d - 1) * 6144 + (i * 2 + (u & 1)) * 1024) + c.av) = acc[i][u];
        }
        SB();
    }
    MFMA_PAD();
    STAMP(9);
    asm volatile("s_waitcnt lgkmcnt(0)\n\ts_barrier" ::: "memory");
#pragma unroll
    for (int d = 0; d < 3; ++d)
#pragma unroll
        for (int i = 0; i < 3; ++i)
#pragma unroll
            for (int a = 0; a < 2; ++a) acc[i][a] += *LDSP(const f32x4, lds + w * 18432 + (d * 6144 + (i * 2 + a) * 1024) + c.av);
#pragma unroll
    for (int i = 0; i < 3; ++i) {
        unsigned a0 = cvtpk(acc[i][0][0], acc[i][0][1]), a1 = cvtpk(acc[i][0][2], acc[i][0][3]);
        unsigned b0 = cvtpk(acc[i][1][0], acc[i][1][1]), b1 = cvtpk(acc[i][1][2], acc[i][1][3]);
        auto r0 = __builtin_amdgcn_permlane32_swap(a0, b0, false, false);
        auto r1 = __builtin_amdgcn_permlane32_swap(a1, b1, false, false);
        auto s0 = __builtin_amdgcn_permlane16_swap(r0[0], r0[1], false, false);
        auto s1 = __builtin_amdgcn_permlane16_swap(r1[0], r1[1], false, false);
        const u32x4 pk = {s0[0], s1[0], s0[1], s1[1]};
        const int row = (mt * 6 + mh * 3 + i) * 16 + r;
        asm volatile("global_store_dwordx4 %0, %1, off nt\n\ts_nop 1" :: "v"(args.out + (size_t)row * (2 * DM * 2) + br * (DM * 2) + nt * 256 + kq * 64 + g * 16), "v"(pk) : "memory");
    }
    return true;
}

struct Geo { const K1Args* a; int br, b, mt, nt, w, h0; bool has; };
DI Geo geo(int w) {
    auto ka = __builtin_amdgcn_kernarg_segment_ptr(); int bid = blockIdx.x;
    asm volatile("" : "+s"(ka), "+s"(bid), "+s"(w));
    const int xcd = bid & 7, idx = bid >> 3;
    Geo g; g.a = (const K1Args*)ka; g.br = xcd >> 2; g.b = ((xcd >> 1) & 1) * 8 + (idx & 7); g.has = idx < 30; g.mt = (xcd & 3) * 6 + min(idx, 29) / 5; g.nt = idx % 5; g.w = w; g.h0 = (xcd & 1) * 16 + (idx >> 3) * 2 + (w >> 2);
    return g;
}
DI unsigned cnt_off(const int l, const int br, const int mt) { return (unsigned)(((br * NB + (mt * 6 + 5 * (l & 1)) / 9) * 2 + ((l >> 1) & 1)) * 128); }
DI void finish(const lds_cptr lds, const int w, const int it, char* ob, const f32x4 (&o)[2][8], const f32x4 (&ox)[2], const Fin& fin, Pend& pd) {
    const int l2 = opaque_lane(), r2 = l2 & 15, wq = w & 3;
    float l3 = 0.f;
#pragma unroll
    for (int i = 0; i < 4; ++i) l3 += *LDSP(const float, lds + STO + (w >> 2) * 256 + i * 64 + r2 * 4);
    pd.pk[3] = opack(o[0][6], o[0][7], fin.il[0]); pd.pk[7] = opack(o[1][6], o[1][7], fin.il[1]);
    pd.pk[8] = opack(ox[0], ox[1], __builtin_amdgcn_rcpf(l3));
    const unsigned bad = (__any(!(fin.lt[0] < 4096.f)) ? 1u : 0u) | (__any(!(fin.lt[1] < 4096.f)) ? 2u : 0u) | (__any(!(l3 < 4096.f)) ? 4u : 0u);
    pd.ob[0] = ob + ((bad & 1) ? DISCARD : 0) + (size_t)(2 * wq) * (NH * 4096); pd.ob[1] = ob + ((bad & 2) ? DISCARD : 0) + (size_t)(2 * wq + 1) * (NH * 4096);
    pd.ob[2] = ob + ((bad & 4) ? DISCARD : 0) + (size_t)8 * (NH * 4096) + wq * 1024; pd.lo = l2 * 16; pd.em = ~0ull;
    if (__builtin_expect(bad != 0, 0)) {
        const Geo g2 = geo(w); const int wq2 = g2.w & 3, h = g2.h0 + 8 * it;
        const size_t ho = (size_t)(g2.b * NH + h) * (SK * DH * 2);
#pragma unroll 1
        for (int a = 0; a < 3; ++a) {
            if (!((bad >> a) & 1)) continue;
            const int qt = a < 2 ? 2 * wq2 + a : 8;
            bf16x8 tq[4] = {};
            slow_q(tq, g2.a->x[g2.br] + (size_t)(g2.b * SQ + qt * 16) * (DM * 2), g2.a->wq[g2.br] + h * (DH * 2));
#pragma unroll 1
            for (int s = a < 2 ? 0 : wq2; s < (a < 2 ? 4 : wq2 + 1); ++s) slow_frag(tq, g2.a->k[g2.br] + ho, g2.a->v[g2.br] + ho + s * 64, ob + (size_t)qt * (NH * 4096) + s * 1024);
        }
        asm volatile("s_waitcnt vmcnt(0)" ::: "memory");
    }
}
__global__ __launch_bounds__(512, 2) void k1_qattn(const K1Args args) {
    extern __shared__ __attribute__((aligned(16))) char smem[];
    const lds_cptr lds = (lds_cptr)smem;
    const unsigned ldsb = (unsigned)(uintptr_t)smem;
    const char* xb_; const char* wb_; char* ows;
    asm volatile("s_load_dwordx2 %0, %3, %4 offset:0\n\ts_load_dwordx2 %1, %3, %4 offset:48\n\ts_load_dwordx2 %2, %3, 80" : "=&s"(xb_), "=&s"(wb_), "=&s"(ows)
                 : "s"(__builtin_amdgcn_kernarg_segment_ptr()), "s"((blockIdx.x & 4) * 2) : "memory");
    SB();
    const int w = rfl(threadIdx.x >> 6), hd = w >> 2, wq = w & 3, hb = (hd ^ 1) * HB;
    const int xcd = blockIdx.x & 7, idx = blockIdx.x >> 3;
    const int br = xcd >> 2, b = ((xcd >> 1) & 1) * 8 + (idx & 7), h0 = (xcd & 1) * 16 + (idx >> 3) * 2 + hd;
    u32x4 a2[4][3];
    QCtx q;
    DQ(q);
    q.lds = lds; q.ldx[0] = ldsb + X0 + w * 2048; q.ldx[1] = ldsb + X1 + (w < 4 ? HB : 0) + (w & 3) * 2048; q.ldxx = ldsb + X0 + 16384 + w * 256; q.ldw = ldsb + hb + wq * 2080; q.w = w;
    {
        const int c0 = ((idx & 7) * 5 / 4 + (idx >> 3) * 5 / 2) % 10;
        q.wk = c0 * (64 * HD * 2); q.xk = c0 * 128;
    }
    const size_t hoff = (size_t)(b * NH + h0) * (SK * DH * 2);
    constexpr size_t HSTEP = (size_t)8 * SK * DH * 2;
    char* ob0;
    AttnCtx ac;
    ac.lds = lds; ac.ldk = ldsb + hb + wq * 4096; ac.ldv = ldsb + hb + VE + wq * 4224;
    Seam sm; sm.ldsd = ldsb + STO + 512;
    Pend pd;
    bf16x4 af[10][2]; bf16x8 xb0[3];
    {
        x_setup(q); w_setup(q);
        asm volatile("" : "+v"(q.xo[0]), "+v"(q.xo[1]), "+v"(q.xo[2]), "+v"(q.wv), "+s"(q.ldx[0]), "+s"(q.ldxx), "+s"(q.ldw), "+s"(q.wk), "+s"(q.xk));
        SB();
        asm volatile("s_waitcnt lgkmcnt(0)" : "+s"(xb_), "+s"(wb_), "+s"(ows) :: "memory");
        q.xp = xb_ + (size_t)b * (SQ * DM * 2); q.wp = wb_ + h0 * (DH * 2);
        const int l = opaque_lane();
        x_dma<0, 0>(q); x_dma<0, 1>(q); x_dma<0, 2>(q); w_dma<0, 0>(q); w_dma<0, 1>(q); w_dma<1, 0>(q); w_dma<1, 1>(q); w_dma<2, 0>(q); w_dma<2, 1>(q);
        w_dma<3, 0>(q); w_dma<3, 1>(q);
        x_dma<1, 0>(q); x_dma<1, 1>(q); x_dma<1, 2>(q);
        const unsigned po = (l & 1) * (unsigned)HSTEP + (l & 2) * 32768;
        touch<true>(args.v[br] + hoff, po, sm.ldsd);
        cnt_fetch(ows + CNT, cnt_off(l, br, geo(w).mt), ldsb + SNAP, wave0(w) & 15u);
        ob0 = ows + ((size_t)((br * NB + b) * 9) * NH + h0) * 4096;
    }
    bf16x8 qf[3][4], kf[4]; f32x4 o[2][8], ox[2], negm[3]; float lsum[3]; Fin fin;
#pragma unroll 1
    for (int it = 0; it < 2; ++it) {
        asm volatile("" : "+s"(it));
        if (it) {
            x_setup(q);
            SB();
            fast_tile<0, false, false, true>(6, ac, qf, o, ox, negm, lsum, kf, q, sm, fin);
            STAMP(15);
            w_setup(q);
            SB();
            fast_tile<1, false, true>(7, ac, qf, o, ox, negm, lsum, kf, q, sm, fin);
            STAMP(2);
            {
                const int l2 = opaque_lane(); const unsigned to = (unsigned)(((idx * 8 + w) * 40 + (l2 < 10 ? l2 : 9)) * 128);
                touch<false>(sm.tb + 2560, to, sm.ldsd); touch<false>(sm.tb + 3840, to, sm.ldsd);
            }
            q_first(q, af, xb0);
            MFMA_PAD(); SB();
            finish(lds, w, 0, ob0, o, ox, fin, pd);
            SB();
            STAMP(3);
        } else {
#pragma unroll
            for (int i = 0; i < 9; ++i) pd.pk[i] = u32x4{0, 0, 0, 0};
            pd.ob[0] = pd.ob[1] = pd.ob[2] = ob0; pd.lo = 0; pd.em = 0;
            q_addr(q, hb);
            SB();
            WAIT_BAR(11);
            q_first(q, af, xb0);
        }
        STAMP(4 * it + 0);
        const int l = opaque_lane(), r = l & 15, g = l >> 4, q4 = r >> 2, p = l & 3, row8 = (l >> 2) & 7, hf = l >> 5;
        const char* kh = args.k[br] + hoff + it * HSTEP;
        const char* vh = args.v[br] + hoff + it * HSTEP;
        ac.kp = kh + wq * 4096; ac.vp = vh + wq * 4096;
#pragma unroll
        for (int j = 0; j < 2; ++j) ac.kvo[j] = row8 * 256 + hf * 64 + ((p ^ fsw((j << 1) | (row8 >> 2))) * 16);
        ac.vvo = q4 * 256 + g * 64 + p * 16;
        ac.pc1 = it ? 0 : PACE;
        ac.pc0 = (xcd & 1) ? (it ? THR2 : THR) : 0;
        sm.kl = hb + (r >> 3) * 2048 + (r & 7) * 64 + ((g ^ fsw((r >> 2) & 3)) * 16);
        ac.ka[0] = sm.kl + wq * 4096;
        sm.to = (unsigned)(((idx * 8 + w) * 40 + (l < 10 ? l : 9)) * 128) & (0u - (unsigned)it);
        sm.tb = args.wo[br] + (size_t)(xcd & 3) * (32 * 320 * 128);
        {
            f32x4 qa[2][8], qx[2]; bf16x8 xb1[3];
            const unsigned qxa = X0 + l * 16;
            SB();
#define QS2(S) q_step<S>(q, ac, pd, qa, qx, af, xb0, xb1, qf, kf, qxa, sm); q_step<S + 1>(q, ac, pd, qa, qx, af, xb1, xb0, qf, kf, qxa, sm)
            QS2(0); QS2(2);
            STAMP(12);
            QS2(4); QS2(6); QS2(8); QS2(10); QS2(12); QS2(14);
            STAMP(13);
            QS2(16); QS2(18);
            MFMA_PAD(); SB();
            qf[0][3] = qfrag(qa[0][6], qa[0][7]); qf[1][3] = qfrag(qa[1][6], qa[1][7]);
        }
        q.wp += 8 * DH * 2;
        const unsigned vl = hb + VE + g * 1056 + q4 * 64 + p * 8;
#pragma unroll
        for (int i = 0; i < 4; ++i) { const unsigned kt = (unsigned)((i + wq) & 3); ac.ka[i] = sm.kl + kt * 4096; ac.va[i] = vl + kt * 4224; }
        ac.vax = vl + wq * 256; ac.pxr = PXO + hd * 2048 + l * 16; ac.pxw = ac.pxr + (wq >> 1) * 1024 + (wq & 1) * 8;
        lsum[0] = lsum[1] = lsum[2] = 0.f;
        fin.pd = &pd; fin.hb = hb; fin.pm = wave0(w) & (unsigned)it; fin.a2 = a2;
        {
            const Geo g2 = geo(w);
            fin.cnt = g2.a->ows + CNT; fin.my = (g2.br * NB + g2.b) * 256; fin.co = cnt_off(opaque_lane(), g2.br, g2.mt);
            k2_setup(fin.c2, *g2.a, lds, ldsb, g2.a->ows, g2.w, g2.br, g2.mt, g2.nt);
        }
        SB();
        STAMP(4 * it + 1);
        fast_tile<0, true, false>(0, ac, qf, o, ox, negm, lsum, kf, q, sm, fin);
        STAMP(14);
        for (int t = 1; t < 5; t += 2) {
            fast_tile<1, false, false>(t, ac, qf, o, ox, negm, lsum, kf, q, sm, fin);
            fast_tile<0, false, false>(t + 1, ac, qf, o, ox, negm, lsum, kf, q, sm, fin);
        }
        fin.cur = ldsb + CUR;
        fast_tile<1, false, false, false, true>(5, ac, qf, o, ox, negm, lsum, kf, q, sm, fin);
    }
    fast_tile<0, false, false, true, false, true>(6, ac, qf, o, ox, negm, lsum, kf, q, sm, fin);
    const unsigned go = k2_peek(lds, 0);
    fin.am = go;
    SB();
    fast_tile<1, false, true, false, false, true>(7, ac, qf, o, ox, negm, lsum, kf, q, sm, fin);
    STAMP(6);
    MFMA_PAD(); SB();
    finish(lds, w, 1, ob0 + 8 * 4096, o, ox, fin, pd);
    SB();
    STAMP(7);
    const Geo g2 = geo(w); const K1Args& A = *g2.a;
    char* const cnt = A.ows + CNT; char* const mycnt = cnt + (g2.br * NB + g2.b) * 256 + 128;
    const unsigned co = cnt_off(opaque_lane(), g2.br, g2.mt);
    bool ok = true;
    if (__builtin_expect(go && g2.has, 1)) ok = k2_run<false>(A, lds, ldsb, A.ows, g2.w, g2.br, g2.mt, g2.nt, pd, cnt, co, mycnt, a2);
    else {
        pstore<0>(pd); pstore<1>(pd); pstore<2>(pd); pstore<3>(pd); pstore<4>(pd); pstore<5>(pd); pstore<6>(pd); pstore<7>(pd); pstore<8>(pd);
        asm volatile("s_waitcnt vmcnt(0)\n\ts_barrier" ::: "memory");
        publish(mycnt, wave0(w) & 1u);
        if (g2.has) ok = k2_wait(lds, cnt, co, w);
    }
    if (__builtin_expect((!ok || !go) && g2.has, 0)) {
#pragma unroll 1
        for (int u = ok ? 6 * NH : g2.w; u < 6 * NH; u += 8) {
            const int rt = g2.mt * 6 + (u >> 5), h = u & 31, bb = rt / 9;
            const size_t ho = (size_t)(bb * NH + h) * (SK * DH * 2);
            bf16x8 tq[4] = {};
            slow_q(tq, A.x[g2.br] + (size_t)rt * (16 * DM * 2), A.wq[g2.br] + h * (DH * 2));
#pragma unroll 1
            for (int s = 0; s < 4; ++s) slow_frag(tq, A.k[g2.br] + ho, A.v[g2.br] + ho + s * 64, A.ows + ((size_t)(g2.br * 144 + rt) * NH + h) * 4096 + s * 1024);
        }
        asm volatile("s_waitcnt vmcnt(0) lgkmcnt(0)\n\ts_barrier" ::: "memory");
        k2_run<true>(A, lds, ldsb, A.ows, g2.w, g2.br, g2.mt, g2.nt, pd, cnt, co, mycnt, a2);
    }
}

extern "C" void kernel_launch(void* const* d_in, const int* in_sizes, int n_in, void* d_out, int out_size, void* d_ws, size_t ws_size,
                              hipStream_t stream) {
    (void)in_sizes; (void)n_in; (void)out_size; (void)ws_size;
    (void)hipFuncSetAttribute((const void*)k1_qattn, hipFuncAttributeMaxDynamicSharedMemorySize, K1_LDS);
    K1Args a1;
    a1.x[0] = (const char*)d_in[0]; a1.k[0] = (const char*)d_in[1]; a1.v[0] = (const char*)d_in[2]; a1.wq[0] = (const char*)d_in[3];
    a1.x[1] = (const char*)d_in[5]; a1.k[1] = (const char*)d_in[6]; a1.v[1] = (const char*)d_in[7]; a1.wq[1] = (const char*)d_in[8];
    a1.wo[0] = (const char*)d_in[4]; a1.wo[1] = (const char*)d_in[9]; a1.ows = (char*)d_ws; a1.out = (char*)d_out;
    hipLaunchKernelGGL(k1_qattn, dim3(256), dim3(512), K1_LDS, stream, a1);
}
```

```cpp
#include <hip/hip_runtime.h>
#include <hip/hip_bf16.h>

typedef short bf16x8 __attribute__((ext_vector_type(8)));
typedef short bf16x4 __attribute__((ext_vector_type(4)));
typedef float f32x4 __attribute__((ext_vector_type(4)));
typedef float f32x2 __attribute__((ext_vector_type(2)));
typedef __bf16 bf2_t __attribute__((ext_vector_type(2)));
typedef unsigned u32x4 __attribute__((ext_vector_type(4)));
typedef unsigned u32x2 __attribute__((ext_vector_type(2)));
typedef __attribute__((address_space(3))) char* lds_ptr;
typedef __attribute__((address_space(3))) const char* lds_cptr;
#define DI __device__ __forceinline__
#define LDSP(T, p) ((__attribute__((address_space(3))) T*)(p))

constexpr int NB = 16, SQ = 144, DM = 640, NH = 32, SK = 512, DH = 128, HD = NH * DH;
constexpr float QSCALE = 0.12751743082459868f;

#define MFMA32(a, b, c) __builtin_amdgcn_mfma_f32_16x16x32_bf16((a), (b), (c), 0, 0, 0)
#define MFMA16(a, b, c) __builtin_amdgcn_mfma_f32_16x16x16bf16_1k((a), (b), (c), 0, 0, 0)

#define MFMA32T(acc, a, b) asm volatile("v_mfma_f32_16x16x32_bf16 %0, %1, %2, %0" : "+v"(acc) : "v"(a), "v"(b))
#define MFMA32Z(acc, a, b) asm volatile("v_mfma_f32_16x16x32_bf16 %0, %1, %2, 0" : "=&v"(acc) : "v"(a), "v"(b))
#define MFMA16T(acc, a, b) asm volatile("v_mfma_f32_16x16x16_bf16 %0, %1, %2, %0" : "+v"(acc) : "v"(a), "v"(b))
#define MFMA16Z(acc, a, b) asm volatile("v_mfma_f32_16x16x16_bf16 %0, %1, %2, 0" : "=&v"(acc) : "v"(a), "v"(b))
#define MFMA_PAD() asm volatile("s_nop 7\n\ts_nop 3" ::: "memory")
DI unsigned cvtpk(float lo, float hi) { f32x2 v = {lo, hi}; bf2_t b = __builtin_convertvector(v, bf2_t); return __builtin_bit_cast(unsigned, b); }
DI bf16x8 ld128(lds_cptr p) { return *LDSP(const bf16x8, p); }
DI bf16x4 ldtr(lds_cptr p) { return __builtin_amdgcn_ds_read_tr16_b64_v4i16(LDSP(bf16x4, (lds_ptr)p)); }
DI bf16x8 cat(bf16x4 lo, bf16x4 hi) { return __builtin_shufflevector(lo, hi, 0, 1, 2, 3, 4, 5, 6, 7); }
DI int fsw(int u) { return (((u ^ (u >> 1)) & 1) << 1) | ((u >> 1) & 1); }
DI unsigned rfl(unsigned x) { return __builtin_amdgcn_readfirstlane(x); }

#define WAIT_BARL(N, L) asm volatile("s_waitcnt vmcnt(%1) lgkmcnt(%0)\n\ts_barrier" :: "i"(L), "i"(N) : "memory")
#define WAIT_BAR(N) WAIT_BARL(N, 0)
constexpr int RG = 16896, HB = 4 * RG, KE = 0, KO = RG, VE = 2 * RG, VO = 3 * RG;
constexpr int X0 = 2 * HB;
constexpr int X1 = VO + 8448;
constexpr int X1X = X0 + 18432;
constexpr int PXO = X1X + 2048;
constexpr int STO = PXO + 4096;
constexpr int K1_LDS = STO + 1024;
#define SB() __builtin_amdgcn_sched_barrier(0)
#define PRIO(x) __builtin_amdgcn_s_setprio(x)
#define TS0 5
#define KST 14
#define PIN(x) asm volatile("" : "+v"(x))
#define DMAP(POL, m0b, M0OFF, vo, sb, IMM) \
    asm volatile("s_add_u32 m0, %0, %4\n\ts_nop 0\n\tglobal_load_lds_dwordx4 %1, %2 offset:%3" POL :: "s"(m0b), "v"(vo), "s"(sb), "i"(IMM), "i"(M0OFF) : "memory", "scc")

struct K1Args { const char* x[2]; const char* k[2]; const char* v[2]; const char* wq[2]; const char* wo[2]; char* ows; char* out; };

struct AttnCtx {
    lds_cptr lds; unsigned ldk, ldv;
    unsigned ka[4], va[4], vax, pxr, pxw, kvo[2], vvo;
    const char* kp; const char* vp;
    int pc0, pc1;
};
#define KVPOL " nt"
template <int SO, int J> DI void dma_k(const AttnCtx& c, const char* kt) { DMAP(KVPOL, c.ldk, SO + (J & 1) * 896, c.kvo[J >> 1], kt, (J >> 1) * 2048 + (J & 1) * 128); }
template <int SO, int J> DI void dma_v(const AttnCtx& c, const char* vt) { DMAP(KVPOL, c.ldv, SO + J * 32, c.vvo, vt, J * 1024); }
#define DMA_K(c, SO, t, j) do { switch (j) { case 0: dma_k<SO, 0>(c, t); break; case 1: dma_k<SO, 1>(c, t); break; case 2: dma_k<SO, 2>(c, t); break; default: dma_k<SO, 3>(c, t); break; } } while (0)
#define DMA_V(c, SO, t, j) do { switch (j) { case 0: dma_v<SO, 0>(c, t); break; case 1: dma_v<SO, 1>(c, t); break; case 2: dma_v<SO, 2>(c, t); break; default: dma_v<SO, 3>(c, t); break; } } while (0)
DI bf16x8 kfrag(const AttnCtx& c, int so, int n) { return ld128(c.lds + c.ka[n >> 2] + (so + (n & 3) * 512)); }
DI bf16x4 vtr(const AttnCtx& c, int so, int i, int dt) { return ldtr(c.lds + c.va[i] + (so + (dt >> 1) * 256 + (dt & 1) * 32)); }
DI bf16x4 vxtr(const AttnCtx& c, int so, int kt, int d) { return ldtr(c.lds + c.vax + (so + kt * 4224 + d * 32)); }
DI float xmax(float m) {
    auto a = __builtin_amdgcn_permlane32_swap(__float_as_uint(m), __float_as_uint(m), false, false);
    m = fmaxf(__uint_as_float(a[0]), __uint_as_float(a[1]));
    auto b = __builtin_amdgcn_permlane16_swap(__float_as_uint(m), __float_as_uint(m), false, false);
    return fmaxf(__uint_as_float(b[0]), __uint_as_float(b[1]));
}
DI float xsum(float m) {
    auto a = __builtin_amdgcn_permlane32_swap(__float_as_uint(m), __float_as_uint(m), false, false);
    m = __uint_as_float(a[0]) + __uint_as_float(a[1]);
    auto b = __builtin_amdgcn_permlane16_swap(__float_as_uint(m), __float_as_uint(m), false, false);
    return __uint_as_float(b[0]) + __uint_as_float(b[1]);
}
DI float max4(const f32x4 v) { return fmaxf(fmaxf(v[0], v[1]), fmaxf(v[2], v[3])); }
#define EXP4(v) do { v[0] = __builtin_amdgcn_exp2f(v[0]); v[1] = __builtin_amdgcn_exp2f(v[1]); v[2] = __builtin_amdgcn_exp2f(v[2]); v[3] = __builtin_amdgcn_exp2f(v[3]); PIN(v); } while (0)
#define EXP2(v, h) do { v[2 * (h)] = __builtin_amdgcn_exp2f(v[2 * (h)]); v[2 * (h) + 1] = __builtin_amdgcn_exp2f(v[2 * (h) + 1]); PIN(v); } while (0)
#define SUM4(v, ls) do { ls += v[0]; ls += v[1]; ls += v[2]; ls += v[3]; PIN(ls); } while (0)
#define PK4(v, d) do { d = u32x2{cvtpk(v[0], v[1]), cvtpk(v[2], v[3])}; PIN(d); } while (0)
#define SUMPK(v, ls, d) do { ls += v[0]; ls += v[1]; ls += v[2]; ls += v[3]; d = u32x2{cvtpk(v[0], v[1]), cvtpk(v[2], v[3])}; PIN(ls); PIN(d); } while (0)

struct QCtx {
    lds_cptr lds; unsigned ldx[2], ldxx, ldw; int w;
    unsigned xo[3], wv, xa_own[2], xa_x, wa, wax;
    unsigned wk, xk;
    const char* xp; const char* wp;
};
constexpr int wslot(int sl) { return sl < 2 ? VE + sl * 8320 : (sl < 4 ? KO + (sl - 2) * 8320 : VO); }
template <int SL, int J> DI void w_dma(QCtx& q) {
    DMAP("", q.ldw, wslot(SL) + J * 1040, q.wv, q.wp + q.wk + J * (4 * HD * 2), 0);
    if (J == 1) { q.wk += 32 * HD * 2; if (q.wk >= DM * HD * 2) q.wk = 0; }
}
template <int ST, int J> DI void x_dma(QCtx& q) {
    if (J < 2) DMAP("", q.ldx[ST], J * 1024, q.xo[J], q.xp + q.xk, 0);
    else {
        asm volatile("s_add_u32 m0, %0, %3\n\ts_nop 0\n\tglobal_load_lds_dword %1, %2" :: "s"(q.ldxx), "v"(q.xo[2]), "s"(q.xp + q.xk), "i"(ST * 2048) : "memory", "scc");
        q.xk += 128; if (q.xk >= DM * 2) q.xk = 0;
    }
}
DI int opaque_lane() { int l; asm volatile("v_mbcnt_lo_u32_b32 %0, -1, 0\n\tv_mbcnt_hi_u32_b32 %0, -1, %0" : "=v"(l)); return l; }
DI void x_setup(QCtx& q) {
    const int l = opaque_lane(), p = l & 3, row8 = (l >> 2) & 7, hf = l >> 5, w = q.w;
#pragma unroll
    for (int j = 0; j < 2; ++j) q.xo[j] = (16 * w + 8 * j + row8) * (DM * 2) + hf * 64 + ((p ^ fsw((j << 1) | (row8 >> 2))) * 16);
    q.xo[2] = (128 + 8 * (w >> 2) + 4 * (w & 1) + (l >> 4)) * (DM * 2) + ((w >> 1) & 1) * 64 + (((row8 & 3) ^ fsw(2 * (w >> 2) + (w & 1))) * 16) + p * 4;
}
DI void w_setup(QCtx& q) {
    const int l = opaque_lane();
    q.wv = (8 * (q.w & 3) + ((l & 15) >> 2)) * (HD * 2) + (l >> 4) * 64 + (l & 3) * 16;
}
template <bool NT> DI void touch(const char* p, const unsigned off, const unsigned ldsd) {
    if (NT) asm volatile("s_mov_b32 m0, %2\n\ts_nop 0\n\tglobal_load_lds_dword %0, %1 nt" :: "v"(off), "s"(p), "s"(ldsd) : "memory");
    else asm volatile("s_mov_b32 m0, %2\n\ts_nop 0\n\tglobal_load_lds_dword %0, %1" :: "v"(off), "s"(p), "s"(ldsd) : "memory");
}
struct Seam { unsigned ldsd, to, kl; const char* tb; };
#define PACE 9
#define THR 9
#define THR2 10
DI void pace(int n) { asm volatile("s_cmp_eq_u32 %0, 0\n\ts_cbranch_scc1 2f\n1:\n\ts_sleep 1\n\ts_sub_u32 %0, %0, 1\n\ts_cmp_lg_u32 %0, 0\n\ts_cbranch_scc1 1b\n2:" : "+s"(n) :: "scc"); }

#define UPOL " sc1"
#define OPOL " sc1"
DI void ostore(char* p, const u32x4 v) { asm volatile("global_store_dwordx4 %0, %1, off" OPOL "\n\ts_nop 1" :: "v"(p), "v"(v) : "memory"); }
struct Pend { u32x4 pk[9]; char* ob[3]; unsigned lo; unsigned long long em; };
template <int I> DI void pstore(const Pend& p) { asm volatile("global_store_dwordx4 %0, %1, %2 offset:%3" OPOL "\n\ts_nop 1" :: "v"(p.lo), "v"(p.pk[I]), "s"(p.ob[I >> 2]), "i"((I & 3) * 1024) : "memory"); }
template <int I> DI void pstore_m(const Pend& p) {
    asm volatile("s_mov_b64 exec, %4\n\tglobal_store_dwordx4 %0, %1, %2 offset:%3" OPOL "\n\ts_mov_b64 exec, -1\n\ts_nop 0" :: "v"(p.lo), "v"(p.pk[I]), "s"(p.ob[I >> 2]), "i"((I & 3) * 1024), "s"(p.em) : "memory");
}
DI u32x4 opack(const f32x4 u, const f32x4 v, const float il) {
    return u32x4{cvtpk(u[0] * il, u[1] * il), cvtpk(u[2] * il, u[3] * il), cvtpk(v[0] * il, v[1] * il), cvtpk(v[2] * il, v[3] * il)};
}
DI bf16x4 wtr(const QCtx& q, const int sl, const int j, const int hi) {
    const int im = wslot(sl) + ((j == 0 || j == 9) ? (j & 1) * 32 : ((j - 1) >> 1) * 256 + ((j - 1) & 1) * 32) + hi * 1040;
    return ldtr(q.lds + ((j == 0 || j == 9) ? q.wax : q.wa) + im);
}
DI bf16x8 xfrag(const QCtx& q, const int s, const int i) {
    return i < 2 ? ld128(q.lds + q.xa_own[(s >> 1) & 1] + ((s & 1) * 512 + i * 2048)) : ld128(q.lds + q.xa_x + (((s >> 1) & 1) * 2048 + (s & 1) * 512));
}
DI void q_addr(QCtx& q, const int hb) {
    const int l = opaque_lane(), r = l & 15, wq = q.w & 3;
    const unsigned xa = (r >> 3) * 1024 + (r & 7) * 64 + (((l >> 4) ^ fsw((r >> 2) & 3)) * 16);
    q.xa_own[0] = X0 + xa + wq * 4096; q.xa_own[1] = X1 + (wq < 2 ? HB : 0) + xa + (wq & 1) * 4096; q.xa_x = X0 + 16384 + xa;
    q.wa = hb + (l >> 4) * 2080 + (r >> 2) * 64 + (l & 3) * 8; q.wax = q.wa + wq * 256;
}
DI void q_first(const QCtx& q, bf16x4 (&af)[10][2], bf16x8 (&xb)[3]) {
    xb[2] = xfrag(q, 0, 2);
#pragma unroll
    for (int j = 0; j < 10; ++j) { const int jj = j == 0 ? 0 : (j == 1 ? 9 : j - 1); if (jj != 8) { af[jj][0] = wtr(q, 0, jj, 0); af[jj][1] = wtr(q, 0, jj, 1); } if (j == 1) { xb[0] = xfrag(q, 0, 0); xb[1] = xfrag(q, 0, 1); } }
}
constexpr size_t DISCARD = (size_t)128 << 20, CNT = (size_t)192 << 20;
constexpr int SNAP = STO + 768, CUR = X0 + 16384;
constexpr int k2_reg(int s) { return s == 0 ? VE : (s == 1 ? KO : (s == 2 ? KE : VO)); }
struct K2Ctx { lds_cptr lds; unsigned ldsw, wv, av, ba[4]; const char* wp; const char* ap; int kd, kq; };
DI unsigned k2_pos(const int st, const int kq) { return (unsigned)(((st + kq) & 15) + (st & 16) * 2); }
DI unsigned k2_woff(const int kd, const int st, const int half) { return (k2_pos(st, kd + half) * 32u + half * 512u) * (DM * 2); }
template <int SET, int I> DI void k2_dma(const K2Ctx& c, const int st) { DMAP("", c.ldsw, k2_reg(SET) + (I >> 1) * 8320 + (I & 1) * 1040, c.wv, c.wp + k2_woff(c.kd, st, I >> 1) + (I & 1) * (16 * DM * 2), 0); }
template <int I> DI void k2_lda(const K2Ctx& c, const int st, u32x4& d) {
    asm volatile("global_load_dwordx4 %0, %1, %2 sc1" : "=v"(d) : "v"(c.av), "s"(c.ap + k2_pos(st, c.kq) * 1024u + I * (NH * 4096)) : "memory");
}
struct Fin { Pend* pd; float lt[2], il[2]; int hb; char* cnt; unsigned pm, my, co, cur, am; K2Ctx c2; u32x4 (*a2)[3]; };
template <int I> DI void k2_lda_m(const K2Ctx& c, const int st, u32x4& d, const unsigned m) {
    asm volatile("s_mov_b32 exec_lo, %3\n\ts_mov_b32 exec_hi, %3\n\tglobal_load_dwordx4 %0, %1, %2 sc1\n\ts_mov_b64 exec, -1" : "=v"(d) : "v"(c.av), "s"(c.ap + k2_pos(st, c.kq) * 1024u + I * (NH * 4096)), "s"(m) : "memory");
}
DI void cnt_fetch(const char* cnt, const unsigned co, const unsigned ldsd, const unsigned lanes) {
    asm volatile("s_mov_b32 m0, %2\n\ts_mov_b32 exec_lo, %3\n\ts_mov_b32 exec_hi, 0\n\tglobal_load_lds_dword %0, %1 sc1\n\ts_mov_b64 exec, -1" :: "v"(co), "s"(cnt), "s"(ldsd), "s"(lanes) : "memory");
}
DI unsigned wave0(const int w) { unsigned m; asm("s_sub_u32 %0, %1, 1\n\ts_ashr_i32 %0, %0, 31" : "=s"(m) : "s"(w) : "scc"); return m; }
DI void publish(char* cnt, const unsigned lanes) {
    asm volatile("s_mov_b32 exec_lo, %3\n\ts_mov_b32 exec_hi, 0\n\tglobal_atomic_add %0, %1, %2 sc1\n\ts_mov_b64 exec, -1" :: "v"(0), "v"(1), "s"(cnt), "s"(lanes) : "memory");
}
template <int SLOT, bool FIRST, bool LAST, bool PRE = false, bool T5 = false, bool R2 = false>
DI void fast_tile(const int t, AttnCtx& c, const bf16x8 (&qf)[3][4], f32x4 (&o)[2][8], f32x4 (&ox)[2], f32x4 (&negm)[3], float (&lsum)[3], bf16x8 (&kf)[4],
                  QCtx& nq, const Seam& sm, Fin& f) {
    constexpr int SO = SLOT * RG, SN = (SLOT ^ 1) * RG;
    const f32x4 z4 = {0.f, 0.f, 0.f, 0.f};
    f32x4 s[2][4], sx, sr; u32x2 ph[2][4], px; bf16x4 vf[5][2]; bf16x8 pxf[2], kr[4]; bf16x4 vx[2][2];
    if (FIRST) {
#pragma unroll
        for (int ks = 0; ks < 4; ++ks) kr[ks] = ld128(c.lds + sm.kl + ks * 512);
    }
    const char* kn = c.kp + (t + 2) * 16384;
    const char* vn = c.vp + (t + 1) * 16384;
#pragma unroll
    for (int n = 0; n < 16; ++n) {
        const int i = n >> 2, ks = n & 3;
        s[0][i] = MFMA32(kf[ks], qf[0][ks], ks ? s[0][i] : ((FIRST && i == 0) ? z4 : negm[0]));
        if (n == 4) EXP2(sx, 0);
        if (n == 5) EXP2(s[0][0], 0);
        if (n == 6) EXP2(s[1][0], 0);
        if (n == 7) SUM4(sx, lsum[2]);
        if (n == 8) SUM4(s[0][0], lsum[0]);
        if (n == 9) EXP2(s[0][1], 0);
        if (n == 10) SUM4(s[1][0], lsum[1]);
        if (n == 11) EXP2(s[1][1], 0);
        if (n == 12) SUM4(s[0][1], lsum[0]);
        if (n == 13) SUM4(s[1][1], lsum[1]);
        if (n >= 14) { vf[n - 14][0] = vtr(c, SO, 0, n - 14); vf[n - 14][1] = vtr(c, SO, 1, n - 14); }
        SB();
        s[1][i] = MFMA32(kf[ks], qf[1][ks], ks ? s[1][i] : ((FIRST && i == 0) ? z4 : negm[1]));
        if (i == 0) { SB(); sx = MFMA32(kf[ks], qf[2][ks], ks ? sx : (FIRST ? z4 : negm[2])); if (FIRST) sr = MFMA32(kr[ks], qf[2][ks], ks ? sr : z4); }
        if (n < 12) kf[ks] = kfrag(c, SO, n + 4);
        if (n == 4) EXP2(sx, 1);
        if (n == 5) EXP2(s[0][0], 1);
        if (n == 6) EXP2(s[1][0], 1);
        if (n == 7) { PK4(sx, px); *LDSP(u32x2, (lds_ptr)c.lds + c.pxw) = px; }
        if (n == 8) PK4(s[0][0], ph[0][0]);
        if (n == 9) EXP2(s[0][1], 1);
        if (n == 10) PK4(s[1][0], ph[1][0]);
        if (n == 11) EXP2(s[1][1], 1);
        if (n == 12) PK4(s[0][1], ph[0][1]);
        if (n == 13) PK4(s[1][1], ph[1][1]);
        if (n == 14) { EXP2(s[0][2], 0); vf[2][0] = vtr(c, SO, 0, 2); }
        if (n == 15) { EXP2(s[0][2], 1); vf[2][1] = vtr(c, SO, 1, 2); }
        if (LAST && n == 12) *LDSP(float, (lds_ptr)c.lds + STO + nq.w * 64 + ((c.pxr >> 2) & 60)) = xsum(lsum[2]);
        if (FIRST && n == 0) publish(f.cnt + f.my, f.pm);
        if (T5 && n == 0) cnt_fetch(f.cnt, f.co, f.cur, f.pm * 15u);
        if (!LAST && (n & 3) == 0) DMA_V(c, SN, vn, n >> 2);
        if (LAST && !R2) { if (n == 0) w_dma<0, 0>(nq); if (n == 4) w_dma<0, 1>(nq); if (n == 8) w_dma<1, 0>(nq); if (n == 12) w_dma<1, 1>(nq); }
        if (LAST && R2) { if (n == 0) k2_dma<0, 0>(f.c2, 0); if (n == 4) k2_dma<0, 1>(f.c2, 0); if (n == 8) k2_dma<0, 2>(f.c2, 0); if (n == 12) k2_dma<0, 3>(f.c2, 0); }
        if (n == 1) PRIO(2); if (n == 5) PRIO(1); if (n == 9) PRIO(0);
        SB();
        if (FIRST && n == 3) {
            const float m0 = xmax(max4(s[0][0])), m1 = xmax(max4(s[1][0])), m2 = xmax(max4(sr));
            negm[0] = f32x4{-m0, -m0, -m0, -m0}; negm[1] = f32x4{-m1, -m1, -m1, -m1}; negm[2] = f32x4{-m2, -m2, -m2, -m2};
            s[0][0] -= m0; s[1][0] -= m1; sx -= m2;
            SB();
        }
        if (n == 12 && !FIRST) pace(t == 1 ? max(c.pc0, c.pc1) : c.pc0);
        if (n == 13) {
            WAIT_BAR((LAST && !R2) ? 7 : ((FIRST || T5) ? 9 : 8));
            PRIO(3); SB();
        }
    }
#pragma unroll
    for (int m = 0; m < 16; ++m) {
        const int pr = m >> 3, dt = m & 7, mm = m + 4;
        const bf16x8 a = cat(vf[m % 5][0], vf[m % 5][1]);
        const u32x4 p0 = __builtin_shufflevector(ph[0][2 * pr], ph[0][2 * pr + 1], 0, 1, 2, 3), p1 = __builtin_shufflevector(ph[1][2 * pr], ph[1][2 * pr + 1], 0, 1, 2, 3);
        if (FIRST && pr == 0) MFMA32Z(o[0][dt], a, p0); else MFMA32T(o[0][dt], a, p0);
        if (m == 0) vf[3][0] = vtr(c, SO, 0, 3);
        if (m < 12) vf[mm % 5][0] = vtr(c, SO, 2 * (mm >> 3), mm & 7);
        if (m == 0) EXP2(s[1][2], 0);
        if (m == 1) EXP2(s[0][3], 0);
        if (m == 2) EXP2(s[1][3], 0);
        if (m == 3) SUM4(s[0][2], lsum[0]);
        if (m == 4) SUM4(s[1][2], lsum[1]);
        if (m == 5) PK4(s[0][3], ph[0][3]);
        if (m == 6) PK4(s[1][3], ph[1][3]);
        if (m == 7 || m == 8) vx[m - 7][0] = vxtr(c, SO, 0, m - 7);
        if (m == 10 || m == 11) vx[m - 10][0] = vxtr(c, SO, 2, m - 10);
        if (!LAST && m >= 14) kf[2 * (m - 14)] = kfrag(c, SN, 2 * (m - 14));
        if (LAST) {
            if (m == 7) { f.lt[0] = xsum(lsum[0]); f.il[0] = __builtin_amdgcn_rcpf(f.lt[0]); PIN(f.il[0]); }
            if (m == 9 && !R2) q_addr(nq, f.hb);
            if (m >= 11 && (m & 1)) { f.pd->pk[(m - 11) >> 1] = opack(o[0][m - 11], o[0][m - 10], f.il[0]); PIN(f.pd->pk[(m - 11) >> 1]); }
        }
        SB();
        if (FIRST && pr == 0) MFMA32Z(o[1][dt], a, p1); else MFMA32T(o[1][dt], a, p1);
        if (m == 0) vf[3][1] = vtr(c, SO, 1, 3);
        if (m < 12) vf[mm % 5][1] = vtr(c, SO, 2 * (mm >> 3) + 1, mm & 7);
        if (m == 0) EXP2(s[1][2], 1);
        if (m == 1) EXP2(s[0][3], 1);
        if (m == 2) EXP2(s[1][3], 1);
        if (m == 3) PK4(s[0][2], ph[0][2]);
        if (m == 4) PK4(s[1][2], ph[1][2]);
        if (m == 5) SUM4(s[0][3], lsum[0]);
        if (m == 6) SUM4(s[1][3], lsum[1]);
        if (m == 5 || m == 6) pxf[m - 5] = ld128(c.lds + c.pxr + (m - 5) * 1024);
        if (m == 7 || m == 8) vx[m - 7][1] = vxtr(c, SO, 1, m - 7);
        if (m == 10 || m == 11) vx[m - 10][1] = vxtr(c, SO, 3, m - 10);
        if (!LAST && m >= 14) kf[2 * (m - 14) + 1] = kfrag(c, SN, 2 * (m - 14) + 1);
        if (LAST) {
            if (m == 8) { f.lt[1] = xsum(lsum[1]); f.il[1] = __builtin_amdgcn_rcpf(f.lt[1]); PIN(f.il[1]); }
            if (m >= 11 && (m & 1)) { f.pd->pk[4 + ((m - 11) >> 1)] = opack(o[1][m - 11], o[1][m - 10], f.il[1]); PIN(f.pd->pk[4 + ((m - 11) >> 1)]); }
        }
        if (m == 9 || m == 10) { SB(); if (FIRST) MFMA32Z(ox[m - 9], cat(vx[m - 9][0], vx[m - 9][1]), pxf[0]); else MFMA32T(ox[m - 9], cat(vx[m - 9][0], vx[m - 9][1]), pxf[0]); }
        if (m == 13 || m == 14) { SB(); MFMA32T(ox[m - 13], cat(vx[m - 13][0], vx[m - 13][1]), pxf[1]); }
        if (!LAST && !PRE && (m & 3) == 0) DMA_K(c, SO, kn, m >> 2);
        if (PRE && !R2) { if (m == 0) x_dma<0, 0>(nq); if (m == 4) x_dma<0, 1>(nq); if (m == 8) x_dma<0, 2>(nq); }
        if (PRE && R2) { if (m == 0) k2_dma<2, 0>(f.c2, 2); if (m == 4) k2_dma<2, 1>(f.c2, 2); if (m == 8) k2_dma<2, 2>(f.c2, 2); if (m == 12) k2_dma<2, 3>(f.c2, 2); }
        if (LAST && !R2) {
            if (m == 0) w_dma<2, 0>(nq); if (m == 4) w_dma<2, 1>(nq); if (m == 8) w_dma<3, 0>(nq); if (m == 12) w_dma<3, 1>(nq);
            if (m == 14) x_dma<1, 0>(nq); if (m == 15) { x_dma<1, 1>(nq); x_dma<1, 2>(nq); }
        }
        if (LAST && R2) {
            const int u = m < 3 ? m : m - 4;
            if (m < 3 || (m >= 7 && m < 13)) {
                if (u % 3 == 0) k2_lda_m<0>(f.c2, u / 3, f.a2[u / 3][0], f.am);
                if (u % 3 == 1) k2_lda_m<1>(f.c2, u / 3, f.a2[u / 3][1], f.am);
                if (u % 3 == 2) k2_lda_m<2>(f.c2, u / 3, f.a2[u / 3][2], f.am);
            }
            if (m == 3) k2_dma<1, 0>(f.c2, 1); if (m == 4) k2_dma<1, 1>(f.c2, 1); if (m == 5) k2_dma<1, 2>(f.c2, 1); if (m == 6) k2_dma<1, 3>(f.c2, 1);
            if (m == 14) { k2_dma<3, 0>(f.c2, 3); k2_dma<3, 1>(f.c2, 3); } if (m == 15) { k2_dma<3, 2>(f.c2, 3); k2_dma<3, 3>(f.c2, 3); }
        }
        if (m == 1) PRIO(2); if (m == 5) PRIO(1); if (m == 9) PRIO(0);
        SB();
        if (m == 12 && !LAST && !PRE) pace((FIRST || t == 1) ? max(c.pc0, c.pc1) : c.pc0);
        if (m == 13) { WAIT_BAR(LAST ? (R2 ? 63 : 6) : ((PRE && !R2) ? 7 : ((FIRST || T5) ? 9 : 8))); PRIO(3); SB(); }
    }
}

DI bf16x8 qfrag(const f32x4 t0, const f32x4 t1) {
    unsigned a0 = cvtpk(t0[0] * QSCALE, t0[1] * QSCALE), a1 = cvtpk(t0[2] * QSCALE, t0[3] * QSCALE);
    unsigned b0 = cvtpk(t1[0] * QSCALE, t1[1] * QSCALE), b1 = cvtpk(t1[2] * QSCALE, t1[3] * QSCALE);
    auto r0 = __builtin_amdgcn_permlane32_swap(a0, b0, false, false);
    auto r1 = __builtin_amdgcn_permlane32_swap(a1, b1, false, false);
    auto s0 = __builtin_amdgcn_permlane16_swap(r0[0], r0[1], false, false);
    auto s1 = __builtin_amdgcn_permlane16_swap(r1[0], r1[1], false, false);
    u32x4 u = {s0[0], s1[0], s0[1], s1[1]};
    return __builtin_bit_cast(bf16x8, u);
}

DI void slow_frag(const bf16x8 (&tq)[4], const char* kp, const char* vp, char* dst) {
    const int l = opaque_lane(), r = l & 15, g = l >> 4;
    float m = -3.0e38f, ls = 0.f; f32x4 acc[2] = {{0, 0, 0, 0}, {0, 0, 0, 0}};
#pragma unroll 1
    for (int kt = 0; kt < 32; ++kt) {
        f32x4 s = {0, 0, 0, 0};
#pragma unroll
        for (int ks = 0; ks < 4; ++ks) s = MFMA32(*(const bf16x8*)(kp + (kt * 16 + r) * 256 + ks * 64 + g * 16), tq[ks], s);
        const float mn = fmaxf(m, xmax(max4(s))), f = __builtin_amdgcn_exp2f(m - mn);
        m = mn; s -= mn; ls *= f; u32x2 pk;
        EXP4(s); SUMPK(s, ls, pk);
#pragma unroll
        for (int d = 0; d < 2; ++d) {
            const short* vq = (const short*)(vp + (kt * 16 + 4 * g) * 256 + (d * 16 + r) * 2);
            const bf16x4 a = {vq[0], vq[128], vq[256], vq[384]};
            acc[d] = MFMA16(a, __builtin_bit_cast(bf16x4, pk), acc[d] * f);
        }
    }
    ostore(dst + l * 16, opack(acc[0], acc[1], __builtin_amdgcn_rcpf(xsum(ls))));
}

DI void slow_q(bf16x8 (&tq)[4], const char* xr, const char* wc) {
    const int l = opaque_lane(), r = l & 15, g = l >> 4;
#pragma unroll 1
    for (int ks = 0; ks < 4; ++ks) {
        f32x4 a0 = {0, 0, 0, 0}, a1 = {0, 0, 0, 0};
#pragma unroll 1
        for (int kk = 0; kk < 20; ++kk) {
            const bf16x8 xb = *(const bf16x8*)(xr + r * (DM * 2) + kk * 64 + g * 16);
            const short* wp = (const short*)(wc + (size_t)(32 * kk + 8 * g) * (HD * 2) + (32 * ks + r) * 2);
            bf16x8 w0, w1;
#pragma unroll
            for (int j = 0; j < 8; ++j) { w0[j] = wp[j * HD]; w1[j] = wp[j * HD + 16]; }
            a0 = MFMA32(w0, xb, a0); a1 = MFMA32(w1, xb, a1);
        }
        tq[0] = tq[1]; tq[1] = tq[2]; tq[2] = tq[3]; tq[3] = qfrag(a0, a1);
    }
}
constexpr int q_w(int s) { return s >= 0 && s < 16 ? 2 : 0; }
constexpr int q_x(int s) { return s >= 0 && s < 17 && (s & 1) ? 3 : 0; }
constexpr bool q_k(int s) { return s <= KST && s > KST - 8 && !((KST - s) & 1); }
constexpr int q_post(int s) { return (s >= 0 && s <= 16 && !(s & 1) ? 1 : 0) + (s == TS0 ? 2 : 0) + (q_k(s) ? 1 : 0) + (s == 17 ? 4 : 0); }
constexpr int q_all(int s) { return q_w(s) + q_x(s) + q_post(s); }
constexpr int q_wait(int s) { return (s & 1) ? q_post(s - 2) + q_all(s - 1) : q_x(s - 3) + q_post(s - 3) + q_all(s - 2) + q_all(s - 1); }
template <int N, int L> DI void wait_bar() { WAIT_BARL(N, L); }
template <int S>
DI void q_step(QCtx& q, const AttnCtx& ac, const Pend& pd, f32x4 (&qa)[2][8], f32x4 (&qx)[2], bf16x4 (&af)[10][2], const bf16x8 (&xc)[3], bf16x8 (&xn)[3],
               bf16x8 (&qf)[3][4], bf16x8 (&kf)[4], const unsigned qxa, const Seam& sm) {
    constexpr int SL = S % 5, SN = (S + 1) % 5, SP = (S + 4) % 5, BJ = S == 19 ? 3 : 1;
#pragma unroll
    for (int j = 0; j < 9; ++j) {
        const bf16x8 a0 = cat(af[j][0], af[j][1]);
        if (j > 0) { if (S == 0) MFMA32Z(qa[0][j - 1], a0, xc[0]); else MFMA32T(qa[0][j - 1], a0, xc[0]); }
        else { if (S == 0) MFMA32Z(qx[0], a0, xc[2]); else MFMA32T(qx[0], a0, xc[2]); }
        if (j == 0) af[8][0] = wtr(q, SL, 8, 0);
        if (S < 19) {
            if (j >= 2 && j < 7) { af[j - 2][0] = wtr(q, SN, j - 2, 0); af[j - 2][1] = wtr(q, SN, j - 2, 1); }
            if (j >= 7) af[j - 1][0] = wtr(q, SN, j - 1, 0);
        } else {
            if (j == 2) *LDSP(bf16x8, (lds_ptr)q.lds + qxa + q.w * 1024) = qfrag(qx[0], qx[1]);
            if (j == 4 || j == 6 || j == 8) qf[0][(j - 4) >> 1] = qfrag(qa[0][j - 4], qa[0][j - 3]);
            if (j >= 4 && j < 8) qf[2][j - 4] = ld128(q.lds + qxa + (q.w >> 2) * 4096 + (j - 4) * 1024);
        }
        SB();
        if (j > 0) { if (S == 0) MFMA32Z(qa[1][j - 1], a0, xc[1]); else MFMA32T(qa[1][j - 1], a0, xc[1]); }
        else { const bf16x8 a1 = cat(af[9][0], af[9][1]); if (S == 0) MFMA32Z(qx[1], a1, xc[2]); else MFMA32T(qx[1], a1, xc[2]); }
        if (j == 0) af[8][1] = wtr(q, SL, 8, 1);
        if (S < 19) {
            if (j == 2) { af[9][0] = wtr(q, SN, 9, 0); af[9][1] = wtr(q, SN, 9, 1); }
            if (j == 3) xn[2] = xfrag(q, S + 1, 2);
            if (j == 4 || j == 5) xn[j - 4] = xfrag(q, S + 1, j - 4);
            if (j == 6) { af[5][0] = wtr(q, SN, 5, 0); af[5][1] = wtr(q, SN, 5, 1); }
            if (j >= 7) af[j - 1][1] = wtr(q, SN, j - 1, 1);
        } else {
            if (j == 4 || j == 6 || j == 8) qf[1][(j - 4) >> 1] = qfrag(qa[1][j - 4], qa[1][j - 3]);
            if (j >= 5) kf[j - 5] = kfrag(ac, 0, j - 5);
        }
        if (S < 16) { if (j == 2) w_dma<SP, 0>(q); if (j == 3) w_dma<SP, 1>(q); }
        if (S < 17 && (S & 1)) {
            if (j == 4) x_dma<((S >> 1) & 1), 0>(q);
            if (j == 5) x_dma<((S >> 1) & 1), 1>(q);
            if (j == 6) x_dma<((S >> 1) & 1), 2>(q);
            if (S == TS0 && j >= 7) touch<false>(sm.tb + (j - 7) * 1280, sm.to, sm.ldsd);
        }
        if (q_k(S) && j == ((S & 1) ? 7 : 5)) DMA_K(ac, KE, ac.kp, 3 - ((KST - S) >> 1));
        if (S < 17 && !(S & 1) && j == 6) pstore_m<(S >> 1)>(pd);
        if (S == 17 && j >= 2 && !(j & 1)) DMA_V(ac, 0, ac.vp, (j - 2) >> 1);
        if (S == 19 && j >= 5) DMA_K(ac, KO, ac.kp + 16384, j - 5);
        if (j == 3) PRIO(2); if (j == 5) PRIO(1); if (j == 7) PRIO(0);
        SB();
        if (j == BJ) {
            if (S == 0) wait_bar<7 + 2, 15>();
            else if (S == 1) wait_bar<q_wait(1) + 2, 8>();
            else if (S == 2) wait_bar<3 + 2 + q_all(0) + q_all(1), 15>();
            else if (S == 19) wait_bar<4, 0>();
            else wait_bar<q_wait(S), (S & 1) ? 8 : 15>();
            PRIO(3);
            SB();
        }
    }
}

template <int SET> DI bf16x8 k2_bfrag(const K2Ctx& c, const int n) {
    const unsigned b = c.ba[n >> 1]; const int im = k2_reg(SET) + (n & 1) * 32;
    return cat(ldtr(c.lds + b + im), ldtr(c.lds + b + im + 1040));
}
template <int SET, int MODE, int W, bool Z = false>
DI void k2_step(const K2Ctx& c, const int st, f32x4 (&acc)[3][8], u32x4 (&af)[4][3], bf16x8 (&bw)[8]) {
    constexpr int SN = (SET + 1) & 3, SA = (SET + 3) & 3;
#pragma unroll
    for (int n = 0; n < 8; ++n) {
        if (Z) MFMA32Z(acc[0][n], bw[n], af[SET][0]); else MFMA32T(acc[0][n], bw[n], af[SET][0]);
        if (n >= 2) { if (n == 2) bw[0] = k2_bfrag<SN>(c, 0); else bw[n - 1] = k2_bfrag<SN>(c, n - 1); }
        if (n == 0) bw[7] = k2_bfrag<SET>(c, 7);
        SB();
        if (Z) MFMA32Z(acc[1][n], bw[n], af[SET][1]); else MFMA32T(acc[1][n], bw[n], af[SET][1]);
        if (n == 2) bw[1] = k2_bfrag<SN>(c, 1);
        if (MODE <= 1) {
            if (n == 2) k2_lda<0>(c, st + 3, af[SA][0]);
            if (n == 4) k2_lda<1>(c, st + 3, af[SA][1]);
            if (n == 6) k2_lda<2>(c, st + 3, af[SA][2]);
        }
        if (MODE == 0) {
            if (n == 3) k2_dma<SET, 0>(c, st + 4);
            if (n == 5) k2_dma<SET, 1>(c, st + 4);
            if (n == 7) k2_dma<SET, 2>(c, st + 4);
        }
        SB();
        if (Z) MFMA32Z(acc[2][n], bw[n], af[SET][2]); else MFMA32T(acc[2][n], bw[n], af[SET][2]);
        if (MODE == 0 && n == 7) k2_dma<SET, 3>(c, st + 4);
        SB();
        if (n == 1) {
            asm volatile("s_waitcnt vmcnt(%3) lgkmcnt(0)\n\ts_barrier" : "+v"(af[SN][0]), "+v"(af[SN][1]), "+v"(af[SN][2]) : "i"(W) : "memory");
            SB();
        }
    }
}
#define NAPW 8
#define PATIENCE 2000000
DI unsigned k2_peek(const lds_cptr lds, const int rd) {
    const int l = opaque_lane();
    const unsigned long long no = __ballot(((l >> 1) & 1) == rd && *LDSP(const unsigned, lds + CUR + (l & 3) * 4) - *LDSP(const unsigned, lds + SNAP + (l & 3) * 4) < 8u);
    unsigned m; asm("s_cmp_eq_u64 %1, 0\n\ts_cselect_b32 %0, -1, 0" : "=s"(m) : "s"(no) : "scc");
    return m;
}
DI bool k2_wait(const lds_cptr lds, const char* cnt, const unsigned co, const int w) {
    if (w == 0) {
        const unsigned snap = *LDSP(const unsigned, lds + SNAP + (opaque_lane() & 3) * 4);
        const unsigned long long t0 = __builtin_amdgcn_s_memrealtime();
        unsigned ok;
        while (true) {
            unsigned v; asm volatile("global_load_dword %0, %1, %2 sc1\n\ts_waitcnt vmcnt(0)" : "=v"(v) : "v"(co), "s"(cnt) : "memory");
            ok = !__any(v - snap < 8u);
            if (ok || __builtin_amdgcn_s_memrealtime() - t0 >= PATIENCE) break;
            __builtin_amdgcn_s_sleep(NAPW);
        }
        *LDSP(unsigned, (lds_ptr)lds + CUR + 128) = ok;
    }
    asm volatile("s_waitcnt lgkmcnt(0)\n\ts_barrier" ::: "memory");
    return rfl(*LDSP(const unsigned, lds + CUR + 128)) != 0;
}
DI void k2_setup(K2Ctx& c, const K1Args& args, const lds_cptr lds, const unsigned ldsb, const char* ows, const int w, const int br, const int mt, const int nt) {
    const int l = opaque_lane(), kq = w & 3, mh = w >> 2;
    c.lds = lds; c.ldsw = ldsb + (mh ^ 1) * HB + kq * 2080; c.kd = (mh ^ 1) * 2; c.kq = kq;
    c.wp = args.wo[br] + (size_t)((mh ^ 1) * 2048) * (DM * 2) + nt * 256;
    c.ap = ows + (size_t)((br * 144 + mt * 6 + mh * 3) * NH + (kq >> 1) * 16 + (kq & 1) * 4) * 4096;
    c.wv = (4 * kq + ((l & 15) >> 2)) * (DM * 2) + (l >> 4) * 64 + (l & 3) * 16; c.av = l * 16;
}
template <bool RETRY>
DI bool k2_run(const K1Args& args, const lds_cptr lds, const unsigned ldsb, char* ows, const int w, const int br, const int mt, const int nt, const Pend& pd,
               char* cnt, const unsigned co, char* mycnt, u32x4 (&af)[4][3], const K2Ctx& c0) {
    const int l = opaque_lane(), r = l & 15, g = l >> 4, q4 = r >> 2, p = l & 3, kq = w & 3, mh = w >> 2;
    K2Ctx c;
    if (!RETRY) c = c0; else k2_setup(c, args, lds, ldsb, ows, w, br, mt, nt);
#pragma unroll
    for (int j = 0; j < 4; ++j) c.ba[j] = (kq >> 1) * HB + (kq & 1) * 8320 + g * 2080 + ((kq + j) & 3) * 256 + q4 * 64 + p * 8;
    f32x4 acc[3][8]; bf16x8 bw[8];
    if (RETRY) {
        k2_dma<0, 0>(c, 0); k2_dma<0, 1>(c, 0); k2_dma<0, 2>(c, 0); k2_dma<0, 3>(c, 0); k2_dma<1, 0>(c, 1); k2_dma<1, 1>(c, 1); k2_dma<1, 2>(c, 1); k2_dma<1, 3>(c, 1);
        k2_dma<2, 0>(c, 2); k2_dma<2, 1>(c, 2); k2_dma<2, 2>(c, 2); k2_dma<2, 3>(c, 2);
#pragma unroll
        for (int i = 0; i < 3; ++i) { k2_lda<0>(c, i, af[i][0]); k2_lda<1>(c, i, af[i][1]); k2_lda<2>(c, i, af[i][2]); }
    }
    if (RETRY) { k2_dma<3, 0>(c, 3); k2_dma<3, 1>(c, 3); k2_dma<3, 2>(c, 3); k2_dma<3, 3>(c, 3); }
    else { pstore<0>(pd); pstore<1>(pd); pstore<2>(pd); pstore<3>(pd); pstore<4>(pd); pstore<5>(pd); pstore<6>(pd); pstore<7>(pd); pstore<8>(pd); }
    asm volatile("s_waitcnt vmcnt(%3)\n\ts_barrier" : "+v"(af[0][0]), "+v"(af[0][1]), "+v"(af[0][2]) : "i"(RETRY ? 10 : 23) : "memory");
#pragma unroll
    for (int n = 0; n < 7; ++n) bw[n] = k2_bfrag<0>(c, n);
    SB();
    const unsigned w0 = wave0(w);
    k2_step<0, 0, RETRY ? 7 : 16, true>(c, 0, acc, af, bw); k2_step<1, 0, RETRY ? 11 : 20>(c, 1, acc, af, bw); k2_step<2, 0, 7>(c, 2, acc, af, bw);
    if (!RETRY) publish(mycnt, w0 & 1u);
    k2_step<3, 0, 7>(c, 3, acc, af, bw);
    for (int st = 4; st < 28; st += 4) {
        if (!RETRY) {
            if (st == 12 && __builtin_expect(!k2_peek(lds, 1), 0) && !k2_wait(lds, cnt, co, w)) { asm volatile("s_waitcnt vmcnt(0)" ::: "memory"); return false; }
            SB();
        }
        k2_step<0, 0, 7>(c, st, acc, af, bw);
        k2_step<1, 0, 7>(c, st + 1, acc, af, bw);
        if (!RETRY && st == 8) cnt_fetch(cnt, co, ldsb + CUR, w0 & 15u);
        k2_step<2, 0, 7>(c, st + 2, acc, af, bw); k2_step<3, 0, 7>(c, st + 3, acc, af, bw);
    }
    k2_step<0, 1, 7>(c, 28, acc, af, bw); k2_step<1, 2, 3>(c, 29, acc, af, bw); k2_step<2, 2, 0>(c, 30, acc, af, bw);
    bw[7] = k2_bfrag<3>(c, 7);
    asm volatile("s_waitcnt lgkmcnt(0)\n\ts_barrier" : "+v"(bw[7]) :: "memory");
    SB();
#pragma unroll
    for (int u = 0; u < 8; ++u) {
        const int n = (u + 2) & 7, d = u >> 1;
        MFMA32T(acc[0][n], bw[n], af[3][0]); MFMA32T(acc[1][n], bw[n], af[3][1]); MFMA32T(acc[2][n], bw[n], af[3][2]);
        if (u >= 2) {
#pragma unroll
            for (int i = 0; i < 3; ++i)
                *LDSP(f32x4, (lds_ptr)lds + (mh * 4 + ((kq + d) & 3)) * 18432 + ((d - 1) * 6144 + (i * 2 + (u & 1)) * 1024) + c.av) = acc[i][u];
        }
        SB();
    }
    MFMA_PAD();
    asm volatile("s_waitcnt lgkmcnt(0)\n\ts_barrier" ::: "memory");
#pragma unroll
    for (int d = 0; d < 3; ++d)
#pragma unroll
        for (int i = 0; i < 3; ++i)
#pragma unroll
            for (int a = 0; a < 2; ++a) acc[i][a] += *LDSP(const f32x4, lds + w * 18432 + (d * 6144 + (i * 2 + a) * 1024) + c.av);
#pragma unroll
    for (int i = 0; i < 3; ++i) {
        unsigned a0 = cvtpk(acc[i][0][0], acc[i][0][1]), a1 = cvtpk(acc[i][0][2], acc[i][0][3]);
        unsigned b0 = cvtpk(acc[i][1][0], acc[i][1][1]), b1 = cvtpk(acc[i][1][2], acc[i][1][3]);
        auto r0 = __builtin_amdgcn_permlane32_swap(a0, b0, false, false);
        auto r1 = __builtin_amdgcn_permlane32_swap(a1, b1, false, false);
        auto s0 = __builtin_amdgcn_permlane16_swap(r0[0], r0[1], false, false);
        auto s1 = __builtin_amdgcn_permlane16_swap(r1[0], r1[1], false, false);
        const u32x4 pk = {s0[0], s1[0], s0[1], s1[1]};
        const int row = (mt * 6 + mh * 3 + i) * 16 + r;
        asm volatile("global_store_dwordx4 %0, %1, off" UPOL "\n\ts_nop 1" :: "v"(args.out + (size_t)row * (2 * DM * 2) + br * (DM * 2) + nt * 256 + kq * 64 + g * 16), "v"(pk) : "memory");
    }
    return true;
}

struct Geo { const K1Args* a; int br, b, mt, nt, w, h0; bool has; };
DI Geo geo(int w) {
    auto ka = __builtin_amdgcn_kernarg_segment_ptr(); int bid = blockIdx.x;
    asm volatile("" : "+s"(ka), "+s"(bid), "+s"(w));
    const int xcd = bid & 7, idx = bid >> 3;
    Geo g; g.a = (const K1Args*)ka; g.br = xcd >> 2; g.b = ((xcd >> 1) & 1) * 8 + (idx & 7); g.has = idx < 30; g.mt = (xcd & 3) * 6 + min(idx, 29) / 5; g.nt = idx % 5; g.w = w; g.h0 = (xcd & 1) * 16 + (idx >> 3) * 2 + (w >> 2);
    return g;
}
DI unsigned cnt_off(const int l, const int br, const int mt) { return (unsigned)(((br * NB + (mt * 6 + 5 * (l & 1)) / 9) * 2 + ((l >> 1) & 1)) * 128); }
DI void finish(const lds_cptr lds, const int w, const int it, char* ob, const f32x4 (&o)[2][8], const f32x4 (&ox)[2], const Fin& fin, Pend& pd) {
    const int l2 = opaque_lane(), r2 = l2 & 15, wq = w & 3;
    float l3 = 0.f;
#pragma unroll
    for (int i = 0; i < 4; ++i) l3 += *LDSP(const float, lds + STO + (w >> 2) * 256 + i * 64 + r2 * 4);
    pd.pk[3] = opack(o[0][6], o[0][7], fin.il[0]); pd.pk[7] = opack(o[1][6], o[1][7], fin.il[1]);
    pd.pk[8] = opack(ox[0], ox[1], __builtin_amdgcn_rcpf(l3));
    const unsigned bad = (__any(!(fin.lt[0] < 4096.f)) ? 1u : 0u) | (__any(!(fin.lt[1] < 4096.f)) ? 2u : 0u) | (__any(!(l3 < 4096.f)) ? 4u : 0u);
    pd.ob[0] = ob + ((bad & 1) ? DISCARD : 0) + (size_t)(2 * wq) * (NH * 4096); pd.ob[1] = ob + ((bad & 2) ? DISCARD : 0) + (size_t)(2 * wq + 1) * (NH * 4096);
    pd.ob[2] = ob + ((bad & 4) ? DISCARD : 0) + (size_t)8 * (NH * 4096) + wq * 1024; pd.lo = l2 * 16; pd.em = ~0ull;
    if (__builtin_expect(bad != 0, 0)) {
        const Geo g2 = geo(w); const int wq2 = g2.w & 3, h = g2.h0 + 8 * it;
        const size_t ho = (size_t)(g2.b * NH + h) * (SK * DH * 2);
#pragma unroll 1
        for (int a = 0; a < 3; ++a) {
            if (!((bad >> a) & 1)) continue;
            const int qt = a < 2 ? 2 * wq2 + a : 8;
            bf16x8 tq[4] = {};
            slow_q(tq, g2.a->x[g2.br] + (size_t)(g2.b * SQ + qt * 16) * (DM * 2), g2.a->wq[g2.br] + h * (DH * 2));
#pragma unroll 1
            for (int s = a < 2 ? 0 : wq2; s < (a < 2 ? 4 : wq2 + 1); ++s) slow_frag(tq, g2.a->k[g2.br] + ho, g2.a->v[g2.br] + ho + s * 64, ob + (size_t)qt * (NH * 4096) + s * 1024);
        }
        asm volatile("s_waitcnt vmcnt(0)" ::: "memory");
    }
}
__global__ __launch_bounds__(512, 2) void k1_qattn(const K1Args args) {
    extern __shared__ __attribute__((aligned(16))) char smem[];
    const lds_cptr lds = (lds_cptr)smem;
    const unsigned ldsb = (unsigned)(uintptr_t)smem;
    const char* xb_; const char* wb_; char* ows;
    asm volatile("s_load_dwordx2 %0, %3, %4 offset:0\n\ts_load_dwordx2 %1, %3, %4 offset:48\n\ts_load_dwordx2 %2, %3, 80" : "=&s"(xb_), "=&s"(wb_), "=&s"(ows)
                 : "s"(__builtin_amdgcn_kernarg_segment_ptr()), "s"((blockIdx.x & 4) * 2) : "memory");
    SB();
    const int w = rfl(threadIdx.x >> 6), hd = w >> 2, wq = w & 3, hb = (hd ^ 1) * HB;
    const int xcd = blockIdx.x & 7, idx = blockIdx.x >> 3;
    const int br = xcd >> 2, b = ((xcd >> 1) & 1) * 8 + (idx & 7), h0 = (xcd & 1) * 16 + (idx >> 3) * 2 + hd;
    u32x4 a2[4][3];
    QCtx q;
    q.lds = lds; q.ldx[0] = ldsb + X0 + w * 2048; q.ldx[1] = ldsb + X1 + (w < 4 ? HB : 0) + (w & 3) * 2048; q.ldxx = ldsb + X0 + 16384 + w * 256; q.ldw = ldsb + hb + wq * 2080; q.w = w;
    {
        const int c0 = ((idx & 7) * 5 / 4 + (idx >> 3) * 5 / 2) % 10;
        q.wk = c0 * (64 * HD * 2); q.xk = c0 * 128;
    }
    const size_t hoff = (size_t)(b * NH + h0) * (SK * DH * 2);
    constexpr size_t HSTEP = (size_t)8 * SK * DH * 2;
    char* ob0;
    AttnCtx ac;
    ac.lds = lds; ac.ldk = ldsb + hb + wq * 4096; ac.ldv = ldsb + hb + VE + wq * 4224;
    Seam sm; sm.ldsd = ldsb + STO + 512;
    Pend pd;
    bf16x4 af[10][2]; bf16x8 xb0[3];
    {
        x_setup(q); w_setup(q);
        asm volatile("" : "+v"(q.xo[0]), "+v"(q.xo[1]), "+v"(q.xo[2]), "+v"(q.wv), "+s"(q.ldx[0]), "+s"(q.ldxx), "+s"(q.ldw), "+s"(q.wk), "+s"(q.xk));
        SB();
        asm volatile("s_waitcnt lgkmcnt(0)" : "+s"(xb_), "+s"(wb_), "+s"(ows) :: "memory");
        q.xp = xb_ + (size_t)b * (SQ * DM * 2); q.wp = wb_ + h0 * (DH * 2);
        const int l = opaque_lane();
        x_dma<0, 0>(q); x_dma<0, 1>(q); x_dma<0, 2>(q); w_dma<0, 0>(q); w_dma<0, 1>(q); w_dma<1, 0>(q); w_dma<1, 1>(q); w_dma<2, 0>(q); w_dma<2, 1>(q);
        w_dma<3, 0>(q); w_dma<3, 1>(q);
        x_dma<1, 0>(q); x_dma<1, 1>(q); x_dma<1, 2>(q);
        const unsigned po = (l & 1) * (unsigned)HSTEP + (l & 2) * 32768;
        touch<true>(args.v[br] + hoff, po, sm.ldsd);
        cnt_fetch(ows + CNT, cnt_off(l, br, geo(w).mt), ldsb + SNAP, wave0(w) & 15u);
        ob0 = ows + ((size_t)((br * NB + b) * 9) * NH + h0) * 4096;
    }
    bf16x8 qf[3][4], kf[4]; f32x4 o[2][8], ox[2], negm[3]; float lsum[3]; Fin fin;
#pragma unroll 1
    for (int it = 0; it < 2; ++it) {
        asm volatile("" : "+s"(it));
        if (it) {
            x_setup(q);
            SB();
            fast_tile<0, false, false, true>(6, ac, qf, o, ox, negm, lsum, kf, q, sm, fin);
            w_setup(q);
            SB();
            fast_tile<1, false, true>(7, ac, qf, o, ox, negm, lsum, kf, q, sm, fin);
            {
                const int l2 = opaque_lane(); const unsigned to = (unsigned)(((idx * 8 + w) * 40 + (l2 < 10 ? l2 : 9)) * 128);
                touch<false>(sm.tb + 2560, to, sm.ldsd); touch<false>(sm.tb + 3840, to, sm.ldsd);
            }
            q_first(q, af, xb0);
            MFMA_PAD(); SB();
            finish(lds, w, 0, ob0, o, ox, fin, pd);
            SB();
        } else {
#pragma unroll
            for (int i = 0; i < 9; ++i) pd.pk[i] = u32x4{0, 0, 0, 0};
            pd.ob[0] = pd.ob[1] = pd.ob[2] = ob0; pd.lo = 0; pd.em = 0;
            q_addr(q, hb);
            SB();
            WAIT_BAR(11);
            q_first(q, af, xb0);
        }
        const int l = opaque_lane(), r = l & 15, g = l >> 4, q4 = r >> 2, p = l & 3, row8 = (l >> 2) & 7, hf = l >> 5;
        const char* kh = args.k[br] + hoff + it * HSTEP;
        const char* vh = args.v[br] + hoff + it * HSTEP;
        ac.kp = kh + wq * 4096; ac.vp = vh + wq * 4096;
#pragma unroll
        for (int j = 0; j < 2; ++j) ac.kvo[j] = row8 * 256 + hf * 64 + ((p ^ fsw((j << 1) | (row8 >> 2))) * 16);
        ac.vvo = q4 * 256 + g * 64 + p * 16;
        ac.pc1 = it ? 0 : PACE;
        ac.pc0 = (xcd & 1) ? (it ? THR2 : THR) : 0;
        sm.kl = hb + (r >> 3) * 2048 + (r & 7) * 64 + ((g ^ fsw((r >> 2) & 3)) * 16);
        ac.ka[0] = sm.kl + wq * 4096;
        sm.to = (unsigned)(((idx * 8 + w) * 40 + (l < 10 ? l : 9)) * 128) & (0u - (unsigned)it);
        sm.tb = args.wo[br] + (size_t)(xcd & 3) * (32 * 320 * 128);
        {
            f32x4 qa[2][8], qx[2]; bf16x8 xb1[3];
            const unsigned qxa = X0 + l * 16;
            SB();
#define QS2(S) q_step<S>(q, ac, pd, qa, qx, af, xb0, xb1, qf, kf, qxa, sm); q_step<S + 1>(q, ac, pd, qa, qx, af, xb1, xb0, qf, kf, qxa, sm)
            QS2(0); QS2(2);
            QS2(4); QS2(6); QS2(8); QS2(10); QS2(12); QS2(14);
            QS2(16); QS2(18);
            MFMA_PAD(); SB();
            qf[0][3] = qfrag(qa[0][6], qa[0][7]); qf[1][3] = qfrag(qa[1][6], qa[1][7]);
        }
        q.wp += 8 * DH * 2;
        const unsigned vl = hb + VE + g * 1056 + q4 * 64 + p * 8;
#pragma unroll
        for (int i = 0; i < 4; ++i) { const unsigned kt = (unsigned)((i + wq) & 3); ac.ka[i] = sm.kl + kt * 4096; ac.va[i] = vl + kt * 4224; }
        ac.vax = vl + wq * 256; ac.pxr = PXO + hd * 2048 + l * 16; ac.pxw = ac.pxr + (wq >> 1) * 1024 + (wq & 1) * 8;
        lsum[0] = lsum[1] = lsum[2] = 0.f;
        fin.pd = &pd; fin.hb = hb; fin.pm = wave0(w) & (unsigned)it; fin.a2 = a2;
        {
            const Geo g2 = geo(w);
            fin.cnt = g2.a->ows + CNT; fin.my = (g2.br * NB + g2.b) * 256; fin.co = cnt_off(opaque_lane(), g2.br, g2.mt);
            k2_setup(fin.c2, *g2.a, lds, ldsb, g2.a->ows, g2.w, g2.br, g2.mt, g2.nt);
        }
        SB();
        fin.cur = ldsb + CUR;
        fast_tile<0, true, false>(0, ac, qf, o, ox, negm, lsum, kf, q, sm, fin);
        for (int t = 1; t < 5; t += 2) {
            fast_tile<1, false, false>(t, ac, qf, o, ox, negm, lsum, kf, q, sm, fin);
            fast_tile<0, false, false>(t + 1, ac, qf, o, ox, negm, lsum, kf, q, sm, fin);
        }
        fast_tile<1, false, false, false, true>(5, ac, qf, o, ox, negm, lsum, kf, q, sm, fin);
    }
    fast_tile<0, false, false, true, false, true>(6, ac, qf, o, ox, negm, lsum, kf, q, sm, fin);
    const unsigned go = k2_peek(lds, 0);
    fin.am = go;
    SB();
    fast_tile<1, false, true, false, false, true>(7, ac, qf, o, ox, negm, lsum, kf, q, sm, fin);
    MFMA_PAD(); SB();
    finish(lds, w, 1, ob0 + 8 * 4096, o, ox, fin, pd);
    SB();
    const Geo g2 = geo(w); const K1Args& A = *g2.a;
    char* const cnt = A.ows + CNT; char* const mycnt = cnt + (g2.br * NB + g2.b) * 256 + 128;
    const unsigned co = cnt_off(opaque_lane(), g2.br, g2.mt);
    bool ok = true;
    if (__builtin_expect(go && g2.has, 1)) ok = k2_run<false>(A, lds, ldsb, A.ows, g2.w, g2.br, g2.mt, g2.nt, pd, cnt, co, mycnt, a2, fin.c2);
    else {
        pstore<0>(pd); pstore<1>(pd); pstore<2>(pd); pstore<3>(pd); pstore<4>(pd); pstore<5>(pd); pstore<6>(pd); pstore<7>(pd); pstore<8>(pd);
        asm volatile("s_waitcnt vmcnt(0)\n\ts_barrier" ::: "memory");
        publish(mycnt, wave0(w) & 1u);
        if (g2.has) ok = k2_wait(lds, cnt, co, w);
    }
    if (__builtin_expect((!ok || !go) && g2.has, 0)) {
#pragma unroll 1
        for (int u = ok ? 6 * NH : g2.w; u < 6 * NH; u += 8) {
            const int rt = g2.mt * 6 + (u >> 5), h = u & 31, bb = rt / 9;
            const size_t ho = (size_t)(bb * NH + h) * (SK * DH * 2);
            bf16x8 tq[4] = {};
            slow_q(tq, A.x[g2.br] + (size_t)rt * (16 * DM * 2), A.wq[g2.br] + h * (DH * 2));
#pragma unroll 1
            for (int s = 0; s < 4; ++s) slow_frag(tq, A.k[g2.br] + ho, A.v[g2.br] + ho + s * 64, A.ows + ((size_t)(g2.br * 144 + rt) * NH + h) * 4096 + s * 1024);
        }
        asm volatile("s_waitcnt vmcnt(0) lgkmcnt(0)\n\ts_barrier" ::: "memory");
        k2_run<true>(A, lds, ldsb, A.ows, g2.w, g2.br, g2.mt, g2.nt, pd, cnt, co, mycnt, a2, fin.c2);
    }
}

extern "C" void kernel_launch(void* const* d_in, const int* in_sizes, int n_in, void* d_out, int out_size, void* d_ws, size_t ws_size,
                              hipStream_t stream) {
    (void)in_sizes; (void)n_in; (void)out_size; (void)ws_size;
    (void)hipFuncSetAttribute((const void*)k1_qattn, hipFuncAttributeMaxDynamicSharedMemorySize, K1_LDS);
    K1Args a1;
    a1.x[0] = (const char*)d_in[0]; a1.k[0] = (const char*)d_in[1]; a1.v[0] = (const char*)d_in[2]; a1.wq[0] = (const char*)d_in[3];
    a1.x[1] = (const char*)d_in[5]; a1.k[1] = (const char*)d_in[6]; a1.v[1] = (const char*)d_in[7]; a1.wq[1] = (const char*)d_in[8];
    a1.wo[0] = (const char*)d_in[4]; a1.wo[1] = (const char*)d_in[9]; a1.ows = (char*)d_ws; a1.out = (char*)d_out;
    hipLaunchKernelGGL(k1_qattn, dim3(256), dim3(512), K1_LDS, stream, a1);
}
```

```cpp
#include <hip/hip_runtime.h>
#include <hip/hip_bf16.h>

typedef short bf16x8 __attribute__((ext_vector_type(8)));
typedef short bf16x4 __attribute__((ext_vector_type(4)));
typedef float f32x4 __attribute__((ext_vector_type(4)));
typedef float f32x2 __attribute__((ext_vector_type(2)));
typedef __bf16 bf2_t __attribute__((ext_vector_type(2)));
typedef unsigned u32x4 __attribute__((ext_vector_type(4)));
typedef unsigned u32x2 __attribute__((ext_vector_type(2)));
typedef __attribute__((address_space(3))) char* lds_ptr;
typedef __attribute__((address_space(3))) const char* lds_cptr;
#define DI __device__ __forceinline__
#define LDSP(T, p) ((__attribute__((address_space(3))) T*)(p))

constexpr int NB = 16, SQ = 144, DM = 640, NH = 32, SK = 512, DH = 128, HD = NH * DH;
constexpr float QSCALE = 0.12751743082459868f;

#define MFMA32(a, b, c) __builtin_amdgcn_mfma_f32_16x16x32_bf16((a), (b), (c), 0, 0, 0)
#define MFMA16(a, b, c) __builtin_amdgcn_mfma_f32_16x16x16bf16_1k((a), (b), (c), 0, 0, 0)

#define MFMA32T(acc, a, b) asm volatile("v_mfma_f32_16x16x32_bf16 %0, %1, %2, %0" : "+v"(acc) : "v"(a), "v"(b))
#define MFMA32Z(acc, a, b) asm volatile("v_mfma_f32_16x16x32_bf16 %0, %1, %2, 0" : "=&v"(acc) : "v"(a), "v"(b))
#define MFMA16T(acc, a, b) asm volatile("v_mfma_f32_16x16x16_bf16 %0, %1, %2, %0" : "+v"(acc) : "v"(a), "v"(b))
#define MFMA16Z(acc, a, b) asm volatile("v_mfma_f32_16x16x16_bf16 %0, %1, %2, 0" : "=&v"(acc) : "v"(a), "v"(b))
#define MFMA_PAD() asm volatile("s_nop 7\n\ts_nop 3" ::: "memory")
DI unsigned cvtpk(float lo, float hi) { f32x2 v = {lo, hi}; bf2_t b = __builtin_convertvector(v, bf2_t); return __builtin_bit_cast(unsigned, b); }
DI bf16x8 ld128(lds_cptr p) { return *LDSP(const bf16x8, p); }
DI bf16x4 ldtr(lds_cptr p) { return __builtin_amdgcn_ds_read_tr16_b64_v4i16(LDSP(bf16x4, (lds_ptr)p)); }
DI bf16x8 cat(bf16x4 lo, bf16x4 hi) { return __builtin_shufflevector(lo, hi, 0, 1, 2, 3, 4, 5, 6, 7); }
DI int fsw(int u) { return (((u ^ (u >> 1)) & 1) << 1) | ((u >> 1) & 1); }
DI unsigned rfl(unsigned x) { return __builtin_amdgcn_readfirstlane(x); }

#define WAIT_BARL(N, L) asm volatile("s_waitcnt vmcnt(%1) lgkmcnt(%0)\n\ts_barrier" :: "i"(L), "i"(N) : "memory")
#define WAIT_BAR(N) WAIT_BARL(N, 0)
constexpr int RG = 16896, HB = 4 * RG, KE = 0, KO = RG, VE = 2 * RG, VO = 3 * RG;
constexpr int X0 = 2 * HB;
constexpr int X1 = VO + 8448;
constexpr int X1X = X0 + 18432;
constexpr int PXO = X1X + 2048;
constexpr int STO = PXO + 4096;
constexpr int K1_LDS = STO + 1024;
#define SB() __builtin_amdgcn_sched_barrier(0)
#define PRIO(x) __builtin_amdgcn_s_setprio(x)
#define TS0 5
#define KST 14
#define PIN(x) asm volatile("" : "+v"(x))
#define DMAP(POL, m0b, M0OFF, vo, sb, IMM) \
    asm volatile("s_add_u32 m0, %0, %4\n\ts_nop 0\n\tglobal_load_lds_dwordx4 %1, %2 offset:%3" POL :: "s"(m0b), "v"(vo), "s"(sb), "i"(IMM), "i"(M0OFF) : "memory", "scc")

struct K1Args { const char* x[2]; const char* k[2]; const char* v[2]; const char* wq[2]; const char* wo[2]; char* ows; char* out; };

struct AttnCtx {
    lds_cptr lds; unsigned ldk, ldv;
    unsigned ka[4], va[4], vax, pxr, pxw, kvo[2], vvo;
    const char* kp; const char* vp;
    int pc0, pc1;
};
#define KVPOL " nt"
template <int SO, int J> DI void dma_k(const AttnCtx& c, const char* kt) { DMAP(KVPOL, c.ldk, SO + (J & 1) * 896, c.kvo[J >> 1], kt, (J >> 1) * 2048 + (J & 1) * 128); }
template <int SO, int J> DI void dma_v(const AttnCtx& c, const char* vt) { DMAP(KVPOL, c.ldv, SO + J * 32, c.vvo, vt, J * 1024); }
#define DMA_K(c, SO, t, j) do { switch (j) { case 0: dma_k<SO, 0>(c, t); break; case 1: dma_k<SO, 1>(c, t); break; case 2: dma_k<SO, 2>(c, t); break; default: dma_k<SO, 3>(c, t); break; } } while (0)
#define DMA_V(c, SO, t, j) do { switch (j) { case 0: dma_v<SO, 0>(c, t); break; case 1: dma_v<SO, 1>(c, t); break; case 2: dma_v<SO, 2>(c, t); break; default: dma_v<SO, 3>(c, t); break; } } while (0)
DI bf16x8 kfrag(const AttnCtx& c, int so, int n) { return ld128(c.lds + c.ka[n >> 2] + (so + (n & 3) * 512)); }
DI bf16x4 vtr(const AttnCtx& c, int so, int i, int dt) { return ldtr(c.lds + c.va[i] + (so + (dt >> 1) * 256 + (dt & 1) * 32)); }
DI bf16x4 vxtr(const AttnCtx& c, int so, int kt, int d) { return ldtr(c.lds + c.vax + (so + kt * 4224 + d * 32)); }
DI float xmax(float m) {
    auto a = __builtin_amdgcn_permlane32_swap(__float_as_uint(m), __float_as_uint(m), false, false);
    m = fmaxf(__uint_as_float(a[0]), __uint_as_float(a[1]));
    auto b = __builtin_amdgcn_permlane16_swap(__float_as_uint(m), __float_as_uint(m), false, false);
    return fmaxf(__uint_as_float(b[0]), __uint_as_float(b[1]));
}
DI float xsum(float m) {
    auto a = __builtin_amdgcn_permlane32_swap(__float_as_uint(m), __float_as_uint(m), false, false);
    m = __uint_as_float(a[0]) + __uint_as_float(a[1]);
    auto b = __builtin_amdgcn_permlane16_swap(__float_as_uint(m), __float_as_uint(m), false, false);
    return __uint_as_float(b[0]) + __uint_as_float(b[1]);
}
DI float max4(const f32x4 v) { return fmaxf(fmaxf(v[0], v[1]), fmaxf(v[2], v[3])); }
#define EXP4(v) do { v[0] = __builtin_amdgcn_exp2f(v[0]); v[1] = __builtin_amdgcn_exp2f(v[1]); v[2] = __builtin_amdgcn_exp2f(v[2]); v[3] = __builtin_amdgcn_exp2f(v[3]); PIN(v); } while (0)
#define EXP2(v, h) do { v[2 * (h)] = __builtin_amdgcn_exp2f(v[2 * (h)]); v[2 * (h) + 1] = __builtin_amdgcn_exp2f(v[2 * (h) + 1]); PIN(v); } while (0)
#define SUM4(v, ls) do { ls += v[0]; ls += v[1]; ls += v[2]; ls += v[3]; PIN(ls); } while (0)
#define PK4(v, d) do { d = u32x2{cvtpk(v[0], v[1]), cvtpk(v[2], v[3])}; PIN(d); } while (0)
#define SUMPK(v, ls, d) do { ls += v[0]; ls += v[1]; ls += v[2]; ls += v[3]; d = u32x2{cvtpk(v[0], v[1]), cvtpk(v[2], v[3])}; PIN(ls); PIN(d); } while (0)

struct QCtx {
    lds_cptr lds; unsigned ldx[2], ldxx, ldw; int w;
    unsigned xo[3], wv, xa_own[2], xa_x, wa[4];
    unsigned wk, xk;
    const char* xp; const char* wp;
};
constexpr int wslot(int sl) { return sl < 2 ? VE + sl * 8320 : (sl < 4 ? KO + (sl - 2) * 8320 : VO); }
template <int SL, int J> DI void w_dma(QCtx& q) {
    DMAP("", q.ldw, wslot(SL) + J * 1040, q.wv, q.wp + q.wk + J * (4 * HD * 2), 0);
    if (J == 1) { q.wk += 32 * HD * 2; if (q.wk >= DM * HD * 2) q.wk = 0; }
}
template <int ST, int J> DI void x_dma(QCtx& q) {
    if (J < 2) DMAP("", q.ldx[ST], J * 1024, q.xo[J], q.xp + q.xk, 0);
    else {
        asm volatile("s_add_u32 m0, %0, %3\n\ts_nop 0\n\tglobal_load_lds_dword %1, %2" :: "s"(q.ldxx), "v"(q.xo[2]), "s"(q.xp + q.xk), "i"(ST * 2048) : "memory", "scc");
        q.xk += 128; if (q.xk >= DM * 2) q.xk = 0;
    }
}
DI int opaque_lane() { int l; asm volatile("v_mbcnt_lo_u32_b32 %0, -1, 0\n\tv_mbcnt_hi_u32_b32 %0, -1, %0" : "=v"(l)); return l; }
DI void x_setup(QCtx& q) {
    const int l = opaque_lane(), p = l & 3, row8 = (l >> 2) & 7, hf = l >> 5, w = q.w;
#pragma unroll
    for (int j = 0; j < 2; ++j) q.xo[j] = (16 * w + 8 * j + row8) * (DM * 2) + hf * 64 + ((p ^ fsw((j << 1) | (row8 >> 2))) * 16);
    q.xo[2] = (128 + 8 * (w >> 2) + 4 * (w & 1) + (l >> 4)) * (DM * 2) + ((w >> 1) & 1) * 64 + (((row8 & 3) ^ fsw(2 * (w >> 2) + (w & 1))) * 16) + p * 4;
}
DI void w_setup(QCtx& q) {
    const int l = opaque_lane();
    q.wv = (8 * (q.w & 3) + ((l & 15) >> 2)) * (HD * 2) + (l >> 4) * 64 + (l & 3) * 16;
}
template <bool NT> DI void touch(const char* p, const unsigned off, const unsigned ldsd) {
    if (NT) asm volatile("s_mov_b32 m0, %2\n\ts_nop 0\n\tglobal_load_lds_dword %0, %1 nt" :: "v"(off), "s"(p), "s"(ldsd) : "memory");
    else asm volatile("s_mov_b32 m0, %2\n\ts_nop 0\n\tglobal_load_lds_dword %0, %1" :: "v"(off), "s"(p), "s"(ldsd) : "memory");
}
struct Seam { unsigned ldsd, to, kl; const char* tb; };
#define PACE 11
#define THR 9
#define THR2 10
DI void pace(int n) { asm volatile("s_cmp_eq_u32 %0, 0\n\ts_cbranch_scc1 2f\n1:\n\ts_sleep 1\n\ts_sub_u32 %0, %0, 1\n\ts_cmp_lg_u32 %0, 0\n\ts_cbranch_scc1 1b\n2:" : "+s"(n) :: "scc"); }

#define UPOL " sc1"
#define OPOL " sc1"
DI void ostore(char* p, const u32x4 v) { asm volatile("global_store_dwordx4 %0, %1, off" OPOL "\n\ts_nop 1" :: "v"(p), "v"(v) : "memory"); }
struct Pend { u32x4 pk[9]; char* ob[3]; unsigned lo; unsigned long long em; };
template <int I> DI void pstore(const Pend& p) { asm volatile("global_store_dwordx4 %0, %1, %2 offset:%3" OPOL "\n\ts_nop 1" :: "v"(p.lo), "v"(p.pk[I]), "s"(p.ob[I >> 2]), "i"((I & 3) * 1024) : "memory"); }
template <int I> DI void pstore_m(const Pend& p) {
    asm volatile("s_mov_b64 exec, %4\n\tglobal_store_dwordx4 %0, %1, %2 offset:%3" OPOL "\n\ts_mov_b64 exec, -1\n\ts_nop 0" :: "v"(p.lo), "v"(p.pk[I]), "s"(p.ob[I >> 2]), "i"((I & 3) * 1024), "s"(p.em) : "memory");
}
DI u32x4 opack(const f32x4 u, const f32x4 v, const float il) {
    return u32x4{cvtpk(u[0] * il, u[1] * il), cvtpk(u[2] * il, u[3] * il), cvtpk(v[0] * il, v[1] * il), cvtpk(v[2] * il, v[3] * il)};
}
DI bf16x4 wtr(const QCtx& q, const int sl, const int j, const int hi) {
    return ldtr(q.lds + q.wa[j >> 1] + (wslot(sl) + (j & 1) * 32 + hi * 1040));
}
DI bf16x8 xfrag(const QCtx& q, const int s, const int i) {
    return i < 2 ? ld128(q.lds + q.xa_own[(s >> 1) & 1] + ((s & 1) * 512 + i * 2048)) : ld128(q.lds + q.xa_x + (((s >> 1) & 1) * 2048 + (s & 1) * 512));
}
DI void q_addr(QCtx& q, const int hb) {
    const int l = opaque_lane(), r = l & 15, wq = q.w & 3;
    const unsigned xa = (r >> 3) * 1024 + (r & 7) * 64 + (((l >> 4) ^ fsw((r >> 2) & 3)) * 16);
    q.xa_own[0] = X0 + xa + wq * 4096; q.xa_own[1] = X1 + (wq < 2 ? HB : 0) + xa + (wq & 1) * 4096; q.xa_x = X0 + 16384 + xa;
    #pragma unroll
    for (int p = 0; p < 4; ++p) q.wa[p] = hb + (l >> 4) * 2080 + (r >> 2) * 64 + (l & 3) * 8 + (p ^ wq) * 256;
}
DI void q_first(const QCtx& q, bf16x4 (&af)[8][2], bf16x8 (&xb)[3]) {
    xb[2] = xfrag(q, 0, 2);
#pragma unroll
    for (int j = 0; j < 7; ++j) { af[j][0] = wtr(q, 0, j, 0); af[j][1] = wtr(q, 0, j, 1); if (j == 1) { xb[0] = xfrag(q, 0, 0); xb[1] = xfrag(q, 0, 1); } }
}
constexpr size_t DISCARD = (size_t)128 << 20, CNT = (size_t)192 << 20;
constexpr int SNAP = STO + 768, CUR = X0 + 16384;
constexpr int k2_reg(int s) { return s == 0 ? VE : (s == 1 ? KO : (s == 2 ? KE : VO)); }
struct K2Ctx { lds_cptr lds; unsigned ldsw, wv, av, ba[4]; const char* wp; const char* ap; int kd, kq; };
DI unsigned k2_pos(const int st, const int kq) { return (unsigned)(((st + kq) & 15) + (st & 16) * 2); }
DI unsigned k2_woff(const int kd, const int st, const int half) { return (k2_pos(st, kd + half) * 32u + half * 512u) * (DM * 2); }
template <int SET, int I> DI void k2_dma(const K2Ctx& c, const int st) { DMAP("", c.ldsw, k2_reg(SET) + (I >> 1) * 8320 + (I & 1) * 1040, c.wv, c.wp + k2_woff(c.kd, st, I >> 1) + (I & 1) * (16 * DM * 2), 0); }
template <int I> DI void k2_lda(const K2Ctx& c, const int st, u32x4& d) {
    asm volatile("global_load_dwordx4 %0, %1, %2 sc1" : "=v"(d) : "v"(c.av), "s"(c.ap + k2_pos(st, c.kq) * 1024u + I * (NH * 4096)) : "memory");
}
struct Fin { Pend* pd; float lt[2], il[2]; int hb; char* cnt; unsigned pm, my, co, cur, am; K2Ctx c2; u32x4 (*a2)[3]; };
template <int I> DI void k2_lda_m(const K2Ctx& c, const int st, u32x4& d, const unsigned m) {
    asm volatile("s_mov_b32 exec_lo, %3\n\ts_mov_b32 exec_hi, %3\n\tglobal_load_dwordx4 %0, %1, %2 sc1\n\ts_mov_b64 exec, -1" : "=v"(d) : "v"(c.av), "s"(c.ap + k2_pos(st, c.kq) * 1024u + I * (NH * 4096)), "s"(m) : "memory");
}
DI void cnt_fetch(const char* cnt, const unsigned co, const unsigned ldsd, const unsigned lanes) {
    asm volatile("s_mov_b32 m0, %2\n\ts_mov_b32 exec_lo, %3\n\ts_mov_b32 exec_hi, 0\n\tglobal_load_lds_dword %0, %1 sc1\n\ts_mov_b64 exec, -1" :: "v"(co), "s"(cnt), "s"(ldsd), "s"(lanes) : "memory");
}
DI unsigned wave0(const int w) { unsigned m; asm("s_sub_u32 %0, %1, 1\n\ts_ashr_i32 %0, %0, 31" : "=s"(m) : "s"(w) : "scc"); return m; }
DI void publish(char* cnt, const unsigned lanes) {
    asm volatile("s_mov_b32 exec_lo, %3\n\ts_mov_b32 exec_hi, 0\n\tglobal_atomic_add %0, %1, %2 sc1\n\ts_mov_b64 exec, -1" :: "v"(0), "v"(1), "s"(cnt), "s"(lanes) : "memory");
}
template <int SLOT, bool FIRST, bool LAST, bool PRE = false, bool T5 = false, bool R2 = false>
DI void fast_tile(const int t, AttnCtx& c, const bf16x8 (&qf)[3][4], f32x4 (&o)[2][8], f32x4 (&ox)[2], f32x4 (&negm)[3], float (&lsum)[3], bf16x8 (&kf)[4],
                  QCtx& nq, const Seam& sm, Fin& f) {
    constexpr int SO = SLOT * RG, SN = (SLOT ^ 1) * RG;
    const f32x4 z4 = {0.f, 0.f, 0.f, 0.f};
    f32x4 s[2][4], sx, sr; u32x2 ph[2][4], px; bf16x4 vf[5][2]; bf16x8 pxf[2], kr[4]; bf16x4 vx[2][2];
    if (FIRST) {
#pragma unroll
        for (int ks = 0; ks < 4; ++ks) kr[ks] = ld128(c.lds + sm.kl + ks * 512);
    }
    const char* kn = c.kp + (t + 2) * 16384;
    const char* vn = c.vp + (t + 1) * 16384;
#pragma unroll
    for (int n = 0; n < 16; ++n) {
        const int i = n >> 2, ks = n & 3;
        s[0][i] = MFMA32(kf[ks], qf[0][ks], ks ? s[0][i] : ((FIRST && i == 0) ? z4 : negm[0]));
        if (n == 4) EXP2(sx, 0);
        if (n == 5) EXP2(s[0][0], 0);
        if (n == 6) EXP2(s[1][0], 0);
        if (n == 7) SUM4(sx, lsum[2]);
        if (n == 8) SUM4(s[0][0], lsum[0]);
        if (n == 9) EXP2(s[0][1], 0);
        if (n == 10) SUM4(s[1][0], lsum[1]);
        if (n == 11) EXP2(s[1][1], 0);
        if (n == 12) SUM4(s[0][1], lsum[0]);
        if (n == 13) SUM4(s[1][1], lsum[1]);
        if (n >= 14) { vf[n - 14][0] = vtr(c, SO, 0, n - 14); vf[n - 14][1] = vtr(c, SO, 1, n - 14); }
        SB();
        s[1][i] = MFMA32(kf[ks], qf[1][ks], ks ? s[1][i] : ((FIRST && i == 0) ? z4 : negm[1]));
        if (i == 0) { SB(); sx = MFMA32(kf[ks], qf[2][ks], ks ? sx : (FIRST ? z4 : negm[2])); if (FIRST) sr = MFMA32(kr[ks], qf[2][ks], ks ? sr : z4); }
        if (n < 12) kf[ks] = kfrag(c, SO, n + 4);
        if (n == 4) EXP2(sx, 1);
        if (n == 5) EXP2(s[0][0], 1);
        if (n == 6) EXP2(s[1][0], 1);
        if (n == 7) { PK4(sx, px); *LDSP(u32x2, (lds_ptr)c.lds + c.pxw) = px; }
        if (n == 8) PK4(s[0][0], ph[0][0]);
        if (n == 9) EXP2(s[0][1], 1);
        if (n == 10) PK4(s[1][0], ph[1][0]);
        if (n == 11) EXP2(s[1][1], 1);
        if (n == 12) PK4(s[0][1], ph[0][1]);
        if (n == 13) PK4(s[1][1], ph[1][1]);
        if (n == 14) { EXP2(s[0][2], 0); vf[2][0] = vtr(c, SO, 0, 2); }
        if (n == 15) { EXP2(s[0][2], 1); vf[2][1] = vtr(c, SO, 1, 2); }
        if (LAST && n == 12) *LDSP(float, (lds_ptr)c.lds + STO + nq.w * 64 + ((c.pxr >> 2) & 60)) = xsum(lsum[2]);
        if (FIRST && n == 0) publish(f.cnt + f.my, f.pm);
        if (T5 && n == 0) cnt_fetch(f.cnt, f.co, f.cur, f.pm * 15u);
        if (!LAST && (n & 3) == 0) DMA_V(c, SN, vn, n >> 2);
        if (LAST && !R2) { if (n == 0) w_dma<0, 0>(nq); if (n == 4) w_dma<0, 1>(nq); if (n == 8) w_dma<1, 0>(nq); if (n == 12) w_dma<1, 1>(nq); }
        if (LAST && R2) { if (n == 0) k2_dma<0, 0>(f.c2, 0); if (n == 4) k2_dma<0, 1>(f.c2, 0); if (n == 8) k2_dma<0, 2>(f.c2, 0); if (n == 12) k2_dma<0, 3>(f.c2, 0); }
        if (n == 1) PRIO(2); if (n == 5) PRIO(1); if (n == 9) PRIO(0);
        SB();
        if (FIRST && n == 3) {
            const float m0 = xmax(max4(s[0][0])), m1 = xmax(max4(s[1][0])), m2 = xmax(max4(sr));
            negm[0] = f32x4{-m0, -m0, -m0, -m0}; negm[1] = f32x4{-m1, -m1, -m1, -m1}; negm[2] = f32x4{-m2, -m2, -m2, -m2};
            s[0][0] -= m0; s[1][0] -= m1; sx -= m2;
            SB();
        }
        if (n == 12 && !FIRST) pace(t == 1 ? max(c.pc0, c.pc1) : c.pc0);
        if (n == 13) {
            WAIT_BAR((LAST && !R2) ? 7 : ((FIRST || T5) ? 9 : 8));
            PRIO(3); SB();
        }
    }
#pragma unroll
    for (int m = 0; m < 16; ++m) {
        const int pr = m >> 3, dt = m & 7, mm = m + 4;
        const bf16x8 a = cat(vf[m % 5][0], vf[m % 5][1]);
        const u32x4 p0 = __builtin_shufflevector(ph[0][2 * pr], ph[0][2 * pr + 1], 0, 1, 2, 3), p1 = __builtin_shufflevector(ph[1][2 * pr], ph[1][2 * pr + 1], 0, 1, 2, 3);
        if (FIRST && pr == 0) MFMA32Z(o[0][dt], a, p0); else MFMA32T(o[0][dt], a, p0);
        if (m == 0) vf[3][0] = vtr(c, SO, 0, 3);
        if (m < 12) vf[mm % 5][0] = vtr(c, SO, 2 * (mm >> 3), mm & 7);
        if (m == 0) EXP2(s[1][2], 0);
        if (m == 1) EXP2(s[0][3], 0);
        if (m == 2) EXP2(s[1][3], 0);
        if (m == 3) SUM4(s[0][2], lsum[0]);
        if (m == 4) SUM4(s[1][2], lsum[1]);
        if (m == 5) PK4(s[0][3], ph[0][3]);
        if (m == 6) PK4(s[1][3], ph[1][3]);
        if (m == 7 || m == 8) vx[m - 7][0] = vxtr(c, SO, 0, m - 7);
        if (m == 10 || m == 11) vx[m - 10][0] = vxtr(c, SO, 2, m - 10);
        if (!LAST && m >= 14) kf[2 * (m - 14)] = kfrag(c, SN, 2 * (m - 14));
        if (LAST) {
            if (m == 7) { f.lt[0] = xsum(lsum[0]); f.il[0] = __builtin_amdgcn_rcpf(f.lt[0]); PIN(f.il[0]); }
            if (m == 9 && !R2) q_addr(nq, f.hb);
            if (m >= 11 && (m & 1)) { f.pd->pk[(m - 11) >> 1] = opack(o[0][m - 11], o[0][m - 10], f.il[0]); PIN(f.pd->pk[(m - 11) >> 1]); }
        }
        SB();
        if (FIRST && pr == 0) MFMA32Z(o[1][dt], a, p1); else MFMA32T(o[1][dt], a, p1);
        if (m == 0) vf[3][1] = vtr(c, SO, 1, 3);
        if (m < 12) vf[mm % 5][1] = vtr(c, SO, 2 * (mm >> 3) + 1, mm & 7);
        if (m == 0) EXP2(s[1][2], 1);
        if (m == 1) EXP2(s[0][3], 1);
        if (m == 2) EXP2(s[1][3], 1);
        if (m == 3) PK4(s[0][2], ph[0][2]);
        if (m == 4) PK4(s[1][2], ph[1][2]);
        if (m == 5) SUM4(s[0][3], lsum[0]);
        if (m == 6) SUM4(s[1][3], lsum[1]);
        if (m == 5 || m == 6) pxf[m - 5] = ld128(c.lds + c.pxr + (m - 5) * 1024);
        if (m == 7 || m == 8) vx[m - 7][1] = vxtr(c, SO, 1, m - 7);
        if (m == 10 || m == 11) vx[m - 10][1] = vxtr(c, SO, 3, m - 10);
        if (!LAST && m >= 14) kf[2 * (m - 14) + 1] = kfrag(c, SN, 2 * (m - 14) + 1);
        if (LAST) {
            if (m == 8) { f.lt[1] = xsum(lsum[1]); f.il[1] = __builtin_amdgcn_rcpf(f.lt[1]); PIN(f.il[1]); }
            if (m >= 11 && (m & 1)) { f.pd->pk[4 + ((m - 11) >> 1)] = opack(o[1][m - 11], o[1][m - 10], f.il[1]); PIN(f.pd->pk[4 + ((m - 11) >> 1)]); }
        }
        if (m == 9 || m == 10) { SB(); if (FIRST) MFMA32Z(ox[m - 9], cat(vx[m - 9][0], vx[m - 9][1]), pxf[0]); else MFMA32T(ox[m - 9], cat(vx[m - 9][0], vx[m - 9][1]), pxf[0]); }
        if (m == 13 || m == 14) { SB(); MFMA32T(ox[m - 13], cat(vx[m - 13][0], vx[m - 13][1]), pxf[1]); }
        if (!LAST && !PRE && (m & 3) == 0) DMA_K(c, SO, kn, m >> 2);
        if (PRE && !R2) { if (m == 0) x_dma<0, 0>(nq); if (m == 4) x_dma<0, 1>(nq); if (m == 8) x_dma<0, 2>(nq); }
        if (PRE && R2) { if (m == 0) k2_dma<2, 0>(f.c2, 2); if (m == 4) k2_dma<2, 1>(f.c2, 2); if (m == 8) k2_dma<2, 2>(f.c2, 2); if (m == 12) k2_dma<2, 3>(f.c2, 2); }
        if (LAST && !R2) {
            if (m == 0) w_dma<2, 0>(nq); if (m == 4) w_dma<2, 1>(nq); if (m == 8) w_dma<3, 0>(nq); if (m == 12) w_dma<3, 1>(nq);
            if (m == 14) x_dma<1, 0>(nq); if (m == 15) { x_dma<1, 1>(nq); x_dma<1, 2>(nq); }
        }
        if (LAST && R2) {
            const int u = m < 3 ? m : m - 4;
            if (m < 3 || (m >= 7 && m < 13)) {
                if (u % 3 == 0) k2_lda_m<0>(f.c2, u / 3, f.a2[u / 3][0], f.am);
                if (u % 3 == 1) k2_lda_m<1>(f.c2, u / 3, f.a2[u / 3][1], f.am);
                if (u % 3 == 2) k2_lda_m<2>(f.c2, u / 3, f.a2[u / 3][2], f.am);
            }
            if (m == 3) k2_dma<1, 0>(f.c2, 1); if (m == 4) k2_dma<1, 1>(f.c2, 1); if (m == 5) k2_dma<1, 2>(f.c2, 1); if (m == 6) k2_dma<1, 3>(f.c2, 1);
            if (m == 14) { k2_dma<3, 0>(f.c2, 3); k2_dma<3, 1>(f.c2, 3); } if (m == 15) { k2_dma<3, 2>(f.c2, 3); k2_dma<3, 3>(f.c2, 3); }
        }
        if (m == 1) PRIO(2); if (m == 5) PRIO(1); if (m == 9) PRIO(0);
        SB();
        if (m == 12 && !LAST && !PRE) pace((FIRST || t == 1) ? max(c.pc0, c.pc1) : c.pc0);
        if (m == 13) { WAIT_BAR(LAST ? (R2 ? 63 : 6) : ((PRE && !R2) ? 7 : ((FIRST || T5) ? 9 : 8))); PRIO(3); SB(); }
    }
}

DI bf16x8 qfrag(const f32x4 t0, const f32x4 t1) {
    unsigned a0 = cvtpk(t0[0] * QSCALE, t0[1] * QSCALE), a1 = cvtpk(t0[2] * QSCALE, t0[3] * QSCALE);
    unsigned b0 = cvtpk(t1[0] * QSCALE, t1[1] * QSCALE), b1 = cvtpk(t1[2] * QSCALE, t1[3] * QSCALE);
    auto r0 = __builtin_amdgcn_permlane32_swap(a0, b0, false, false);
    auto r1 = __builtin_amdgcn_permlane32_swap(a1, b1, false, false);
    auto s0 = __builtin_amdgcn_permlane16_swap(r0[0], r0[1], false, false);
    auto s1 = __builtin_amdgcn_permlane16_swap(r1[0], r1[1], false, false);
    u32x4 u = {s0[0], s1[0], s0[1], s1[1]};
    return __builtin_bit_cast(bf16x8, u);
}

DI void q_unperm(bf16x8 (&f)[4], const int x) {
    u32x4 a = __builtin_bit_cast(u32x4, f[0]), b = __builtin_bit_cast(u32x4, f[1]), c = __builtin_bit_cast(u32x4, f[2]), d = __builtin_bit_cast(u32x4, f[3]);
#define SW4(p, q) "v_swap_b32 %" #p ", %" #q "\n\t"
    asm volatile("s_bitcmp1_b32 %16, 0\n\ts_cbranch_scc0 1f\n\t" SW4(0, 4) SW4(1, 5) SW4(2, 6) SW4(3, 7) SW4(8, 12) SW4(9, 13) SW4(10, 14) SW4(11, 15)
                 "1:\n\ts_bitcmp1_b32 %16, 1\n\ts_cbranch_scc0 2f\n\t" SW4(0, 8) SW4(1, 9) SW4(2, 10) SW4(3, 11) SW4(4, 12) SW4(5, 13) SW4(6, 14) SW4(7, 15) "2:\n\ts_nop 1"
                 : "+v"(a[0]), "+v"(a[1]), "+v"(a[2]), "+v"(a[3]), "+v"(b[0]), "+v"(b[1]), "+v"(b[2]), "+v"(b[3]), "+v"(c[0]), "+v"(c[1]), "+v"(c[2]), "+v"(c[3]),
                   "+v"(d[0]), "+v"(d[1]), "+v"(d[2]), "+v"(d[3]) : "s"(x) : "scc");
    f[0] = __builtin_bit_cast(bf16x8, a); f[1] = __builtin_bit_cast(bf16x8, b); f[2] = __builtin_bit_cast(bf16x8, c); f[3] = __builtin_bit_cast(bf16x8, d);
}
DI void slow_frag(const bf16x8 (&tq)[4], const char* kp, const char* vp, char* dst) {
    const int l = opaque_lane(), r = l & 15, g = l >> 4;
    float m = -3.0e38f, ls = 0.f; f32x4 acc[2] = {{0, 0, 0, 0}, {0, 0, 0, 0}};
#pragma unroll 1
    for (int kt = 0; kt < 32; ++kt) {
        f32x4 s = {0, 0, 0, 0};
#pragma unroll
        for (int ks = 0; ks < 4; ++ks) s = MFMA32(*(const bf16x8*)(kp + (kt * 16 + r) * 256 + ks * 64 + g * 16), tq[ks], s);
        const float mn = fmaxf(m, xmax(max4(s))), f = __builtin_amdgcn_exp2f(m - mn);
        m = mn; s -= mn; ls *= f; u32x2 pk;
        EXP4(s); SUMPK(s, ls, pk);
#pragma unroll
        for (int d = 0; d < 2; ++d) {
            const short* vq = (const short*)(vp + (kt * 16 + 4 * g) * 256 + (d * 16 + r) * 2);
            const bf16x4 a = {vq[0], vq[128], vq[256], vq[384]};
            acc[d] = MFMA16(a, __builtin_bit_cast(bf16x4, pk), acc[d] * f);
        }
    }
    ostore(dst + l * 16, opack(acc[0], acc[1], __builtin_amdgcn_rcpf(xsum(ls))));
}

DI void slow_q(bf16x8 (&tq)[4], const char* xr, const char* wc) {
    const int l = opaque_lane(), r = l & 15, g = l >> 4;
#pragma unroll 1
    for (int ks = 0; ks < 4; ++ks) {
        f32x4 a0 = {0, 0, 0, 0}, a1 = {0, 0, 0, 0};
#pragma unroll 1
        for (int kk = 0; kk < 20; ++kk) {
            const bf16x8 xb = *(const bf16x8*)(xr + r * (DM * 2) + kk * 64 + g * 16);
            const short* wp = (const short*)(wc + (size_t)(32 * kk + 8 * g) * (HD * 2) + (32 * ks + r) * 2);
            bf16x8 w0, w1;
#pragma unroll
            for (int j = 0; j < 8; ++j) { w0[j] = wp[j * HD]; w1[j] = wp[j * HD + 16]; }
            a0 = MFMA32(w0, xb, a0); a1 = MFMA32(w1, xb, a1);
        }
        tq[0] = tq[1]; tq[1] = tq[2]; tq[2] = tq[3]; tq[3] = qfrag(a0, a1);
    }
}
constexpr int q_w(int s) { return s >= 0 && s < 16 ? 2 : 0; }
constexpr int q_x(int s) { return s >= 0 && s < 17 && (s & 1) ? 3 : 0; }
constexpr bool q_k(int s) { return s <= KST && s > KST - 8 && !((KST - s) & 1); }
constexpr int q_post(int s) { return (s >= 0 && s <= 16 && !(s & 1) ? 1 : 0) + (s == TS0 ? 2 : 0) + (q_k(s) ? 1 : 0) + (s == 17 ? 4 : 0); }
constexpr int q_all(int s) { return q_w(s) + q_x(s) + q_post(s); }
constexpr int q_wait(int s) { return (s & 1) ? q_post(s - 2) + q_all(s - 1) : q_x(s - 3) + q_post(s - 3) + q_all(s - 2) + q_all(s - 1); }
template <int N, int L> DI void wait_bar() { WAIT_BARL(N, L); }
template <int S>
DI void q_step(QCtx& q, const AttnCtx& ac, const Pend& pd, f32x4 (&qa)[2][8], f32x4 (&qx)[2], bf16x4 (&af)[8][2], const bf16x8 (&xc)[3], bf16x8 (&xn)[3],
               bf16x8 (&qf)[3][4], bf16x8 (&kf)[4], const unsigned qxa, const Seam& sm) {
    constexpr int SL = S % 5, SN = (S + 1) % 5, SP = (S + 4) % 5, BJ = S == 19 ? 3 : 1;
#pragma unroll
    for (int j = 0; j < 9; ++j) {
        const bf16x8 a0 = cat(af[j ? j - 1 : 0][0], af[j ? j - 1 : 0][1]);
        if (j > 0) { if (S == 0) MFMA32Z(qa[0][j - 1], a0, xc[0]); else MFMA32T(qa[0][j - 1], a0, xc[0]); }
        else { if (S == 0) MFMA32Z(qx[0], a0, xc[2]); else MFMA32T(qx[0], a0, xc[2]); }
        if (j == 0) af[7][0] = wtr(q, SL, 7, 0);
        if (S < 19) {
            if (j >= 2) { af[j - 2][0] = wtr(q, SN, j - 2, 0); af[j - 2][1] = wtr(q, SN, j - 2, 1); }
        } else {
            if (j == 2) *LDSP(bf16x8, (lds_ptr)q.lds + qxa + q.w * 1024) = qfrag(qx[0], qx[1]);
            if (j == 4 || j == 6 || j == 8) qf[0][(j - 4) >> 1] = qfrag(qa[0][j - 4], qa[0][j - 3]);
            if (j >= 4 && j < 8) qf[2][j - 4] = ld128(q.lds + qxa + (q.w >> 2) * 4096 + (j - 4) * 1024);
        }
        SB();
        if (j > 0) { if (S == 0) MFMA32Z(qa[1][j - 1], a0, xc[1]); else MFMA32T(qa[1][j - 1], a0, xc[1]); }
        else { const bf16x8 a1 = cat(af[1][0], af[1][1]); if (S == 0) MFMA32Z(qx[1], a1, xc[2]); else MFMA32T(qx[1], a1, xc[2]); }
        if (j == 0) af[7][1] = wtr(q, SL, 7, 1);
        if (S < 19) {
            if (j == 2) xn[2] = xfrag(q, S + 1, 2);
            if (j == 3 || j == 4) xn[j - 3] = xfrag(q, S + 1, j - 3);
        } else {
            if (j == 4 || j == 6 || j == 8) qf[1][(j - 4) >> 1] = qfrag(qa[1][j - 4], qa[1][j - 3]);
            if (j >= 5) kf[j - 5] = kfrag(ac, 0, j - 5);
        }
        if (S < 16) { if (j == 2) w_dma<SP, 0>(q); if (j == 3) w_dma<SP, 1>(q); }
        if (S < 17 && (S & 1)) {
            if (j == 4) x_dma<((S >> 1) & 1), 0>(q);
            if (j == 5) x_dma<((S >> 1) & 1), 1>(q);
            if (j == 6) x_dma<((S >> 1) & 1), 2>(q);
            if (S == TS0 && j >= 7) touch<false>(sm.tb + (j - 7) * 1280, sm.to, sm.ldsd);
        }
        if (q_k(S) && j == ((S & 1) ? 7 : 5)) DMA_K(ac, KE, ac.kp, 3 - ((KST - S) >> 1));
        if (S < 17 && !(S & 1) && j == 6) pstore_m<(S >> 1)>(pd);
        if (S == 17 && j >= 2 && !(j & 1)) DMA_V(ac, 0, ac.vp, (j - 2) >> 1);
        if (S == 19 && j >= 5) DMA_K(ac, KO, ac.kp + 16384, j - 5);
        if (j == 3) PRIO(2); if (j == 5) PRIO(1); if (j == 7) PRIO(0);
        SB();
        if (j == BJ) {
            if (S == 0) wait_bar<7 + 2, 15>();
            else if (S == 1) wait_bar<q_wait(1) + 2, 10>();
            else if (S == 2) wait_bar<3 + 2 + q_all(0) + q_all(1), 15>();
            else if (S == 19) wait_bar<4, 0>();
            else wait_bar<q_wait(S), (S & 1) ? 10 : 15>();
            PRIO(3);
            SB();
        }
    }
}

template <int SET> DI bf16x8 k2_bfrag(const K2Ctx& c, const int n) {
    const unsigned b = c.ba[n >> 1]; const int im = k2_reg(SET) + (n & 1) * 32;
    return cat(ldtr(c.lds + b + im), ldtr(c.lds + b + im + 1040));
}
template <int SET, int MODE, int W, bool Z = false>
DI void k2_step(const K2Ctx& c, const int st, f32x4 (&acc)[3][8], u32x4 (&af)[4][3], bf16x8 (&bw)[8]) {
    constexpr int SN = (SET + 1) & 3, SA = (SET + 3) & 3;
#pragma unroll
    for (int n = 0; n < 8; ++n) {
        if (Z) MFMA32Z(acc[0][n], bw[n], af[SET][0]); else MFMA32T(acc[0][n], bw[n], af[SET][0]);
        if (n >= 2) { if (n == 2) bw[0] = k2_bfrag<SN>(c, 0); else bw[n - 1] = k2_bfrag<SN>(c, n - 1); }
        if (n == 0) bw[7] = k2_bfrag<SET>(c, 7);
        SB();
        if (Z) MFMA32Z(acc[1][n], bw[n], af[SET][1]); else MFMA32T(acc[1][n], bw[n], af[SET][1]);
        if (n == 2) bw[1] = k2_bfrag<SN>(c, 1);
        if (MODE <= 1) {
            if (n == 2) k2_lda<0>(c, st + 3, af[SA][0]);
            if (n == 4) k2_lda<1>(c, st + 3, af[SA][1]);
            if (n == 6) k2_lda<2>(c, st + 3, af[SA][2]);
        }
        if (MODE == 0) {
            if (n == 3) k2_dma<SET, 0>(c, st + 4);
            if (n == 5) k2_dma<SET, 1>(c, st + 4);
            if (n == 7) k2_dma<SET, 2>(c, st + 4);
        }
        SB();
        if (Z) MFMA32Z(acc[2][n], bw[n], af[SET][2]); else MFMA32T(acc[2][n], bw[n], af[SET][2]);
        if (MODE == 0 && n == 7) k2_dma<SET, 3>(c, st + 4);
        SB();
        if (n == 1) {
            asm volatile("s_waitcnt vmcnt(%3) lgkmcnt(0)\n\ts_barrier" : "+v"(af[SN][0]), "+v"(af[SN][1]), "+v"(af[SN][2]) : "i"(W) : "memory");
            SB();
        }
    }
}
#define NAPW 8
#define PATIENCE 2000000
DI unsigned k2_peek(const lds_cptr lds, const int rd) {
    const int l = opaque_lane();
    const unsigned long long no = __ballot(((l >> 1) & 1) == rd && *LDSP(const unsigned, lds + CUR + (l & 3) * 4) - *LDSP(const unsigned, lds + SNAP + (l & 3) * 4) < 8u);
    unsigned m; asm("s_cmp_eq_u64 %1, 0\n\ts_cselect_b32 %0, -1, 0" : "=s"(m) : "s"(no) : "scc");
    return m;
}
DI bool k2_wait(const lds_cptr lds, const char* cnt, const unsigned co, const int w) {
    if (w == 0) {
        const unsigned snap = *LDSP(const unsigned, lds + SNAP + (opaque_lane() & 3) * 4);
        const unsigned long long t0 = __builtin_amdgcn_s_memrealtime();
        unsigned ok;
        while (true) {
            unsigned v; asm volatile("global_load_dword %0, %1, %2 sc1\n\ts_waitcnt vmcnt(0)" : "=v"(v) : "v"(co), "s"(cnt) : "memory");
            ok = !__any(v - snap < 8u);
            if (ok || __builtin_amdgcn_s_memrealtime() - t0 >= PATIENCE) break;
            __builtin_amdgcn_s_sleep(NAPW);
        }
        *LDSP(unsigned, (lds_ptr)lds + CUR + 128) = ok;
    }
    asm volatile("s_waitcnt lgkmcnt(0)\n\ts_barrier" ::: "memory");
    return rfl(*LDSP(const unsigned, lds + CUR + 128)) != 0;
}
DI void k2_setup(K2Ctx& c, const K1Args& args, const lds_cptr lds, const unsigned ldsb, const char* ows, const int w, const int br, const int mt, const int nt) {
    const int l = opaque_lane(), kq = w & 3, mh = w >> 2;
    c.lds = lds; c.ldsw = ldsb + (mh ^ 1) * HB + kq * 2080; c.kd = (mh ^ 1) * 2; c.kq = kq;
    c.wp = args.wo[br] + (size_t)((mh ^ 1) * 2048) * (DM * 2) + nt * 256;
    c.ap = ows + (size_t)((br * 144 + mt * 6 + mh * 3) * NH + (kq >> 1) * 16 + (kq & 1) * 4) * 4096;
    c.wv = (4 * kq + ((l & 15) >> 2)) * (DM * 2) + (l >> 4) * 64 + (l & 3) * 16; c.av = l * 16;
}
template <bool RETRY>
DI bool k2_run(const K1Args& args, const lds_cptr lds, const unsigned ldsb, char* ows, const int w, const int br, const int mt, const int nt, const Pend& pd,
               char* cnt, const unsigned co, char* mycnt, u32x4 (&af)[4][3], const K2Ctx& c0) {
    const int l = opaque_lane(), r = l & 15, g = l >> 4, q4 = r >> 2, p = l & 3, kq = w & 3, mh = w >> 2;
    K2Ctx c;
    if (!RETRY) c = c0; else k2_setup(c, args, lds, ldsb, ows, w, br, mt, nt);
#pragma unroll
    for (int j = 0; j < 4; ++j) c.ba[j] = (kq >> 1) * HB + (kq & 1) * 8320 + g * 2080 + ((kq + j) & 3) * 256 + q4 * 64 + p * 8;
    f32x4 acc[3][8]; bf16x8 bw[8];
    if (RETRY) {
        k2_dma<0, 0>(c, 0); k2_dma<0, 1>(c, 0); k2_dma<0, 2>(c, 0); k2_dma<0, 3>(c, 0); k2_dma<1, 0>(c, 1); k2_dma<1, 1>(c, 1); k2_dma<1, 2>(c, 1); k2_dma<1, 3>(c, 1);
        k2_dma<2, 0>(c, 2); k2_dma<2, 1>(c, 2); k2_dma<2, 2>(c, 2); k2_dma<2, 3>(c, 2);
#pragma unroll
        for (int i = 0; i < 3; ++i) { k2_lda<0>(c, i, af[i][0]); k2_lda<1>(c, i, af[i][1]); k2_lda<2>(c, i, af[i][2]); }
    }
    if (RETRY) { k2_dma<3, 0>(c, 3); k2_dma<3, 1>(c, 3); k2_dma<3, 2>(c, 3); k2_dma<3, 3>(c, 3); }
    else { pstore<0>(pd); pstore<1>(pd); pstore<2>(pd); pstore<3>(pd); pstore<4>(pd); pstore<5>(pd); pstore<6>(pd); pstore<7>(pd); pstore<8>(pd); }
    asm volatile("s_waitcnt vmcnt(%3)\n\ts_barrier" : "+v"(af[0][0]), "+v"(af[0][1]), "+v"(af[0][2]) : "i"(RETRY ? 10 : 23) : "memory");
#pragma unroll
    for (int n = 0; n < 7; ++n) bw[n] = k2_bfrag<0>(c, n);
    SB();
    const unsigned w0 = wave0(w);
    k2_step<0, 0, RETRY ? 7 : 16, true>(c, 0, acc, af, bw); k2_step<1, 0, RETRY ? 11 : 20>(c, 1, acc, af, bw); k2_step<2, 0, 7>(c, 2, acc, af, bw);
    if (!RETRY) publish(mycnt, w0 & 1u);
    k2_step<3, 0, 7>(c, 3, acc, af, bw);
    for (int st = 4; st < 28; st += 4) {
        if (!RETRY) {
            if (st == 12 && __builtin_expect(!k2_peek(lds, 1), 0) && !k2_wait(lds, cnt, co, w)) { asm volatile("s_waitcnt vmcnt(0)" ::: "memory"); return false; }
            SB();
        }
        k2_step<0, 0, 7>(c, st, acc, af, bw);
        k2_step<1, 0, 7>(c, st + 1, acc, af, bw);
        if (!RETRY && st == 8) cnt_fetch(cnt, co, ldsb + CUR, w0 & 15u);
        k2_step<2, 0, 7>(c, st + 2, acc, af, bw); k2_step<3, 0, 7>(c, st + 3, acc, af, bw);
    }
    k2_step<0, 1, 7>(c, 28, acc, af, bw); k2_step<1, 2, 3>(c, 29, acc, af, bw); k2_step<2, 2, 0>(c, 30, acc, af, bw);
    bw[7] = k2_bfrag<3>(c, 7);
    asm volatile("s_waitcnt lgkmcnt(0)\n\ts_barrier" : "+v"(bw[7]) :: "memory");
    SB();
#pragma unroll
    for (int u = 0; u < 8; ++u) {
        const int n = (u + 2) & 7, d = u >> 1;
        MFMA32T(acc[0][n], bw[n], af[3][0]); MFMA32T(acc[1][n], bw[n], af[3][1]); MFMA32T(acc[2][n], bw[n], af[3][2]);
        if (u >= 2) {
#pragma unroll
            for (int i = 0; i < 3; ++i)
                *LDSP(f32x4, (lds_ptr)lds + (mh * 4 + ((kq + d) & 3)) * 18432 + ((d - 1) * 6144 + (i * 2 + (u & 1)) * 1024) + c.av) = acc[i][u];
        }
        SB();
    }
    MFMA_PAD();
    asm volatile("s_waitcnt lgkmcnt(0)\n\ts_barrier" ::: "memory");
#pragma unroll
    for (int d = 0; d < 3; ++d)
#pragma unroll
        for (int i = 0; i < 3; ++i)
#pragma unroll
            for (int a = 0; a < 2; ++a) acc[i][a] += *LDSP(const f32x4, lds + w * 18432 + (d * 6144 + (i * 2 + a) * 1024) + c.av);
#pragma unroll
    for (int i = 0; i < 3; ++i) {
        unsigned a0 = cvtpk(acc[i][0][0], acc[i][0][1]), a1 = cvtpk(acc[i][0][2], acc[i][0][3]);
        unsigned b0 = cvtpk(acc[i][1][0], acc[i][1][1]), b1 = cvtpk(acc[i][1][2], acc[i][1][3]);
        auto r0 = __builtin_amdgcn_permlane32_swap(a0, b0, false, false);
        auto r1 = __builtin_amdgcn_permlane32_swap(a1, b1, false, false);
        auto s0 = __builtin_amdgcn_permlane16_swap(r0[0], r0[1], false, false);
        auto s1 = __builtin_amdgcn_permlane16_swap(r1[0], r1[1], false, false);
        const u32x4 pk = {s0[0], s1[0], s0[1], s1[1]};
        const int row = (mt * 6 + mh * 3 + i) * 16 + r;
        asm volatile("global_store_dwordx4 %0, %1, off" UPOL "\n\ts_nop 1" :: "v"(args.out + (size_t)row * (2 * DM * 2) + br * (DM * 2) + nt * 256 + kq * 64 + g * 16), "v"(pk) : "memory");
    }
    return true;
}

struct Geo { const K1Args* a; int br, b, mt, nt, w, h0; bool has; };
DI Geo geo(int w) {
    auto ka = __builtin_amdgcn_kernarg_segment_ptr(); int bid = blockIdx.x;
    asm volatile("" : "+s"(ka), "+s"(bid), "+s"(w));
    const int xcd = bid & 7, idx = bid >> 3;
    Geo g; g.a = (const K1Args*)ka; g.br = xcd >> 2; g.b = ((xcd >> 1) & 1) * 8 + (idx & 7); g.has = idx < 30; g.mt = (xcd & 3) * 6 + min(idx, 29) / 5; g.nt = idx % 5; g.w = w; g.h0 = (xcd & 1) * 16 + (idx >> 3) * 2 + (w >> 2);
    return g;
}
DI unsigned cnt_off(const int l, const int br, const int mt) { return (unsigned)(((br * NB + (mt * 6 + 5 * (l & 1)) / 9) * 2 + ((l >> 1) & 1)) * 128); }
DI void finish(const lds_cptr lds, const int w, const int it, char* ob, const f32x4 (&o)[2][8], const f32x4 (&ox)[2], const Fin& fin, Pend& pd) {
    const int l2 = opaque_lane(), r2 = l2 & 15, wq = w & 3;
    float l3 = 0.f;
#pragma unroll
    for (int i = 0; i < 4; ++i) l3 += *LDSP(const float, lds + STO + (w >> 2) * 256 + i * 64 + r2 * 4);
    pd.pk[3] = opack(o[0][6], o[0][7], fin.il[0]); pd.pk[7] = opack(o[1][6], o[1][7], fin.il[1]);
    pd.pk[8] = opack(ox[0], ox[1], __builtin_amdgcn_rcpf(l3));
    const unsigned bad = (__any(!(fin.lt[0] < 4096.f)) ? 1u : 0u) | (__any(!(fin.lt[1] < 4096.f)) ? 2u : 0u) | (__any(!(l3 < 4096.f)) ? 4u : 0u);
    pd.ob[0] = ob + ((bad & 1) ? DISCARD : 0) + (size_t)(2 * wq) * (NH * 4096); pd.ob[1] = ob + ((bad & 2) ? DISCARD : 0) + (size_t)(2 * wq + 1) * (NH * 4096);
    pd.ob[2] = ob + ((bad & 4) ? DISCARD : 0) + (size_t)8 * (NH * 4096) + wq * 1024; pd.lo = l2 * 16; pd.em = ~0ull;
    if (__builtin_expect(bad != 0, 0)) {
        const Geo g2 = geo(w); const int wq2 = g2.w & 3, h = g2.h0 + 8 * it;
        const size_t ho = (size_t)(g2.b * NH + h) * (SK * DH * 2);
#pragma unroll 1
        for (int a = 0; a < 3; ++a) {
            if (!((bad >> a) & 1)) continue;
            const int qt = a < 2 ? 2 * wq2 + a : 8;
            bf16x8 tq[4] = {};
            slow_q(tq, g2.a->x[g2.br] + (size_t)(g2.b * SQ + qt * 16) * (DM * 2), g2.a->wq[g2.br] + h * (DH * 2));
#pragma unroll 1
            for (int s = a < 2 ? 0 : wq2; s < (a < 2 ? 4 : wq2 + 1); ++s) slow_frag(tq, g2.a->k[g2.br] + ho, g2.a->v[g2.br] + ho + s * 64, ob + (size_t)qt * (NH * 4096) + s * 1024);
        }
        asm volatile("s_waitcnt vmcnt(0)" ::: "memory");
    }
}
__global__ __launch_bounds__(512, 2) void k1_qattn(const K1Args args) {
    extern __shared__ __attribute__((aligned(16))) char smem[];
    const lds_cptr lds = (lds_cptr)smem;
    const unsigned ldsb = (unsigned)(uintptr_t)smem;
    const char* xb_; const char* wb_; char* ows;
    asm volatile("s_load_dwordx2 %0, %3, %4 offset:0\n\ts_load_dwordx2 %1, %3, %4 offset:48\n\ts_load_dwordx2 %2, %3, 80" : "=&s"(xb_), "=&s"(wb_), "=&s"(ows)
                 : "s"(__builtin_amdgcn_kernarg_segment_ptr()), "s"((blockIdx.x & 4) * 2) : "memory");
    SB();
    const int w = rfl(threadIdx.x >> 6), hd = w >> 2, wq = w & 3, hb = (hd ^ 1) * HB;
    const int xcd = blockIdx.x & 7, idx = blockIdx.x >> 3;
    const int br = xcd >> 2, b = ((xcd >> 1) & 1) * 8 + (idx & 7), h0 = (xcd & 1) * 16 + (idx >> 3) * 2 + hd;
    u32x4 a2[4][3];
    QCtx q;
    q.lds = lds; q.ldx[0] = ldsb + X0 + w * 2048; q.ldx[1] = ldsb + X1 + (w < 4 ? HB : 0) + (w & 3) * 2048; q.ldxx = ldsb + X0 + 16384 + w * 256; q.ldw = ldsb + hb + wq * 2080; q.w = w;
    {
        const int c0 = ((idx & 7) * 5 / 4 + (idx >> 3) * 5 / 2) % 10;
        q.wk = c0 * (64 * HD * 2); q.xk = c0 * 128;
    }
    const size_t hoff = (size_t)(b * NH + h0) * (SK * DH * 2);
    constexpr size_t HSTEP = (size_t)8 * SK * DH * 2;
    char* ob0;
    AttnCtx ac;
    ac.lds = lds; ac.ldk = ldsb + hb + wq * 4096; ac.ldv = ldsb + hb + VE + wq * 4224;
    Seam sm; sm.ldsd = ldsb + STO + 512;
    Pend pd;
    bf16x4 af[8][2]; bf16x8 xb0[3];
    {
        x_setup(q); w_setup(q);
        asm volatile("" : "+v"(q.xo[0]), "+v"(q.xo[1]), "+v"(q.xo[2]), "+v"(q.wv), "+s"(q.ldx[0]), "+s"(q.ldxx), "+s"(q.ldw), "+s"(q.wk), "+s"(q.xk));
        SB();
        asm volatile("s_waitcnt lgkmcnt(0)" : "+s"(xb_), "+s"(wb_), "+s"(ows) :: "memory");
        q.xp = xb_ + (size_t)b * (SQ * DM * 2); q.wp = wb_ + h0 * (DH * 2);
        const int l = opaque_lane();
        x_dma<0, 0>(q); x_dma<0, 1>(q); x_dma<0, 2>(q); w_dma<0, 0>(q); w_dma<0, 1>(q); w_dma<1, 0>(q); w_dma<1, 1>(q); w_dma<2, 0>(q); w_dma<2, 1>(q);
        w_dma<3, 0>(q); w_dma<3, 1>(q);
        x_dma<1, 0>(q); x_dma<1, 1>(q); x_dma<1, 2>(q);
        const unsigned po = (l & 1) * (unsigned)HSTEP + (l & 2) * 32768;
        touch<true>(args.v[br] + hoff, po, sm.ldsd);
        cnt_fetch(ows + CNT, cnt_off(l, br, geo(w).mt), ldsb + SNAP, wave0(w) & 15u);
        ob0 = ows + ((size_t)((br * NB + b) * 9) * NH + h0) * 4096;
    }
    bf16x8 qf[3][4], kf[4]; f32x4 o[2][8], ox[2], negm[3]; float lsum[3]; Fin fin;
#pragma unroll 1
    for (int it = 0; it < 2; ++it) {
        asm volatile("" : "+s"(it));
        if (it) {
            x_setup(q);
            SB();
            fast_tile<0, false, false, true>(6, ac, qf, o, ox, negm, lsum, kf, q, sm, fin);
            w_setup(q);
            SB();
            fast_tile<1, false, true>(7, ac, qf, o, ox, negm, lsum, kf, q, sm, fin);
            {
                const int l2 = opaque_lane(); const unsigned to = (unsigned)(((idx * 8 + w) * 40 + (l2 < 10 ? l2 : 9)) * 128);
                touch<false>(sm.tb + 2560, to, sm.ldsd); touch<false>(sm.tb + 3840, to, sm.ldsd);
            }
            q_first(q, af, xb0);
            MFMA_PAD(); SB();
            finish(lds, w, 0, ob0, o, ox, fin, pd);
            SB();
        } else {
#pragma unroll
            for (int i = 0; i < 9; ++i) pd.pk[i] = u32x4{0, 0, 0, 0};
            pd.ob[0] = pd.ob[1] = pd.ob[2] = ob0; pd.lo = 0; pd.em = 0;
            q_addr(q, hb);
            SB();
            WAIT_BAR(11);
            q_first(q, af, xb0);
        }
        const int l = opaque_lane(), r = l & 15, g = l >> 4, q4 = r >> 2, p = l & 3, row8 = (l >> 2) & 7, hf = l >> 5;
        const char* kh = args.k[br] + hoff + it * HSTEP;
        const char* vh = args.v[br] + hoff + it * HSTEP;
        ac.kp = kh + wq * 4096; ac.vp = vh + wq * 4096;
#pragma unroll
        for (int j = 0; j < 2; ++j) ac.kvo[j] = row8 * 256 + hf * 64 + ((p ^ fsw((j << 1) | (row8 >> 2))) * 16);
        ac.vvo = q4 * 256 + g * 64 + p * 16;
        ac.pc1 = it ? 0 : PACE;
        ac.pc0 = (xcd & 1) ? (it ? THR2 : THR) : 0;
        sm.kl = hb + (r >> 3) * 2048 + (r & 7) * 64 + ((g ^ fsw((r >> 2) & 3)) * 16);
        ac.ka[0] = sm.kl + wq * 4096;
        sm.to = (unsigned)(((idx * 8 + w) * 40 + (l < 10 ? l : 9)) * 128) & (0u - (unsigned)it);
        sm.tb = args.wo[br] + (size_t)(xcd & 3) * (32 * 320 * 128);
        {
            f32x4 qa[2][8], qx[2]; bf16x8 xb1[3];
            const unsigned qxa = X0 + l * 16;
            SB();
#define QS2(S) q_step<S>(q, ac, pd, qa, qx, af, xb0, xb1, qf, kf, qxa, sm); q_step<S + 1>(q, ac, pd, qa, qx, af, xb1, xb0, qf, kf, qxa, sm)
            QS2(0); QS2(2);
            QS2(4); QS2(6); QS2(8); QS2(10); QS2(12); QS2(14);
            QS2(16); QS2(18);
            MFMA_PAD(); SB();
            qf[0][3] = qfrag(qa[0][6], qa[0][7]); qf[1][3] = qfrag(qa[1][6], qa[1][7]);
            q_unperm(qf[0], wq); q_unperm(qf[1], wq);
        }
        q.wp += 8 * DH * 2;
        const unsigned vl = hb + VE + g * 1056 + q4 * 64 + p * 8;
#pragma unroll
        for (int i = 0; i < 4; ++i) { const unsigned kt = (unsigned)((i + wq) & 3); ac.ka[i] = sm.kl + kt * 4096; ac.va[i] = vl + kt * 4224; }
        ac.vax = vl + wq * 256; ac.pxr = PXO + hd * 2048 + l * 16; ac.pxw = ac.pxr + (wq >> 1) * 1024 + (wq & 1) * 8;
        lsum[0] = lsum[1] = lsum[2] = 0.f;
        fin.pd = &pd; fin.hb = hb; fin.pm = wave0(w) & (unsigned)it; fin.a2 = a2;
        {
            const Geo g2 = geo(w);
            fin.cnt = g2.a->ows + CNT; fin.my = (g2.br * NB + g2.b) * 256; fin.co = cnt_off(opaque_lane(), g2.br, g2.mt);
            k2_setup(fin.c2, *g2.a, lds, ldsb, g2.a->ows, g2.w, g2.br, g2.mt, g2.nt);
        }
        SB();
        fin.cur = ldsb + CUR;
        fast_tile<0, true, false>(0, ac, qf, o, ox, negm, lsum, kf, q, sm, fin);
        for (int t = 1; t < 5; t += 2) {
            fast_tile<1, false, false>(t, ac, qf, o, ox, negm, lsum, kf, q, sm, fin);
            fast_tile<0, false, false>(t + 1, ac, qf, o, ox, negm, lsum, kf, q, sm, fin);
        }
        fast_tile<1, false, false, false, true>(5, ac, qf, o, ox, negm, lsum, kf, q, sm, fin);
    }
    fast_tile<0, false, false, true, false, true>(6, ac, qf, o, ox, negm, lsum, kf, q, sm, fin);
    const unsigned go = k2_peek(lds, 0);
    fin.am = go;
    SB();
    fast_tile<1, false, true, false, false, true>(7, ac, qf, o, ox, negm, lsum, kf, q, sm, fin);
    MFMA_PAD(); SB();
    finish(lds, w, 1, ob0 + 8 * 4096, o, ox, fin, pd);
    SB();
    const Geo g2 = geo(w); const K1Args& A = *g2.a;
    char* const cnt = A.ows + CNT; char* const mycnt = cnt + (g2.br * NB + g2.b) * 256 + 128;
    const unsigned co = cnt_off(opaque_lane(), g2.br, g2.mt);
    bool ok = true;
    if (__builtin_expect(go && g2.has, 1)) ok = k2_run<false>(A, lds, ldsb, A.ows, g2.w, g2.br, g2.mt, g2.nt, pd, cnt, co, mycnt, a2, fin.c2);
    else {
        pstore<0>(pd); pstore<1>(pd); pstore<2>(pd); pstore<3>(pd); pstore<4>(pd); pstore<5>(pd); pstore<6>(pd); pstore<7>(pd); pstore<8>(pd);
        asm volatile("s_waitcnt vmcnt(0)\n\ts_barrier" ::: "memory");
        publish(mycnt, wave0(w) & 1u);
        if (g2.has) ok = k2_wait(lds, cnt, co, w);
    }
    if (__builtin_expect((!ok || !go) && g2.has, 0)) {
#pragma unroll 1
        for (int u = ok ? 6 * NH : g2.w; u < 6 * NH; u += 8) {
            const int rt = g2.mt * 6 + (u >> 5), h = u & 31, bb = rt / 9;
            const size_t ho = (size_t)(bb * NH + h) * (SK * DH * 2);
            bf16x8 tq[4] = {};
            slow_q(tq, A.x[g2.br] + (size_t)rt * (16 * DM * 2), A.wq[g2.br] + h * (DH * 2));
#pragma unroll 1
            for (int s = 0; s < 4; ++s) slow_frag(tq, A.k[g2.br] + ho, A.v[g2.br] + ho + s * 64, A.ows + ((size_t)(g2.br * 144 + rt) * NH + h) * 4096 + s * 1024);
        }
        asm volatile("s_waitcnt vmcnt(0) lgkmcnt(0)\n\ts_barrier" ::: "memory");
        k2_run<true>(A, lds, ldsb, A.ows, g2.w, g2.br, g2.mt, g2.nt, pd, cnt, co, mycnt, a2, fin.c2);
    }
}

extern "C" void kernel_launch(void* const* d_in, const int* in_sizes, int n_in, void* d_out, int out_size, void* d_ws, size_t ws_size,
                              hipStream_t stream) {
    (void)in_sizes; (void)n_in; (void)out_size; (void)ws_size;
    (void)hipFuncSetAttribute((const void*)k1_qattn, hipFuncAttributeMaxDynamicSharedMemorySize, K1_LDS);
    K1Args a1;
    a1.x[0] = (const char*)d_in[0]; a1.k[0] = (const char*)d_in[1]; a1.v[0] = (const char*)d_in[2]; a1.wq[0] = (const char*)d_in[3];
    a1.x[1] = (const char*)d_in[5]; a1.k[1] = (const char*)d_in[6]; a1.v[1] = (const char*)d_in[7]; a1.wq[1] = (const char*)d_in[8];
    a1.wo[0] = (const char*)d_in[4]; a1.wo[1] = (const char*)d_in[9]; a1.ows = (char*)d_ws; a1.out = (char*)d_out;
    hipLaunchKernelGGL(k1_qattn, dim3(256), dim3(512), K1_LDS, stream, a1);
}
```
